```python
import torch

def _softmax(x, axis=-1):
    return torch.softmax(x, dim=axis)
def _gelu(x):
    return torch.nn.functional.gelu(x, approximate="tanh")
def _silu(x):
    return torch.nn.functional.silu(x)
def _sigmoid(x):
    return torch.sigmoid(x)
def _layernorm(x, axis=-1, eps=1e-5):
    m = x.mean(dim=axis, keepdim=True)
    v = x.var(dim=axis, keepdim=True, unbiased=False)
    return (x - m) / torch.sqrt(v + eps)
def _scatter_add(tgt, idx, src, axis=0):
    return tgt.clone().index_add_(axis, idx, src)
def _segment_sum(src, seg, n):
    out = torch.zeros((n,) + src.shape[1:], dtype=src.dtype)
    return out.index_add_(0, seg, src)
def _pad(x, pw):
    flat = tuple(v for pair in reversed(pw) for v in pair)
    return torch.nn.functional.pad(x, flat)

kernel_name = 'concat_heads__gqa_block_cached__compressed_kv_cla__decode'

def setup_inputs():

    _w = (lambda a: (lambda t: t.to(torch.bfloat16) if t.is_floating_point() else t)(torch.as_tensor(a)))
    def _A_setup():
        B, Sq, D = 2, 1, 576
        Hq, Hkv, Sk, Dh = 32, 16, 4096, 128
        assert Hq % Hkv == 0, (Hq, Hkv)
        return {
            "x": _w((lambda *s: torch.randn(*s, dtype=torch.bfloat16) * 1.6)(B, Sq, D)),
            "k_cache": _w((lambda *s: torch.randn(*s, dtype=torch.bfloat16) * 1.6)(B, Hkv, Sk, Dh)),
            "v_cache": _w((lambda *s: torch.randn(*s, dtype=torch.bfloat16) * 0.05)(B, Hkv, Sk, Dh)),
            "wq": _w((lambda *s: torch.randn(*s, dtype=torch.bfloat16) * 1.6)(D, Hq * Dh) * (D ** -0.5)),
            "wo": _w((lambda *s: torch.randn(*s, dtype=torch.bfloat16) * 0.05)(Hq * Dh, D) * ((Hq * Dh) ** -0.5)),
        }
    _A_in = _A_setup()

    def _B_setup():
        B, H, Sq, Sk, D, M = 2, 32, 1, 4096, 128, 576
        Dc = 4 * D
        return {
            "x": _w((lambda *s: torch.randn(*s, dtype=torch.bfloat16) * 3.2)(B, Sq, M)),
            "c_kv": _w((lambda *s: torch.randn(*s, dtype=torch.bfloat16) * 0.1)(B, Sk, Dc)),
            "w_q": _w((lambda *s: torch.randn(*s, dtype=torch.bfloat16) * 6.4)(M, H, D) * (M ** -0.5)),
            "w_uk": _w((lambda *s: torch.randn(*s, dtype=torch.bfloat16) * 6.4)(Dc, H, D) * (Dc ** -0.5)),
            "w_uv": _w((lambda *s: torch.randn(*s, dtype=torch.bfloat16) * 0.1)(Dc, H, D) * (Dc ** -0.5)),
            "w_o": _w((lambda *s: torch.randn(*s, dtype=torch.bfloat16) * 0.1)(H, D, M) * ((H * D) ** -0.5)),
        }
    _B_in = _B_setup()

    return {
        "A_x": _A_in["x"],
        "A_k_cache": _A_in["k_cache"],
        "A_v_cache": _A_in["v_cache"],
        "A_wq": _A_in["wq"],
        "A_wo": _A_in["wo"],
        "B_x": _B_in["x"],
        "B_c_kv": _B_in["c_kv"],
        "B_w_q": _B_in["w_q"],
        "B_w_uk": _B_in["w_uk"],
        "B_w_uv": _B_in["w_uv"],
        "B_w_o": _B_in["w_o"],
    }

def reference(A_x, A_k_cache, A_v_cache, A_wq, A_wo, B_x, B_c_kv, B_w_q, B_w_uk, B_w_uv, B_w_o):
    def _A_ref(x, k_cache, v_cache, wq, wo):
        B, Sq, D = x.shape
        Hq, Dh = 32, 128
        Hkv = k_cache.shape[1]
        rep = Hq // Hkv
        q = (lambda x, *d: x.permute(*d))((x @ wq).reshape(B, Sq, Hq, Dh), 0, 2, 1, 3)
        k = (lambda x, n, axis: torch.repeat_interleave(x, n, dim=axis))(k_cache, rep, axis=1)
        v = (lambda x, n, axis: torch.repeat_interleave(x, n, dim=axis))(v_cache, rep, axis=1)
        scores = torch.einsum("bhqd,bhkd->bhqk", q, k) * (Dh ** -0.5)
        p = _softmax(scores, axis=-1)
        o = torch.einsum("bhqk,bhkd->bhqd", p, v)
        return (lambda x, *d: x.permute(*d))(o, 0, 2, 1, 3).reshape(B, Sq, Hq * Dh) @ wo

    def _B_ref(x, c_kv, w_q, w_uk, w_uv, w_o):
        B, Sq, M = x.shape[0], x.shape[1], x.shape[2]
        Sk = c_kv.shape[1]
        H, D = w_q.shape[1], w_q.shape[2]
        q = torch.einsum("bqm,mhd->bhqd", x, w_q)
        k = torch.einsum("bkc,chd->bhkd", c_kv, w_uk)
        v = torch.einsum("bkc,chd->bhkd", c_kv, w_uv)
        scores = torch.einsum("bhqd,bhkd->bhqk", q, k) * (D ** -0.5)
        off = Sk - Sq
        qi = torch.arange(Sq)[:, None]
        kj = torch.arange(Sk)[None, :]
        cmask = kj <= (qi + off)
        scores = torch.where(cmask[None, None], scores, -1e9)
        p = _softmax(scores, axis=-1)
        ctx = torch.einsum("bhqk,bhkd->bhqd", p, v)
        return torch.einsum("bhqd,hdm->bqm", ctx, w_o)

    a = _A_ref(A_x, A_k_cache, A_v_cache, A_wq, A_wo)
    b = _B_ref(B_x, B_c_kv, B_w_q, B_w_uk, B_w_uv, B_w_o)
    return torch.concatenate([a, b], axis=-1)
```

```cpp
#include <hip/hip_runtime.h>
#include <hip/hip_bf16.h>
#include <stdint.h>

typedef unsigned short u16;
__device__ __forceinline__ float bf2f(u16 v) { return __uint_as_float(((unsigned)v) << 16); }
__device__ __forceinline__ float bfr(float f) {
  unsigned u = __float_as_uint(f);
  u += 0x7FFFu + ((u >> 16) & 1u);
  return __uint_as_float(u & 0xFFFF0000u);
}
__device__ __forceinline__ u16 f2bf(float f) {
  unsigned u = __float_as_uint(f);
  u += 0x7FFFu + ((u >> 16) & 1u);
  return (u16)(u >> 16);
}

__global__ void k_qproj(const u16* xA, const u16* wA, const u16* xB, const u16* wB, float* qA, float* qB) {
  const u16* x = blockIdx.y ? xB : xA; const u16* w = blockIdx.y ? wB : wA; float* q = blockIdx.y ? qB : qA;
  int n = blockIdx.x * 64 + (threadIdx.x & 63);
  int part = threadIdx.x >> 6;
  float a0 = 0, a1 = 0;
  for (int m = part; m < 576; m += 4) { float wv = bf2f(w[(size_t)m * 4096 + n]); a0 += bf2f(x[m]) * wv; a1 += bf2f(x[576 + m]) * wv; }
  __shared__ float s[2][4][64];
  s[0][part][threadIdx.x & 63] = a0; s[1][part][threadIdx.x & 63] = a1;
  __syncthreads();
  if (threadIdx.x < 128) { int b = threadIdx.x >> 6, l = threadIdx.x & 63; float t = s[b][0][l] + s[b][1][l] + s[b][2][l] + s[b][3][l]; q[b * 4096 + blockIdx.x * 64 + l] = bfr(t); }
}
__global__ void k_qabs(const float* qB, const u16* wuk, float* qabs) {
  int h = blockIdx.x, c = blockIdx.y * 64 + threadIdx.x;
  float a0 = 0, a1 = 0;
  for (int d = 0; d < 128; ++d) { float wv = bf2f(wuk[((size_t)c * 32 + h) * 128 + d]); a0 += qB[h * 128 + d] * wv; a1 += qB[4096 + h * 128 + d] * wv; }
  qabs[(0 * 32 + h) * 512 + c] = a0; qabs[(1 * 32 + h) * 512 + c] = a1;
}
__global__ void k_scoreA(const float* qA, const u16* K, float* sA) {
  int bh = blockIdx.y, b = bh >> 5, h = bh & 31, k = blockIdx.x * 256 + threadIdx.x;
  const u16* kr = K + (((size_t)b * 16 + (h >> 1)) * 4096 + k) * 128;
  float a = 0; for (int d = 0; d < 128; ++d) a += qA[b * 4096 + h * 128 + d] * bf2f(kr[d]);
  sA[(size_t)bh * 4096 + k] = bfr(bfr(a) * 0.08838834764831845f);
}
__global__ void k_scoreB(const float* qabs, const u16* C, float* sB) {
  int bh = blockIdx.y, b = bh >> 5, k = blockIdx.x * 256 + threadIdx.x;
  const u16* cr = C + ((size_t)b * 4096 + k) * 512;
  float a = 0; for (int c = 0; c < 512; ++c) a += qabs[bh * 512 + c] * bf2f(cr[c]);
  sB[(size_t)bh * 4096 + k] = bfr(bfr(a) * 0.08838834764831845f);
}
__global__ void k_softmax(float* s) {
  float* r = s + (size_t)blockIdx.x * 4096;
  __shared__ float red[256];
  float m = -INFINITY; for (int k = threadIdx.x; k < 4096; k += 256) m = fmaxf(m, r[k]);
  red[threadIdx.x] = m; __syncthreads();
  for (int o = 128; o > 0; o >>= 1) { if (threadIdx.x < o) red[threadIdx.x] = fmaxf(red[threadIdx.x], red[threadIdx.x + o]); __syncthreads(); }
  m = red[0]; __syncthreads();
  float l = 0; for (int k = threadIdx.x; k < 4096; k += 256) { float e = __expf(r[k] - m); r[k] = e; l += e; }
  red[threadIdx.x] = l; __syncthreads();
  for (int o = 128; o > 0; o >>= 1) { if (threadIdx.x < o) red[threadIdx.x] += red[threadIdx.x + o]; __syncthreads(); }
  l = red[0];
  for (int k = threadIdx.x; k < 4096; k += 256) r[k] = r[k] / l;
}
__global__ void k_pvA(const float* p, const u16* V, float* oA) {
  int bh = blockIdx.x, b = bh >> 5, h = bh & 31, d = threadIdx.x;
  const u16* vb = V + (((size_t)b * 16 + (h >> 1)) * 4096) * 128 + d;
  float a = 0; for (int k = 0; k < 4096; ++k) a += p[(size_t)bh * 4096 + k] * bf2f(vb[(size_t)k * 128]);
  oA[bh * 128 + d] = bfr(a);
}
__global__ void k_pvB(const float* p, const u16* C, float* cc) {
  int bh = blockIdx.x, b = bh >> 5, c = threadIdx.x;
  const u16* cb = C + ((size_t)b * 4096) * 512 + c;
  float a = 0; for (int k = 0; k < 4096; ++k) a += p[(size_t)bh * 4096 + k] * bf2f(cb[(size_t)k * 512]);
  cc[bh * 512 + c] = a;
}
__global__ void k_ctx(const float* cc, const u16* wuv, float* ctx) {
  int bh = blockIdx.x, h = bh & 31, d = threadIdx.x;
  float a = 0; for (int c = 0; c < 512; ++c) a += cc[bh * 512 + c] * bf2f(wuv[((size_t)c * 32 + h) * 128 + d]);
  ctx[bh * 128 + d] = bfr(a);
}
__global__ void k_out(const float* oA, const u16* wA, const float* oB, const u16* wB, u16* out) {
  const float* o = blockIdx.y ? oB : oA; const u16* w = blockIdx.y ? wB : wA;
  int b = blockIdx.z, n = blockIdx.x * 64 + (threadIdx.x & 63), part = threadIdx.x >> 6;
  float a = 0; for (int k = part; k < 4096; k += 16) a += o[b * 4096 + k] * bf2f(w[(size_t)k * 576 + n]);
  __shared__ float s[16][64];
  s[part][threadIdx.x & 63] = a; __syncthreads();
  if (threadIdx.x < 64) { float t = 0; for (int i = 0; i < 16; ++i) t += s[i][threadIdx.x]; out[b * 1152 + blockIdx.y * 576 + n] = f2bf(t); }
}

extern "C" void kernel_launch(void* const* d_in, const int* in_sizes, int n_in, void* d_out, int out_size, void* d_ws, size_t ws_size, hipStream_t stream) {
  const u16* Ax = (const u16*)d_in[0]; const u16* Ak = (const u16*)d_in[1]; const u16* Av = (const u16*)d_in[2];
  const u16* Awq = (const u16*)d_in[3]; const u16* Awo = (const u16*)d_in[4];
  const u16* Bx = (const u16*)d_in[5]; const u16* Bc = (const u16*)d_in[6]; const u16* Bwq = (const u16*)d_in[7];
  const u16* Bwuk = (const u16*)d_in[8]; const u16* Bwuv = (const u16*)d_in[9]; const u16* Bwo = (const u16*)d_in[10];
  float* ws = (float*)d_ws;
  float* qA = ws; float* qB = qA + 8192; float* qabs = qB + 8192; float* sA = qabs + 32768; float* sB = sA + 64 * 4096;
  float* oA = sB + 64 * 4096; float* cc = oA + 8192; float* ctx = cc + 32768;
  k_qproj<<<dim3(64, 2), 256, 0, stream>>>(Ax, Awq, Bx, Bwq, qA, qB);
  k_qabs<<<dim3(32, 8), 64, 0, stream>>>(qB, Bwuk, qabs);
  k_scoreA<<<dim3(16, 64), 256, 0, stream>>>(qA, Ak, sA);
  k_scoreB<<<dim3(16, 64), 256, 0, stream>>>(qabs, Bc, sB);
  k_softmax<<<128, 256, 0, stream>>>(sA);
  k_pvA<<<64, 128, 0, stream>>>(sA, Av, oA);
  k_pvB<<<64, 512, 0, stream>>>(sB, Bc, cc);
  k_ctx<<<64, 128, 0, stream>>>(cc, Bwuv, ctx);
  k_out<<<dim3(9, 2, 2), 1024, 0, stream>>>(oA, Awo, ctx, Bwo, (u16*)d_out);
}
```

```cpp
#include <hip/hip_runtime.h>
#include <hip/hip_bf16.h>
#include <stdint.h>

typedef unsigned short u16;
__device__ __forceinline__ float bf2f(u16 v) { return __uint_as_float(((unsigned)v) << 16); }
__device__ __forceinline__ float bfr(float f) {
  unsigned u = __float_as_uint(f);
  u += 0x7FFFu + ((u >> 16) & 1u);
  return __uint_as_float(u & 0xFFFF0000u);
}
__device__ __forceinline__ u16 f2bf(float f) {
  unsigned u = __float_as_uint(f);
  u += 0x7FFFu + ((u >> 16) & 1u);
  return (u16)(u >> 16);
}

__global__ void k_qproj(const u16* xA, const u16* wA, const u16* xB, const u16* wB, float* qA, float* qB) {
  const u16* x = blockIdx.y ? xB : xA; const u16* w = blockIdx.y ? wB : wA; float* q = blockIdx.y ? qB : qA;
  int n = blockIdx.x * 64 + (threadIdx.x & 63);
  int part = threadIdx.x >> 6;
  float a0 = 0, a1 = 0;
  for (int m = part; m < 576; m += 4) { float wv = bf2f(w[(size_t)m * 4096 + n]); a0 += bf2f(x[m]) * wv; a1 += bf2f(x[576 + m]) * wv; }
  __shared__ float s[2][4][64];
  s[0][part][threadIdx.x & 63] = a0; s[1][part][threadIdx.x & 63] = a1;
  __syncthreads();
  if (threadIdx.x < 128) { int b = threadIdx.x >> 6, l = threadIdx.x & 63; float t = s[b][0][l] + s[b][1][l] + s[b][2][l] + s[b][3][l]; q[b * 4096 + blockIdx.x * 64 + l] = (t); }
}
__global__ void k_qabs(const float* qB, const u16* wuk, float* qabs) {
  int h = blockIdx.x, c = blockIdx.y * 64 + threadIdx.x;
  float a0 = 0, a1 = 0;
  for (int d = 0; d < 128; ++d) { float wv = bf2f(wuk[((size_t)c * 32 + h) * 128 + d]); a0 += qB[h * 128 + d] * wv; a1 += qB[4096 + h * 128 + d] * wv; }
  qabs[(0 * 32 + h) * 512 + c] = a0; qabs[(1 * 32 + h) * 512 + c] = a1;
}
__global__ void k_scoreA(const float* qA, const u16* K, float* sA) {
  int bh = blockIdx.y, b = bh >> 5, h = bh & 31, k = blockIdx.x * 256 + threadIdx.x;
  const u16* kr = K + (((size_t)b * 16 + (h >> 1)) * 4096 + k) * 128;
  float a = 0; for (int d = 0; d < 128; ++d) a += qA[b * 4096 + h * 128 + d] * bf2f(kr[d]);
  sA[(size_t)bh * 4096 + k] = (a * 0.08838834764831845f);
}
__global__ void k_scoreB(const float* qabs, const u16* C, float* sB) {
  int bh = blockIdx.y, b = bh >> 5, k = blockIdx.x * 256 + threadIdx.x;
  const u16* cr = C + ((size_t)b * 4096 + k) * 512;
  float a = 0; for (int c = 0; c < 512; ++c) a += qabs[bh * 512 + c] * bf2f(cr[c]);
  sB[(size_t)bh * 4096 + k] = (a * 0.08838834764831845f);
}
__global__ void k_softmax(float* s) {
  float* r = s + (size_t)blockIdx.x * 4096;
  __shared__ float red[256];
  float m = -INFINITY; for (int k = threadIdx.x; k < 4096; k += 256) m = fmaxf(m, r[k]);
  red[threadIdx.x] = m; __syncthreads();
  for (int o = 128; o > 0; o >>= 1) { if (threadIdx.x < o) red[threadIdx.x] = fmaxf(red[threadIdx.x], red[threadIdx.x + o]); __syncthreads(); }
  m = red[0]; __syncthreads();
  float l = 0; for (int k = threadIdx.x; k < 4096; k += 256) { float e = __expf(r[k] - m); r[k] = e; l += e; }
  red[threadIdx.x] = l; __syncthreads();
  for (int o = 128; o > 0; o >>= 1) { if (threadIdx.x < o) red[threadIdx.x] += red[threadIdx.x + o]; __syncthreads(); }
  l = red[0];
  for (int k = threadIdx.x; k < 4096; k += 256) r[k] = r[k] / l;
}
__global__ void k_pvA(const float* p, const u16* V, float* oA) {
  int bh = blockIdx.x, b = bh >> 5, h = bh & 31, d = threadIdx.x;
  const u16* vb = V + (((size_t)b * 16 + (h >> 1)) * 4096) * 128 + d;
  float a = 0; for (int k = 0; k < 4096; ++k) a += p[(size_t)bh * 4096 + k] * bf2f(vb[(size_t)k * 128]);
  oA[bh * 128 + d] = (a);
}
__global__ void k_pvB(const float* p, const u16* C, float* cc) {
  int bh = blockIdx.x, b = bh >> 5, c = threadIdx.x;
  const u16* cb = C + ((size_t)b * 4096) * 512 + c;
  float a = 0; for (int k = 0; k < 4096; ++k) a += p[(size_t)bh * 4096 + k] * bf2f(cb[(size_t)k * 512]);
  cc[bh * 512 + c] = a;
}
__global__ void k_ctx(const float* cc, const u16* wuv, float* ctx) {
  int bh = blockIdx.x, h = bh & 31, d = threadIdx.x;
  float a = 0; for (int c = 0; c < 512; ++c) a += cc[bh * 512 + c] * bf2f(wuv[((size_t)c * 32 + h) * 128 + d]);
  ctx[bh * 128 + d] = (a);
}
__global__ void k_out(const float* oA, const u16* wA, const float* oB, const u16* wB, u16* out) {
  const float* o = blockIdx.y ? oB : oA; const u16* w = blockIdx.y ? wB : wA;
  int b = blockIdx.z, n = blockIdx.x * 64 + (threadIdx.x & 63), part = threadIdx.x >> 6;
  float a = 0; for (int k = part; k < 4096; k += 16) a += o[b * 4096 + k] * bf2f(w[(size_t)k * 576 + n]);
  __shared__ float s[16][64];
  s[part][threadIdx.x & 63] = a; __syncthreads();
  if (threadIdx.x < 64) { float t = 0; for (int i = 0; i < 16; ++i) t += s[i][threadIdx.x]; out[b * 1152 + blockIdx.y * 576 + n] = f2bf(t); }
}

extern "C" void kernel_launch(void* const* d_in, const int* in_sizes, int n_in, void* d_out, int out_size, void* d_ws, size_t ws_size, hipStream_t stream) {
  const u16* Ax = (const u16*)d_in[0]; const u16* Ak = (const u16*)d_in[1]; const u16* Av = (const u16*)d_in[2];
  const u16* Awq = (const u16*)d_in[3]; const u16* Awo = (const u16*)d_in[4];
  const u16* Bx = (const u16*)d_in[5]; const u16* Bc = (const u16*)d_in[6]; const u16* Bwq = (const u16*)d_in[7];
  const u16* Bwuk = (const u16*)d_in[8]; const u16* Bwuv = (const u16*)d_in[9]; const u16* Bwo = (const u16*)d_in[10];
  float* ws = (float*)d_ws;
  float* qA = ws; float* qB = qA + 8192; float* qabs = qB + 8192; float* sA = qabs + 32768; float* sB = sA + 64 * 4096;
  float* oA = sB + 64 * 4096; float* cc = oA + 8192; float* ctx = cc + 32768;
  k_qproj<<<dim3(64, 2), 256, 0, stream>>>(Ax, Awq, Bx, Bwq, qA, qB);
  k_qabs<<<dim3(32, 8), 64, 0, stream>>>(qB, Bwuk, qabs);
  k_scoreA<<<dim3(16, 64), 256, 0, stream>>>(qA, Ak, sA);
  k_scoreB<<<dim3(16, 64), 256, 0, stream>>>(qabs, Bc, sB);
  k_softmax<<<128, 256, 0, stream>>>(sA);
  k_pvA<<<64, 128, 0, stream>>>(sA, Av, oA);
  k_pvB<<<64, 512, 0, stream>>>(sB, Bc, cc);
  k_ctx<<<64, 128, 0, stream>>>(cc, Bwuv, ctx);
  k_out<<<dim3(9, 2, 2), 1024, 0, stream>>>(oA, Awo, ctx, Bwo, (u16*)d_out);
}
```

```cpp
#include <hip/hip_runtime.h>
#include <stdint.h>

typedef unsigned short u16;
typedef unsigned int u32;
typedef unsigned long long u64;
typedef u32 u32x4 __attribute__((ext_vector_type(4)));
typedef u32 u32x2 __attribute__((ext_vector_type(2)));
typedef float f32x4 __attribute__((ext_vector_type(4)));
typedef float f32x2 __attribute__((ext_vector_type(2)));
typedef float f32x16 __attribute__((ext_vector_type(16)));
typedef __bf16 bf16x2 __attribute__((ext_vector_type(2)));
typedef __bf16 bf16x8 __attribute__((ext_vector_type(8)));
typedef short s16x4 __attribute__((ext_vector_type(4)));
#define GAS __attribute__((address_space(1)))
#define LAS __attribute__((address_space(3)))

constexpr int NT = 512;
constexpr int NWG = 256;
constexpr int ASPLIT = 6;
constexpr float SCALE = 0.08838834764831845f;
constexpr float L2E = 1.4426950408889634f;

constexpr u32 WS_EPOCH = 0;
constexpr u32 WS_QFLAG = 256;
constexpr u32 WS_B2FLAG = WS_QFLAG + 2048;
constexpr u32 WS_B3FLAG = WS_B2FLAG + 512;
constexpr u32 WS_B4FLAG = WS_B3FLAG + 512;
constexpr u32 WS_B5FLAG = WS_B4FLAG + 512;
constexpr u32 WS_ATFLAG = WS_B5FLAG + 512;
constexpr u32 WS_AFLAG = WS_ATFLAG + 512;
constexpr u32 WS_Q = 8192;
constexpr u32 WS_QH = WS_Q + 32768;
constexpr u32 WS_QL = WS_QH + 65536;
constexpr u32 WS_BML = WS_QL + 65536;
constexpr u32 WS_CTXP = WS_BML + 16384;
constexpr u32 WS_OUTB = WS_CTXP + 65536;
constexpr u32 WS_OUTA = WS_OUTB + 294912;
constexpr u32 WS_AML = WS_OUTA + 294912;
constexpr u32 WS_APART = WS_AML + 4096;
constexpr u32 WS_BPART = WS_APART + 196608;
constexpr u32 WS_END = WS_BPART + 4194304;

constexpr int CSTR = 1040;
constexpr int L_C = 0;
constexpr int L_QH = 66560;
constexpr int L_QL = 99840;
constexpr int L_P = 133120;
constexpr int L_ST = 137728;
constexpr int L_SCR = 139776;
constexpr int LDS_BYTES = 143872;

struct Params {
  const u16* Ax; const u16* Ak; const u16* Av; const u16* Awq; const u16* Awo;
  const u16* Bx; const u16* Bc; const u16* Bwq; const u16* Bwuk; const u16* Bwuv; const u16* Bwo;
  u16* out; unsigned char* ws;
};

__device__ __forceinline__ float bf2f(u16 v) { return __uint_as_float(((u32)v) << 16); }
__device__ __forceinline__ float bflo(u32 v) { return __uint_as_float(v << 16); }
__device__ __forceinline__ float bfhi(u32 v) { return __uint_as_float(v & 0xffff0000u); }
__device__ __forceinline__ float bfr(float f) { return (float)(__bf16)f; }
__device__ __forceinline__ u32 pack_bf16(float lo, float hi) {
  bf16x2 v = {(__bf16)lo, (__bf16)hi};
  return __builtin_bit_cast(u32, v);
}
__device__ __forceinline__ float dot2(u32 a, u32 b, float c) {
  return __builtin_amdgcn_fdot2_f32_bf16(__builtin_bit_cast(bf16x2, a), __builtin_bit_cast(bf16x2, b), c, false);
}
template <int CTRL> __device__ __forceinline__ float dppf(float x) {
  return __builtin_bit_cast(float, __builtin_amdgcn_mov_dpp(__builtin_bit_cast(int, x), CTRL, 0xf, 0xf, true));
}
__device__ __forceinline__ float row16_sum(float x) {
  x += dppf<0x128>(x); x += dppf<0x124>(x); x += dppf<0x122>(x); x += dppf<0x121>(x);
  return x;
}
__device__ __forceinline__ float ex2(float x) { return __builtin_amdgcn_exp2f(x); }

typedef __amdgpu_buffer_rsrc_t rsrc_t;
__device__ __forceinline__ u32x4 ld128(rsrc_t r, u32 off) { return __builtin_amdgcn_raw_buffer_load_b128(r, off, 0, 16); }
__device__ __forceinline__ u32x2 ld64(rsrc_t r, u32 off) { return __builtin_amdgcn_raw_buffer_load_b64(r, off, 0, 16); }
__device__ __forceinline__ u32 ld32(rsrc_t r, u32 off) { return __builtin_amdgcn_raw_buffer_load_b32(r, off, 0, 16); }
__device__ __forceinline__ void st128(rsrc_t r, u32 off, u32x4 v) { __builtin_amdgcn_raw_buffer_store_b128(v, r, off, 0, 16); }
__device__ __forceinline__ void st64(rsrc_t r, u32 off, u32x2 v) { __builtin_amdgcn_raw_buffer_store_b64(v, r, off, 0, 16); }
__device__ __forceinline__ void st32(rsrc_t r, u32 off, u32 v) { __builtin_amdgcn_raw_buffer_store_b32(v, r, off, 0, 16); }
__device__ __forceinline__ u32x4 ldnt(const u16* p) { return __builtin_nontemporal_load((const u32x4*)p); }
__device__ __forceinline__ u32x4 ldg(const u16* p) { return *(const u32x4*)p; }

__device__ __forceinline__ void flag_set(unsigned char* ws, u32 off, u64 tag) {
  __hip_atomic_store((GAS u64*)(ws + off), tag, __ATOMIC_RELAXED, __HIP_MEMORY_SCOPE_AGENT);
}
__device__ __forceinline__ u64 flag_get(unsigned char* ws, u32 off) {
  return __hip_atomic_load((GAS u64*)(ws + off), __ATOMIC_RELAXED, __HIP_MEMORY_SCOPE_AGENT);
}
#define DRAIN() asm volatile("s_waitcnt vmcnt(0)" ::: "memory")

__device__ __forceinline__ void wait_flags(unsigned char* ws, u32 off, bool active, u64 tag) {
  for (int spin = 0; spin < 400000; ++spin) {
    u64 v = tag;
    if (active) v = flag_get(ws, off);
    if (__all(v == tag)) break;
    __builtin_amdgcn_s_sleep(1);
  }
  __builtin_amdgcn_fence(__ATOMIC_ACQUIRE, "wavefront");
}

__device__ __forceinline__ void proj_load(const u16* W, int j, int t, u32x4 (&w)[10]) {
  const int cc = t % 72, rg = t / 72;
#pragma unroll
  for (int i = 0; i < 10; ++i) {
    const int row = rg + 7 * i;
    w[i] = u32x4{0u, 0u, 0u, 0u};
    if (t < 504 && row < 64) w[i] = ldnt(W + (size_t)(64 * j + row) * 576 + cc * 8);
  }
}
__device__ __forceinline__ void proj_compute(unsigned char* lds, rsrc_t wsr, unsigned char* ws, u32 out_off, u32 flag_off, u64 tag,
                                             int t, const u32x4 (&w)[10]) {
  const float* ov = (const float*)(lds + L_SCR);
  float* red = (float*)(lds + L_C);
  const int cc = t % 72, rg = t / 72;
  float a0[8], a1[8];
#pragma unroll
  for (int k = 0; k < 8; ++k) { a0[k] = 0.f; a1[k] = 0.f; }
#pragma unroll
  for (int i = 0; i < 10; ++i) {
    int row = rg + 7 * i; row = row < 64 ? row : 63;
    const float x0 = ov[row], x1 = ov[64 + row];
#pragma unroll
    for (int k = 0; k < 4; ++k) {
      const float lo = bflo(w[i][k]), hi = bfhi(w[i][k]);
      a0[2 * k] = fmaf(x0, lo, a0[2 * k]); a0[2 * k + 1] = fmaf(x0, hi, a0[2 * k + 1]);
      a1[2 * k] = fmaf(x1, lo, a1[2 * k]); a1[2 * k + 1] = fmaf(x1, hi, a1[2 * k + 1]);
    }
  }
  if (t < 504) {
    float* r0 = red + (rg * 2 + 0) * 576 + cc * 8; float* r1 = red + (rg * 2 + 1) * 576 + cc * 8;
    *(f32x4*)r0 = f32x4{a0[0], a0[1], a0[2], a0[3]}; *(f32x4*)(r0 + 4) = f32x4{a0[4], a0[5], a0[6], a0[7]};
    *(f32x4*)r1 = f32x4{a1[0], a1[1], a1[2], a1[3]}; *(f32x4*)(r1 + 4) = f32x4{a1[4], a1[5], a1[6], a1[7]};
  }
  __syncthreads();
  if (t < 288) {
    f32x4 s = *(const f32x4*)(red + t * 4);
#pragma unroll
    for (int g = 1; g < 7; ++g) s += *(const f32x4*)(red + g * 1152 + t * 4);
    st128(wsr, out_off + t * 16, __builtin_bit_cast(u32x4, s));
  }
  DRAIN();
  __syncthreads();
  if (t == 0) flag_set(ws, flag_off, tag);
}
__device__ __forceinline__ void final_reduce(unsigned char* lds, rsrc_t wsr, unsigned char* ws, u32 part_off, u32 flag_off, u64 tag,
                                             int j, int t, u16* out, int base) {
  const int wave = t >> 6, lane = t & 63;
  if (wave == 0) wait_flags(ws, flag_off + lane * 8, true, tag);
  __syncthreads();
  const int pidx = t >> 3, b = (t >> 2) & 1, ch = t & 3;
  f32x4 v = __builtin_bit_cast(f32x4, ld128(wsr, part_off + ((pidx * 2 + b) * 576 + 16 * j + 4 * ch) * 4));
#pragma unroll
  for (int k = 0; k < 4; ++k) { float x = v[k]; x += __shfl_xor(x, 8); x += __shfl_xor(x, 16); x += __shfl_xor(x, 32); v[k] = x; }
  float* red = (float*)(lds + L_SCR);
  if (lane < 8) *(f32x4*)(red + wave * 32 + lane * 4) = v;
  __syncthreads();
  if (t < 32) {
    float s = 0.f;
#pragma unroll
    for (int w = 0; w < 8; ++w) s += red[w * 32 + t];
    const int bb = t >> 4, col = t & 15;
    __bf16 o = (__bf16)s;
    out[bb * 1152 + base + 16 * j + col] = __builtin_bit_cast(u16, o);
  }
}

__global__ void __launch_bounds__(NT, 2) fused_kernel(Params p) {
  __shared__ __attribute__((aligned(16))) unsigned char lds[LDS_BYTES];
  const int bid = blockIdx.x, t = threadIdx.x, lane = t & 63;
  const int wave = __builtin_amdgcn_readfirstlane(t >> 6);
  unsigned char* ws = p.ws;
  const rsrc_t wsr = __builtin_amdgcn_make_buffer_rsrc(ws, 0, WS_END, 0x00020000);
  const u64 tag = flag_get(ws, WS_EPOCH) + 1ull;
  const bool isB = ((bid >> 3) & 3) == 3;
  const int bj = ((bid >> 5) << 3) | (bid & 7);
  const int ai = (bid >> 5) * 24 + ((bid >> 3) & 3) * 8 + (bid & 7);

  const int qi = ((bid >> 4) << 4) | ((bid & 7) << 1) | ((bid >> 3) & 1);
  const int qmat = qi >> 7, qcol0 = (qi & 127) * 32;
  const u16* qW = qmat ? p.Bwq : p.Awq;
  const u16* qx = qmat ? p.Bx : p.Ax;
  u32x4 qw[5]; float qx0[5], qx1[5];
  {
    const int cch = t & 3, row0 = t >> 2;
#pragma unroll
    for (int i = 0; i < 5; ++i) {
      const int row = row0 + 128 * i;
      qw[i] = u32x4{0u, 0u, 0u, 0u}; qx0[i] = 0.f; qx1[i] = 0.f;
      if (row < 576) { qw[i] = ldnt(qW + (size_t)row * 4096 + qcol0 + cch * 8); qx0[i] = bf2f(qx[row]); qx1[i] = bf2f(qx[576 + row]); }
    }
  }

  if (!isB) {
    const int pair = ai / ASPLIT, split = ai % ASPLIT;
    const int b = pair >> 4, hkv = pair & 15;
    const int ustart = split * 42 + (split < 4 ? split : 4), ucnt = split < 4 ? 43 : 42;
    const int nw = (ucnt - wave + 7) >> 3;
    const u16* Kb = p.Ak + (size_t)pair * 4096 * 128 + (size_t)(ustart + wave) * 2048 + (lane >> 4) * 128 + (lane & 15) * 8;
    const u16* Vb = p.Av + (size_t)pair * 4096 * 128 + (size_t)(ustart + wave) * 2048 + (lane >> 4) * 128 + (lane & 15) * 8;
    u32x4 K0[4], V0[4], K1[4], V1[4];
#define A_LOAD(KK, VV, it) do { _Pragma("unroll") for (int i_ = 0; i_ < 4; ++i_) { KK[i_] = ldnt(Kb + (size_t)(it) * 16384 + i_ * 512); } \
                                _Pragma("unroll") for (int i_ = 0; i_ < 4; ++i_) { VV[i_] = ldnt(Vb + (size_t)(it) * 16384 + i_ * 512); } } while (0)
    if (wave != 0) { A_LOAD(K0, V0, 0); A_LOAD(K1, V1, 1); }
    {
      float a0[8], a1[8];
#pragma unroll
      for (int k = 0; k < 8; ++k) { a0[k] = 0.f; a1[k] = 0.f; }
#pragma unroll
      for (int i = 0; i < 5; ++i)
#pragma unroll
        for (int k = 0; k < 4; ++k) {
          const float lo = bflo(qw[i][k]), hi = bfhi(qw[i][k]);
          a0[2 * k] = fmaf(qx0[i], lo, a0[2 * k]); a0[2 * k + 1] = fmaf(qx0[i], hi, a0[2 * k + 1]);
          a1[2 * k] = fmaf(qx1[i], lo, a1[2 * k]); a1[2 * k + 1] = fmaf(qx1[i], hi, a1[2 * k + 1]);
        }
#pragma unroll
      for (int k = 0; k < 8; ++k) {
        float x = a0[k]; x += dppf<0x124>(x); x += dppf<0x128>(x); x += __shfl_xor(x, 16); x += __shfl_xor(x, 32); a0[k] = x;
        float y = a1[k]; y += dppf<0x124>(y); y += dppf<0x128>(y); y += __shfl_xor(y, 16); y += __shfl_xor(y, 32); a1[k] = y;
      }
      float* red = (float*)(lds + L_SCR);
      if (lane < 4) {
        float* r0 = red + wave * 64 + lane * 8;
        *(f32x4*)r0 = f32x4{a0[0], a0[1], a0[2], a0[3]}; *(f32x4*)(r0 + 4) = f32x4{a0[4], a0[5], a0[6], a0[7]};
        *(f32x4*)(r0 + 32) = f32x4{a1[0], a1[1], a1[2], a1[3]}; *(f32x4*)(r0 + 36) = f32x4{a1[4], a1[5], a1[6], a1[7]};
      }
      __syncthreads();
      if (wave == 0) {
        if (lane < 32) {
          float s0 = 0.f, s1 = 0.f;
#pragma unroll
          for (int w = 0; w < 8; ++w) { s0 += red[w * 64 + lane * 2]; s1 += red[w * 64 + lane * 2 + 1]; }
          const int bb = lane >> 4, c2 = lane & 15;
          st32(wsr, WS_Q + ((qmat * 2 + bb) * 4096 + qcol0 + c2 * 2) * 2, pack_bf16(s0, s1));
        }
        DRAIN();
        if (lane == 0) flag_set(ws, WS_QFLAG + qi * 8, tag);
        A_LOAD(K0, V0, 0); A_LOAD(K1, V1, 1);
        wait_flags(ws, WS_QFLAG + (8 * hkv + (lane & 7)) * 8, lane < 8, tag);
      }
      __syncthreads();
    }
    u32x4 q0 = ld128(wsr, WS_Q + ((0 * 2 + b) * 4096 + (2 * hkv) * 128 + (lane & 15) * 8) * 2);
    u32x4 q1 = ld128(wsr, WS_Q + ((0 * 2 + b) * 4096 + (2 * hkv + 1) * 128 + (lane & 15) * 8) * 2);
    float m0 = -INFINITY, m1 = -INFINITY, l0 = 0.f, l1 = 0.f;
    float acc0[8], acc1[8];
#pragma unroll
    for (int k = 0; k < 8; ++k) { acc0[k] = 0.f; acc1[k] = 0.f; }
#define A_COMPUTE(KK, VV) do { \
      float s0_[4], s1_[4]; \
      _Pragma("unroll") for (int i_ = 0; i_ < 4; ++i_) { \
        float d0_ = 0.f, d1_ = 0.f; \
        _Pragma("unroll") for (int k_ = 0; k_ < 4; ++k_) { d0_ = dot2(KK[i_][k_], q0[k_], d0_); d1_ = dot2(KK[i_][k_], q1[k_], d1_); } \
        d0_ = row16_sum(d0_); d1_ = row16_sum(d1_); \
        s0_[i_] = bfr(bfr(d0_) * SCALE); s1_[i_] = bfr(bfr(d1_) * SCALE); } \
      const float mx0_ = fmaxf(fmaxf(s0_[0], s0_[1]), fmaxf(s0_[2], s0_[3])), mx1_ = fmaxf(fmaxf(s1_[0], s1_[1]), fmaxf(s1_[2], s1_[3])); \
      if (__any(mx0_ > m0 || mx1_ > m1)) { \
        const float n0_ = fmaxf(m0, mx0_), n1_ = fmaxf(m1, mx1_); \
        const float al0_ = ex2((m0 - n0_) * L2E), al1_ = ex2((m1 - n1_) * L2E); \
        l0 *= al0_; l1 *= al1_; m0 = n0_; m1 = n1_; \
        _Pragma("unroll") for (int k_ = 0; k_ < 8; ++k_) { acc0[k_] *= al0_; acc1[k_] *= al1_; } } \
      float p0_[4], p1_[4]; \
      _Pragma("unroll") for (int i_ = 0; i_ < 4; ++i_) { p0_[i_] = ex2((s0_[i_] - m0) * L2E); p1_[i_] = ex2((s1_[i_] - m1) * L2E); } \
      l0 += (p0_[0] + p0_[1]) + (p0_[2] + p0_[3]); l1 += (p1_[0] + p1_[1]) + (p1_[2] + p1_[3]); \
      const u32 pa0_ = pack_bf16(p0_[0], p0_[1]), pb0_ = pack_bf16(p0_[2], p0_[3]), pa1_ = pack_bf16(p1_[0], p1_[1]), pb1_ = pack_bf16(p1_[2], p1_[3]); \
      _Pragma("unroll") for (int k_ = 0; k_ < 4; ++k_) { \
        const u32 lo01_ = __builtin_amdgcn_perm(VV[1][k_], VV[0][k_], 0x05040100u), hi01_ = __builtin_amdgcn_perm(VV[1][k_], VV[0][k_], 0x07060302u); \
        const u32 lo23_ = __builtin_amdgcn_perm(VV[3][k_], VV[2][k_], 0x05040100u), hi23_ = __builtin_amdgcn_perm(VV[3][k_], VV[2][k_], 0x07060302u); \
        acc0[2 * k_] = dot2(lo23_, pb0_, dot2(lo01_, pa0_, acc0[2 * k_])); acc0[2 * k_ + 1] = dot2(hi23_, pb0_, dot2(hi01_, pa0_, acc0[2 * k_ + 1])); \
        acc1[2 * k_] = dot2(lo23_, pb1_, dot2(lo01_, pa1_, acc1[2 * k_])); acc1[2 * k_ + 1] = dot2(hi23_, pb1_, dot2(hi01_, pa1_, acc1[2 * k_ + 1])); } \
    } while (0)
    for (int it = 0; it < nw; it += 2) {
      A_COMPUTE(K0, V0);
      if (it + 2 < nw) A_LOAD(K0, V0, it + 2);
      if (it + 1 < nw) {
        A_COMPUTE(K1, V1);
        if (it + 3 < nw) A_LOAD(K1, V1, it + 3);
      }
    }
    float* mrg = (float*)(lds + L_C);
    float* mml = (float*)(lds + L_ST);
    {
      const int rec = wave * 4 + (lane >> 4), c = lane & 15;
      float* r0 = mrg + (rec * 2 + 0) * 128 + c * 8; float* r1 = mrg + (rec * 2 + 1) * 128 + c * 8;
      *(f32x4*)r0 = f32x4{acc0[0], acc0[1], acc0[2], acc0[3]}; *(f32x4*)(r0 + 4) = f32x4{acc0[4], acc0[5], acc0[6], acc0[7]};
      *(f32x4*)r1 = f32x4{acc1[0], acc1[1], acc1[2], acc1[3]}; *(f32x4*)(r1 + 4) = f32x4{acc1[4], acc1[5], acc1[6], acc1[7]};
      if (c == 0) *(f32x4*)(mml + rec * 4) = f32x4{m0, l0, m1, l1};
    }
    __syncthreads();
    if (t < 256) {
      const int g = t >> 7, d = t & 127;
      float M = -INFINITY;
#pragma unroll
      for (int r = 0; r < 32; ++r) M = fmaxf(M, mml[r * 4 + g * 2]);
      float L = 0.f, o = 0.f;
#pragma unroll
      for (int r = 0; r < 32; ++r) { const float w = ex2((mml[r * 4 + g * 2] - M) * L2E); L = fmaf(w, mml[r * 4 + g * 2 + 1], L); o = fmaf(w, mrg[(r * 2 + g) * 128 + d], o); }
      st32(wsr, WS_APART + ((ai * 2 + g) * 128 + d) * 4, __float_as_uint(o));
      if (d == 0) st64(wsr, WS_AML + (ai * 2 + g) * 8, u32x2{__float_as_uint(M), __float_as_uint(L)});
    }
    DRAIN();
    __syncthreads();
    if (t == 0) flag_set(ws, WS_AFLAG + ai * 8, tag);
    return;
  }

  const int j = bj;
  const int hh = j >> 1, half = j & 1;
  const int b3b = j >> 5, b3ks = j & 31;
  u32x4 wA[8];
  u32x4 c0[8], c1[8];
#define B_PREFETCH() do { \
    _Pragma("unroll") for (int i_ = 0; i_ < 8; ++i_) wA[i_] = ldnt(p.Bwuk + ((size_t)(256 * half + (t >> 4) + 32 * i_) * 32 + hh) * 128 + (t & 15) * 8); \
    _Pragma("unroll") for (int i_ = 0; i_ < 8; ++i_) c0[i_] = ldnt(p.Bc + ((size_t)b3b * 4096 + 128 * b3ks) * 512 + (size_t)(t + 512 * i_) * 8); \
    _Pragma("unroll") for (int i_ = 0; i_ < 8; ++i_) c1[i_] = ldnt(p.Bc + ((size_t)b3b * 4096 + 128 * b3ks + 64) * 512 + (size_t)(t + 512 * i_) * 8); } while (0)
  if (wave != 0) B_PREFETCH();
  {
    float a0[8], a1[8];
#pragma unroll
    for (int k = 0; k < 8; ++k) { a0[k] = 0.f; a1[k] = 0.f; }
#pragma unroll
    for (int i = 0; i < 5; ++i)
#pragma unroll
      for (int k = 0; k < 4; ++k) {
        const float lo = bflo(qw[i][k]), hi = bfhi(qw[i][k]);
        a0[2 * k] = fmaf(qx0[i], lo, a0[2 * k]); a0[2 * k + 1] = fmaf(qx0[i], hi, a0[2 * k + 1]);
        a1[2 * k] = fmaf(qx1[i], lo, a1[2 * k]); a1[2 * k + 1] = fmaf(qx1[i], hi, a1[2 * k + 1]);
      }
#pragma unroll
    for (int k = 0; k < 8; ++k) {
      float x = a0[k]; x += dppf<0x124>(x); x += dppf<0x128>(x); x += __shfl_xor(x, 16); x += __shfl_xor(x, 32); a0[k] = x;
      float y = a1[k]; y += dppf<0x124>(y); y += dppf<0x128>(y); y += __shfl_xor(y, 16); y += __shfl_xor(y, 32); a1[k] = y;
    }
    float* red = (float*)(lds + L_SCR);
    if (lane < 4) {
      float* r0 = red + wave * 64 + lane * 8;
      *(f32x4*)r0 = f32x4{a0[0], a0[1], a0[2], a0[3]}; *(f32x4*)(r0 + 4) = f32x4{a0[4], a0[5], a0[6], a0[7]};
      *(f32x4*)(r0 + 32) = f32x4{a1[0], a1[1], a1[2], a1[3]}; *(f32x4*)(r0 + 36) = f32x4{a1[4], a1[5], a1[6], a1[7]};
    }
    __syncthreads();
    if (wave == 0) {
      if (lane < 32) {
        float s0 = 0.f, s1 = 0.f;
#pragma unroll
        for (int w = 0; w < 8; ++w) { s0 += red[w * 64 + lane * 2]; s1 += red[w * 64 + lane * 2 + 1]; }
        const int bb = lane >> 4, c2 = lane & 15;
        st32(wsr, WS_Q + ((qmat * 2 + bb) * 4096 + qcol0 + c2 * 2) * 2, pack_bf16(s0, s1));
      }
      DRAIN();
      if (lane == 0) flag_set(ws, WS_QFLAG + qi * 8, tag);
      B_PREFETCH();
      wait_flags(ws, WS_QFLAG + (128 + 4 * hh + (lane & 3)) * 8, lane < 4, tag);
    }
    __syncthreads();
  }
  {
    const u32x4 qa = ld128(wsr, WS_Q + ((1 * 2 + 0) * 4096 + hh * 128 + (t & 15) * 8) * 2);
    const u32x4 qb = ld128(wsr, WS_Q + ((1 * 2 + 1) * 4096 + hh * 128 + (t & 15) * 8) * 2);
    float* qs = (float*)(lds + L_SCR);
#pragma unroll
    for (int i = 0; i < 8; ++i) {
      float d0 = 0.f, d1 = 0.f;
#pragma unroll
      for (int k = 0; k < 4; ++k) { d0 = dot2(wA[i][k], qa[k], d0); d1 = dot2(wA[i][k], qb[k], d1); }
      d0 = row16_sum(d0); d1 = row16_sum(d1);
      if ((t & 15) == 0) { qs[(t >> 4) + 32 * i] = d0; qs[256 + (t >> 4) + 32 * i] = d1; }
    }
#pragma unroll
    for (int i = 0; i < 8; ++i) { const int idx = t + 512 * i; *(u32x4*)(lds + L_C + (idx >> 6) * CSTR + (idx & 63) * 16) = c0[i]; }
    __syncthreads();
    if (wave == 0) {
      const int bb = lane >> 5, c8 = lane & 31;
      u32x4 hi, lo;
#pragma unroll
      for (int k = 0; k < 4; ++k) {
        const float v0 = qs[bb * 256 + c8 * 8 + 2 * k], v1 = qs[bb * 256 + c8 * 8 + 2 * k + 1];
        const float h0 = bfr(v0), h1 = bfr(v1);
        hi[k] = pack_bf16(h0, h1); lo[k] = pack_bf16(v0 - h0, v1 - h1);
      }
      const u32 o = ((bb * 32 + hh) * 512 + 256 * half + c8 * 8) * 2;
      st128(wsr, WS_QH + o, hi); st128(wsr, WS_QL + o, lo);
      DRAIN();
      if (lane == 0) flag_set(ws, WS_B2FLAG + j * 8, tag);
    }
#pragma unroll
    for (int i = 0; i < 8; ++i) wA[i] = ldnt(p.Bwuv + ((size_t)(256 * half + (t >> 4) + 32 * i) * 32 + hh) * 128 + (t & 15) * 8);
    if (wave == 0) wait_flags(ws, WS_B2FLAG + lane * 8, true, tag);
    __syncthreads();
  }
  {
#pragma unroll
    for (int i = 0; i < 4; ++i) {
      const int idx = t + 512 * i;
      const u32x4 vh = ld128(wsr, WS_QH + b3b * 32768 + idx * 16);
      const u32x4 vl = ld128(wsr, WS_QL + b3b * 32768 + idx * 16);
      *(u32x4*)(lds + L_QH + (idx >> 6) * CSTR + (idx & 63) * 16) = vh;
      *(u32x4*)(lds + L_QL + (idx >> 6) * CSTR + (idx & 63) * 16) = vl;
    }
    __syncthreads();
    float* smax = (float*)(lds + L_ST);
    float* salpha = smax + 128;
    float* slsum = smax + 160;
    float* smfin = smax + 288;
    const int kb = wave & 3, hb = wave >> 2;
    const int head = 16 * hb + (lane & 15);
    float mold = -INFINITY, lsum = 0.f;
    f32x16 oacc0, oacc1;
#pragma unroll
    for (int r = 0; r < 16; ++r) { oacc0[r] = 0.f; oacc1[r] = 0.f; }
#pragma unroll
    for (int ch = 0; ch < 2; ++ch) {
      if (ch == 1) {
#pragma unroll
        for (int i = 0; i < 8; ++i) { const int idx = t + 512 * i; *(u32x4*)(lds + L_C + (idx >> 6) * CSTR + (idx & 63) * 16) = c1[i]; }
        __syncthreads();
      }
      f32x4 sacc = {0.f, 0.f, 0.f, 0.f};
      const unsigned char* ap = lds + L_C + (16 * kb + (lane & 15)) * CSTR + (lane >> 4) * 16;
      const unsigned char* bhp = lds + L_QH + head * CSTR + (lane >> 4) * 16;
      const unsigned char* blp = lds + L_QL + head * CSTR + (lane >> 4) * 16;
#pragma unroll
      for (int s = 0; s < 16; ++s) {
        const bf16x8 a = __builtin_bit_cast(bf16x8, *(const u32x4*)(ap + s * 64));
        const bf16x8 bh = __builtin_bit_cast(bf16x8, *(const u32x4*)(bhp + s * 64));
        const bf16x8 bl = __builtin_bit_cast(bf16x8, *(const u32x4*)(blp + s * 64));
        sacc = __builtin_amdgcn_mfma_f32_16x16x32_bf16(a, bh, sacc, 0, 0, 0);
        sacc = __builtin_amdgcn_mfma_f32_16x16x32_bf16(a, bl, sacc, 0, 0, 0);
      }
      float sv[4];
#pragma unroll
      for (int i = 0; i < 4; ++i) sv[i] = bfr(bfr(sacc[i]) * SCALE);
      float mx = fmaxf(fmaxf(sv[0], sv[1]), fmaxf(sv[2], sv[3]));
      mx = fmaxf(mx, __shfl_xor(mx, 16)); mx = fmaxf(mx, __shfl_xor(mx, 32));
      if (lane < 16) smax[kb * 32 + head] = mx;
      __syncthreads();
      const float cm = fmaxf(fmaxf(smax[head], smax[32 + head]), fmaxf(smax[64 + head], smax[96 + head]));
      const float mnew = fmaxf(mold, cm);
      const float alpha = ex2((mold - mnew) * L2E);
      float pv[4];
#pragma unroll
      for (int i = 0; i < 4; ++i) pv[i] = ex2((sv[i] - mnew) * L2E);
      lsum = lsum * alpha + ((pv[0] + pv[1]) + (pv[2] + pv[3]));
      mold = mnew;
      *(u32x2*)(lds + L_P + head * 144 + (16 * kb + 4 * (lane >> 4)) * 2) = u32x2{pack_bf16(pv[0], pv[1]), pack_bf16(pv[2], pv[3])};
      if (kb == 0 && lane < 16) salpha[head] = alpha;
      __syncthreads();
      if (ch == 1) {
#pragma unroll
        for (int q4 = 0; q4 < 4; ++q4) {
          const f32x4 al = *(const f32x4*)(salpha + 8 * q4 + 4 * (lane >> 5));
#pragma unroll
          for (int e = 0; e < 4; ++e) { oacc0[q4 * 4 + e] *= al[e]; oacc1[q4 * 4 + e] *= al[e]; }
        }
      }
      {
        const int g4 = lane >> 4, i16 = lane & 15, qq = i16 >> 2, pp = i16 & 3;
        const unsigned char* pa = lds + L_P + (lane & 31) * 144 + (lane >> 5) * 16;
        const unsigned char* tb = lds + L_C + (8 * (g4 >> 1) + qq) * CSTR + (64 * wave + 16 * (g4 & 1) + 4 * pp) * 2;
#pragma unroll
        for (int ks = 0; ks < 4; ++ks) {
          const bf16x8 a = __builtin_bit_cast(bf16x8, *(const u32x4*)(pa + ks * 32));
          const s16x4 t00 = __builtin_amdgcn_ds_read_tr16_b64_v4i16((LAS s16x4*)(tb + (16 * ks) * CSTR));
          const s16x4 t01 = __builtin_amdgcn_ds_read_tr16_b64_v4i16((LAS s16x4*)(tb + (16 * ks + 4) * CSTR));
          const s16x4 t10 = __builtin_amdgcn_ds_read_tr16_b64_v4i16((LAS s16x4*)(tb + (16 * ks) * CSTR + 64));
          const s16x4 t11 = __builtin_amdgcn_ds_read_tr16_b64_v4i16((LAS s16x4*)(tb + (16 * ks + 4) * CSTR + 64));
          typedef short s16x8 __attribute__((ext_vector_type(8)));
          const s16x8 b0 = __builtin_shufflevector(t00, t01, 0, 1, 2, 3, 4, 5, 6, 7);
          const s16x8 b1 = __builtin_shufflevector(t10, t11, 0, 1, 2, 3, 4, 5, 6, 7);
          oacc0 = __builtin_amdgcn_mfma_f32_32x32x16_bf16(a, __builtin_bit_cast(bf16x8, b0), oacc0, 0, 0, 0);
          oacc1 = __builtin_amdgcn_mfma_f32_32x32x16_bf16(a, __builtin_bit_cast(bf16x8, b1), oacc1, 0, 0, 0);
        }
      }
      __syncthreads();
    }
    lsum += __shfl_xor(lsum, 16); lsum += __shfl_xor(lsum, 32);
    if (lane < 16) { slsum[kb * 32 + head] = lsum; if (kb == 0) smfin[head] = mold; }
#pragma unroll
    for (int r = 0; r < 16; ++r) {
      const int hrow = (r & 3) + 8 * (r >> 2) + 4 * (lane >> 5);
      float* o = (float*)(lds + L_C + hrow * 2064) + 64 * wave + (lane & 31);
      o[0] = oacc0[r]; o[32] = oacc1[r];
    }
    __syncthreads();
#pragma unroll
    for (int i = 0; i < 8; ++i) {
      const int idx = t + 512 * i, hrow = idx >> 7, c4 = idx & 127;
      const u32x4 v = *(const u32x4*)(lds + L_C + hrow * 2064 + c4 * 16);
      st128(wsr, WS_BPART + (((b3b * 32 + b3ks) * 32 + hrow) * 512 + c4 * 4) * 4, v);
    }
    if (t < 32) {
      const float L = (slsum[t] + slsum[32 + t]) + (slsum[64 + t] + slsum[96 + t]);
      st64(wsr, WS_BML + ((b3b * 32 + b3ks) * 32 + t) * 8, u32x2{__float_as_uint(smfin[t]), __float_as_uint(L)});
    }
    DRAIN();
    __syncthreads();
    if (t == 0) flag_set(ws, WS_B3FLAG + j * 8, tag);
  }
  u32x4 wP[10];
  proj_load(p.Bwo, j, t, wP);
  {
    if (wave == 0) wait_flags(ws, WS_B3FLAG + lane * 8, true, tag);
    __syncthreads();
    u32x4 g[8];
#pragma unroll
    for (int i = 0; i < 8; ++i) {
      const int row = (t >> 6) + 8 * i;
      g[i] = ld128(wsr, WS_BPART + ((row * 32 + hh) * 512 + 256 * half + (t & 63) * 4) * 4);
    }
    float* wgt = (float*)(lds + L_SCR);
    float* ctxc = wgt + 64;
    if (wave == 0) {
      const u32x2 ml = ld64(wsr, WS_BML + (lane * 32 + hh) * 8);
      const float m = __uint_as_float(ml[0]), l = __uint_as_float(ml[1]);
      float M = m;
#pragma unroll
      for (int o = 1; o < 32; o <<= 1) M = fmaxf(M, __shfl_xor(M, o));
      const float w = ex2((m - M) * L2E);
      float L = w * l;
#pragma unroll
      for (int o = 1; o < 32; o <<= 1) L += __shfl_xor(L, o);
      wgt[lane] = w / L;
    }
    __syncthreads();
    f32x4 s0 = {0.f, 0.f, 0.f, 0.f}, s1 = {0.f, 0.f, 0.f, 0.f};
#pragma unroll
    for (int i = 0; i < 4; ++i) { s0 += wgt[(t >> 6) + 8 * i] * __builtin_bit_cast(f32x4, g[i]); s1 += wgt[(t >> 6) + 8 * (i + 4)] * __builtin_bit_cast(f32x4, g[i + 4]); }
    float* red = (float*)(lds + L_C);
    *(f32x4*)(red + (wave * 2 + 0) * 256 + lane * 4) = s0;
    *(f32x4*)(red + (wave * 2 + 1) * 256 + lane * 4) = s1;
    __syncthreads();
    {
      float s = 0.f;
#pragma unroll
      for (int w = 0; w < 8; ++w) s += red[w * 512 + t];
      ctxc[t] = s;
    }
    __syncthreads();
    float a0[8], a1[8];
#pragma unroll
    for (int k = 0; k < 8; ++k) { a0[k] = 0.f; a1[k] = 0.f; }
#pragma unroll
    for (int i = 0; i < 8; ++i) {
      const float x0 = ctxc[(t >> 4) + 32 * i], x1 = ctxc[256 + (t >> 4) + 32 * i];
#pragma unroll
      for (int k = 0; k < 4; ++k) {
        const float lo = bflo(wA[i][k]), hi = bfhi(wA[i][k]);
        a0[2 * k] = fmaf(x0, lo, a0[2 * k]); a0[2 * k + 1] = fmaf(x0, hi, a0[2 * k + 1]);
        a1[2 * k] = fmaf(x1, lo, a1[2 * k]); a1[2 * k + 1] = fmaf(x1, hi, a1[2 * k + 1]);
      }
    }
#pragma unroll
    for (int k = 0; k < 8; ++k) {
      float x = a0[k]; x += __shfl_xor(x, 16); x += __shfl_xor(x, 32); a0[k] = x;
      float y = a1[k]; y += __shfl_xor(y, 16); y += __shfl_xor(y, 32); a1[k] = y;
    }
    float* red2 = (float*)(lds + L_QH);
    if (lane < 16) {
      float* r0 = red2 + (wave * 2 + 0) * 128 + lane * 8; float* r1 = red2 + (wave * 2 + 1) * 128 + lane * 8;
      *(f32x4*)r0 = f32x4{a0[0], a0[1], a0[2], a0[3]}; *(f32x4*)(r0 + 4) = f32x4{a0[4], a0[5], a0[6], a0[7]};
      *(f32x4*)r1 = f32x4{a1[0], a1[1], a1[2], a1[3]}; *(f32x4*)(r1 + 4) = f32x4{a1[4], a1[5], a1[6], a1[7]};
    }
    __syncthreads();
    if (wave == 0) {
      f32x4 s = *(const f32x4*)(red2 + lane * 4);
#pragma unroll
      for (int w = 1; w < 8; ++w) s += *(const f32x4*)(red2 + w * 256 + lane * 4);
      st128(wsr, WS_CTXP + (j * 256 + lane * 4) * 4, __builtin_bit_cast(u32x4, s));
      DRAIN();
      if (lane == 0) flag_set(ws, WS_B4FLAG + j * 8, tag);
      wait_flags(ws, WS_B4FLAG + (2 * hh + (lane & 1)) * 8, lane < 2, tag);
    }
    __syncthreads();
  }
  {
    float* ov = (float*)(lds + L_SCR);
    if (t < 128) {
      const int bb = t >> 6, r = t & 63;
      const float v0 = __uint_as_float(ld32(wsr, WS_CTXP + (((2 * hh + 0) * 2 + bb) * 128 + 64 * half + r) * 4));
      const float v1 = __uint_as_float(ld32(wsr, WS_CTXP + (((2 * hh + 1) * 2 + bb) * 128 + 64 * half + r) * 4));
      ov[t] = bfr(v0 + v1);
    }
    __syncthreads();
    proj_compute(lds, wsr, ws, WS_OUTB + j * 4608, WS_B5FLAG + j * 8, tag, t, wP);
  }
  proj_load(p.Awo, j, t, wP);
  if (j < 36) final_reduce(lds, wsr, ws, WS_OUTB, WS_B5FLAG, tag, j, t, p.out, 576);
  {
    const int hq = j >> 1, dh = j & 1, hkv = hq >> 1, g = hq & 1;
    __syncthreads();
    if (wave == 0) {
      const int bb = lane / 6, s = lane % 6;
      wait_flags(ws, WS_AFLAG + (((bb & 1) * 16 + hkv) * 6 + s) * 8, lane < 12, tag);
    }
    __syncthreads();
    float* ov = (float*)(lds + L_SCR);
    if (t < 128) {
      const int bb = t >> 6, r = t & 63;
      const int a0i = (bb * 16 + hkv) * 6;
      float mm[6], ll[6], oo[6];
#pragma unroll
      for (int s = 0; s < 6; ++s) {
        const u32x2 ml = ld64(wsr, WS_AML + ((a0i + s) * 2 + g) * 8);
        mm[s] = __uint_as_float(ml[0]); ll[s] = __uint_as_float(ml[1]);
        oo[s] = __uint_as_float(ld32(wsr, WS_APART + (((a0i + s) * 2 + g) * 128 + 64 * dh + r) * 4));
      }
      float M = mm[0];
#pragma unroll
      for (int s = 1; s < 6; ++s) M = fmaxf(M, mm[s]);
      float L = 0.f, o = 0.f;
#pragma unroll
      for (int s = 0; s < 6; ++s) { const float w = ex2((mm[s] - M) * L2E); L = fmaf(w, ll[s], L); o = fmaf(w, oo[s], o); }
      ov[t] = bfr(o / L);
    }
    __syncthreads();
    proj_compute(lds, wsr, ws, WS_OUTA + j * 4608, WS_ATFLAG + j * 8, tag, t, wP);
  }
  if (j < 36) {
    __syncthreads();
    final_reduce(lds, wsr, ws, WS_OUTA, WS_ATFLAG, tag, j, t, p.out, 0);
    if (j == 0 && t == 0) flag_set(ws, WS_EPOCH, tag);
  }
}

extern "C" void kernel_launch(void* const* d_in, const int* in_sizes, int n_in, void* d_out, int out_size, void* d_ws, size_t ws_size,
                              hipStream_t stream) {
  Params p;
  p.Ax = (const u16*)d_in[0]; p.Ak = (const u16*)d_in[1]; p.Av = (const u16*)d_in[2]; p.Awq = (const u16*)d_in[3]; p.Awo = (const u16*)d_in[4];
  p.Bx = (const u16*)d_in[5]; p.Bc = (const u16*)d_in[6]; p.Bwq = (const u16*)d_in[7]; p.Bwuk = (const u16*)d_in[8]; p.Bwuv = (const u16*)d_in[9];
  p.Bwo = (const u16*)d_in[10];
  p.out = (u16*)d_out; p.ws = (unsigned char*)d_ws;
  fused_kernel<<<dim3(NWG), dim3(NT), 0, stream>>>(p);
}
```

```cpp
#include <hip/hip_runtime.h>
#include <stdint.h>

typedef unsigned short u16;
typedef unsigned int u32;
typedef unsigned long long u64;
typedef u32 u32x4 __attribute__((ext_vector_type(4)));
typedef u32 u32x2 __attribute__((ext_vector_type(2)));
typedef float f32x4 __attribute__((ext_vector_type(4)));
typedef float f32x16 __attribute__((ext_vector_type(16)));
typedef __bf16 bf16x2 __attribute__((ext_vector_type(2)));
typedef __bf16 bf16x8 __attribute__((ext_vector_type(8)));
typedef short s16x4 __attribute__((ext_vector_type(4)));
typedef short s16x8 __attribute__((ext_vector_type(8)));
#define GAS __attribute__((address_space(1)))
#define LAS __attribute__((address_space(3)))

constexpr int NT = 512;
constexpr int NWG = 256;
constexpr int ASPLIT = 6;
constexpr float SCALE = 0.08838834764831845f;
constexpr float L2E = 1.4426950408889634f;
constexpr int SPIN_MAX = 300000;

constexpr u32 WS_EPOCH = 0;
constexpr u32 WS_B2FLAG = 256;
constexpr u32 WS_B3FLAG = WS_B2FLAG + 512;
constexpr u32 WS_QG = 4096;
constexpr u32 WS_QH = WS_QG + 65536;
constexpr u32 WS_QL = WS_QH + 65536;
constexpr u32 WS_BML = WS_QL + 65536;
constexpr u32 WS_OUTB = WS_BML + 16384;
constexpr u32 WS_OUTA = WS_OUTB + 589824;
constexpr u32 WS_APG = WS_OUTA + 589824;
constexpr u32 WS_BPART = WS_APG + 401408;
constexpr u32 WS_END = WS_BPART + 2097152;

constexpr int CSTR = 1040;
constexpr int L_C = 0;
constexpr int L_WUV = 0;
constexpr int L_QH = 65536;
constexpr int L_QL = 98816;
constexpr int L_OT = 65536;
constexpr int L_WO = 65536;
constexpr int L_CA = 139264;
constexpr int L_RED2 = 143360;
constexpr int L_XB = 133120;
constexpr int L_P = 149504;
constexpr int L_ST = 158208;
constexpr int L_SCR = 159744;
constexpr int L_MRG = 81920;
constexpr int LDS_BYTES = 163840;

struct Params {
  const u16* Ax; const u16* Ak; const u16* Av; const u16* Awq; const u16* Awo;
  const u16* Bx; const u16* Bc; const u16* Bwq; const u16* Bwuk; const u16* Bwuv; const u16* Bwo;
  u16* out; unsigned char* ws;
};

__device__ __forceinline__ float bf2f(u16 v) { return __uint_as_float(((u32)v) << 16); }
__device__ __forceinline__ float bflo(u32 v) { return __uint_as_float(v << 16); }
__device__ __forceinline__ float bfhi(u32 v) { return __uint_as_float(v & 0xffff0000u); }
__device__ __forceinline__ float bfr(float f) { return (float)(__bf16)f; }
__device__ __forceinline__ u32 pack_bf16(float lo, float hi) {
  bf16x2 v = {(__bf16)lo, (__bf16)hi};
  return __builtin_bit_cast(u32, v);
}
__device__ __forceinline__ float dot2(u32 a, u32 b, float c) {
  return __builtin_amdgcn_fdot2_f32_bf16(__builtin_bit_cast(bf16x2, a), __builtin_bit_cast(bf16x2, b), c, false);
}
template <int CTRL> __device__ __forceinline__ float dppf(float x) {
  return __builtin_bit_cast(float, __builtin_amdgcn_mov_dpp(__builtin_bit_cast(int, x), CTRL, 0xf, 0xf, true));
}
__device__ __forceinline__ float row16_sum(float x) {
  x += dppf<0x128>(x); x += dppf<0x124>(x); x += dppf<0x122>(x); x += dppf<0x121>(x);
  return x;
}
__device__ __forceinline__ float xsum16(float x) { auto r = __builtin_amdgcn_permlane16_swap(__float_as_uint(x), __float_as_uint(x), false, false); return __uint_as_float(r[0]) + __uint_as_float(r[1]); }
__device__ __forceinline__ float xsum32(float x) { auto r = __builtin_amdgcn_permlane32_swap(__float_as_uint(x), __float_as_uint(x), false, false); return __uint_as_float(r[0]) + __uint_as_float(r[1]); }
__device__ __forceinline__ float xmax16(float x) { auto r = __builtin_amdgcn_permlane16_swap(__float_as_uint(x), __float_as_uint(x), false, false); return fmaxf(__uint_as_float(r[0]), __uint_as_float(r[1])); }
__device__ __forceinline__ float xmax32(float x) { auto r = __builtin_amdgcn_permlane32_swap(__float_as_uint(x), __float_as_uint(x), false, false); return fmaxf(__uint_as_float(r[0]), __uint_as_float(r[1])); }
__device__ __forceinline__ float ex2(float x) { return __builtin_amdgcn_exp2f(x); }

typedef __amdgpu_buffer_rsrc_t rsrc_t;
__device__ __forceinline__ u32x4 ld128(rsrc_t r, u32 off) { return __builtin_amdgcn_raw_buffer_load_b128(r, off, 0, 16); }
__device__ __forceinline__ u32x2 ld64(rsrc_t r, u32 off) { return __builtin_amdgcn_raw_buffer_load_b64(r, off, 0, 16); }
__device__ __forceinline__ void st128(rsrc_t r, u32 off, u32x4 v) { __builtin_amdgcn_raw_buffer_store_b128(v, r, off, 0, 16); }
__device__ __forceinline__ void st64(rsrc_t r, u32 off, u32x2 v) { __builtin_amdgcn_raw_buffer_store_b64(v, r, off, 0, 16); }
__device__ __forceinline__ u32x4 ldnt(const u16* p) { return __builtin_nontemporal_load((const u32x4*)p); }

__device__ __forceinline__ void flag_set(unsigned char* ws, u32 off, u64 tag) {
  __hip_atomic_store((GAS u64*)(ws + off), tag, __ATOMIC_RELAXED, __HIP_MEMORY_SCOPE_AGENT);
}
__device__ __forceinline__ u64 flag_get(unsigned char* ws, u32 off) {
  return __hip_atomic_load((GAS u64*)(ws + off), __ATOMIC_RELAXED, __HIP_MEMORY_SCOPE_AGENT);
}
#define DRAIN() asm volatile("s_waitcnt vmcnt(0)" ::: "memory")
#define BAR() asm volatile("s_waitcnt lgkmcnt(0)\n\ts_barrier" ::: "memory")
#define LGKM0() asm volatile("s_waitcnt lgkmcnt(0)" ::: "memory")

__device__ __forceinline__ void glds16(const void* gsrc, u32 lds_dst) {
  u32 keep;
  asm volatile("s_mov_b32 %0, m0\n\ts_mov_b32 m0, %2\n\ts_nop 0\n\tglobal_load_lds_dwordx4 %1, off nt\n\ts_mov_b32 m0, %0"
               : "=&s"(keep) : "v"(gsrc), "s"(lds_dst) : "memory");
}

__device__ __forceinline__ void wait_flags(unsigned char* ws, u32 off, bool active, u64 tag) {
  for (int spin = 0; spin < SPIN_MAX; ++spin) {
    u64 v = tag;
    if (active) v = flag_get(ws, off);
    if (__all(v == tag)) break;
    __builtin_amdgcn_s_sleep(1);
  }
  __builtin_amdgcn_fence(__ATOMIC_ACQUIRE, "wavefront");
}
__device__ __forceinline__ void gst(rsrc_t r, u32 off, u32 d0, u32 d1, u64 tag) { st128(r, off, u32x4{d0, d1, (u32)tag, (u32)(tag >> 32)}); }
__device__ __forceinline__ bool gok(u32x4 g, u64 tag) { return g[2] == (u32)tag && g[3] == (u32)(tag >> 32); }
template <int N> __device__ __forceinline__ void gpoll(rsrc_t r, const u32 (&off)[N], u32x4 (&g)[N], bool need, u64 tag) {
  for (int spin = 0; spin < SPIN_MAX; ++spin) {
    bool ok = true;
#pragma unroll
    for (int i = 0; i < N; ++i) { g[i] = ld128(r, off[i]); ok = ok && gok(g[i], tag); }
    if (__all(ok || !need)) break;
    __builtin_amdgcn_s_sleep(1);
  }
}

#define STAMP(i) do {} while (0)
#define STAMP_FLUSH() do {} while (0)

__device__ __forceinline__ bf16x8 tr_pair(const unsigned char* a0, const unsigned char* a1) {
  const s16x4 t0 = __builtin_amdgcn_ds_read_tr16_b64_v4i16((LAS s16x4*)a0);
  const s16x4 t1 = __builtin_amdgcn_ds_read_tr16_b64_v4i16((LAS s16x4*)a1);
  return __builtin_bit_cast(bf16x8, __builtin_shufflevector(t0, t1, 0, 1, 2, 3, 4, 5, 6, 7));
}

__device__ __forceinline__ void proj_mfma(unsigned char* lds, int wbase, rsrc_t wsr, u32 out_off, u64 tag, int wave, int lane) {
  const int g4 = lane >> 4, i16 = lane & 15, qq = i16 >> 2, pp = i16 & 3;
  bf16x8 af[2];
#pragma unroll
  for (int ks = 0; ks < 2; ++ks) {
    u32x2 lo = {0u, 0u}, hi = {0u, 0u};
    if (i16 < 2) {
      lo = *(const u32x2*)(lds + L_SCR + 2048 + i16 * 128 + (32 * ks + 4 * g4) * 2);
      hi = *(const u32x2*)(lds + L_SCR + 2048 + i16 * 128 + (32 * ks + 16 + 4 * g4) * 2);
    }
    af[ks] = __builtin_bit_cast(bf16x8, u32x4{lo[0], lo[1], hi[0], hi[1]});
  }
  const int r0 = 4 * g4 + qq;
  const int sw = (r0 >> 1) & 3;
#pragma unroll
  for (int i = 0; i < 5; ++i) {
    const int nb = wave + 8 * i;
    if (nb < 36) {
      f32x4 d = {0.f, 0.f, 0.f, 0.f};
      const unsigned char* base = lds + wbase + r0 * 1152 + ((nb ^ sw) * 32) + pp * 8;
#pragma unroll
      for (int ks = 0; ks < 2; ++ks) {
        const bf16x8 b = tr_pair(base + (32 * ks) * 1152, base + (32 * ks + 16) * 1152);
        d = __builtin_amdgcn_mfma_f32_16x16x32_bf16(af[ks], b, d, 0, 0, 0);
      }
      if (lane < 16) gst(wsr, out_off + (16 * nb + lane) * 16, __float_as_uint(d[0]), __float_as_uint(d[1]), tag);
    }
  }
}
__device__ __forceinline__ void final_reduce(unsigned char* lds, rsrc_t wsr, u32 part_off, u64 tag, int f, int t, u16* out, int base) {
  const int wave = t >> 6, lane = t & 63;
  const int pidx = t >> 3, sub = t & 7;
  u32 off[2] = {part_off + (u32)(pidx * 576 + 16 * f + sub) * 16, part_off + (u32)(pidx * 576 + 16 * f + 8 + sub) * 16};
  u32x4 g[2];
  gpoll<2>(wsr, off, g, true, tag);
  float v[4] = {__uint_as_float(g[0][0]), __uint_as_float(g[0][1]), __uint_as_float(g[1][0]), __uint_as_float(g[1][1])};
#pragma unroll
  for (int k = 0; k < 4; ++k) { float x = v[k]; x += dppf<0x128>(x); x = xsum16(x); x = xsum32(x); v[k] = x; }
  float* red = (float*)(lds + L_SCR);
  if (lane < 8) *(f32x4*)(red + wave * 32 + lane * 4) = f32x4{v[0], v[1], v[2], v[3]};
  BAR();
  if (t < 32) {
    float s = 0.f;
#pragma unroll
    for (int w = 0; w < 8; ++w) s += red[w * 32 + t];
    const int sb = t >> 2, k = t & 3, bb = k & 1, col = sb + 8 * (k >> 1);
    __bf16 o = (__bf16)s;
    out[bb * 1152 + base + 16 * f + col] = __builtin_bit_cast(u16, o);
  }
}

template <int NL, int RS>
__device__ __forceinline__ void q_load(const u16* qW, const u16* qx, int qcol0, int t, u32x4 (&qw)[NL], u16 (&qx0)[NL], u16 (&qx1)[NL]) {
  const int cch = t & 3, row0 = t >> 2;
#pragma unroll
  for (int i = 0; i < NL; ++i) {
    const int row = row0 + RS * i, rc = row < 576 ? row : 575;
    qx0[i] = qx[rc]; qx1[i] = qx[576 + rc];
  }
#pragma unroll
  for (int i = 0; i < NL; ++i) {
    const int row = row0 + RS * i, rc = row < 576 ? row : 575;
    qw[i] = ldnt(qW + (size_t)rc * 4096 + qcol0 + cch * 8);
  }
}
template <int NL, int RS>
__device__ __forceinline__ void q_fma(int t, const u32x4 (&qw)[NL], const u16 (&qx0)[NL], const u16 (&qx1)[NL], float (&qa0)[8], float (&qa1)[8]) {
#pragma unroll
  for (int k = 0; k < 8; ++k) { qa0[k] = 0.f; qa1[k] = 0.f; }
#pragma unroll
  for (int i = 0; i < NL; ++i) {
    const bool live = (t >> 2) + RS * i < 576;
    const float x0 = live ? bf2f(qx0[i]) : 0.f, x1 = live ? bf2f(qx1[i]) : 0.f;
#pragma unroll
    for (int k = 0; k < 4; ++k) {
      const float lo = bflo(qw[i][k]), hi = bfhi(qw[i][k]);
      qa0[2 * k] = fmaf(x0, lo, qa0[2 * k]); qa0[2 * k + 1] = fmaf(x0, hi, qa0[2 * k + 1]);
      qa1[2 * k] = fmaf(x1, lo, qa1[2 * k]); qa1[2 * k + 1] = fmaf(x1, hi, qa1[2 * k + 1]);
    }
  }
}
__device__ __forceinline__ void q_reduce(unsigned char* lds, int wave, int lane, float (&qa0)[8], float (&qa1)[8]) {
#pragma unroll
  for (int k = 0; k < 8; ++k) {
    float x = qa0[k]; x += dppf<0x124>(x); x += dppf<0x128>(x); x = xsum16(x); x = xsum32(x); qa0[k] = x;
    float y = qa1[k]; y += dppf<0x124>(y); y += dppf<0x128>(y); y = xsum16(y); y = xsum32(y); qa1[k] = y;
  }
  float* red_ = (float*)(lds + L_SCR);
  if (lane < 4) {
    float* r0 = red_ + wave * 64 + lane * 8;
    *(f32x4*)r0 = f32x4{qa0[0], qa0[1], qa0[2], qa0[3]}; *(f32x4*)(r0 + 4) = f32x4{qa0[4], qa0[5], qa0[6], qa0[7]};
    *(f32x4*)(r0 + 32) = f32x4{qa1[0], qa1[1], qa1[2], qa1[3]}; *(f32x4*)(r0 + 36) = f32x4{qa1[4], qa1[5], qa1[6], qa1[7]};
  }
}
template <int NW>
__device__ __forceinline__ void q_publish(unsigned char* lds, rsrc_t wsr, int qmat, int qcol0, int t, u64 tag) {
  if (t < 16) {
    const float* red_ = (const float*)(lds + L_SCR);
    f32x4 s_ = *(const f32x4*)(red_ + t * 4);
#pragma unroll
    for (int w = 1; w < NW; ++w) s_ += *(const f32x4*)(red_ + w * 64 + t * 4);
    gst(wsr, WS_QG + ((qmat * 2 + (t >> 3)) * 1024 + (qcol0 >> 2) + (t & 7)) * 16, pack_bf16(s_[0], s_[1]), pack_bf16(s_[2], s_[3]), tag);
  }
}

__global__ void __launch_bounds__(NT, 2) fused_kernel(Params p) {
  __shared__ __attribute__((aligned(16))) unsigned char lds[LDS_BYTES];
  const int bid = blockIdx.x, t = threadIdx.x, lane = t & 63;
  const int wave = __builtin_amdgcn_readfirstlane(t >> 6);
  unsigned char* ws = p.ws;
  const rsrc_t wsr = __builtin_amdgcn_make_buffer_rsrc(ws, 0, WS_END, 0x00020000);
  const u32 ldsb = (u32)(uintptr_t)(LAS unsigned char*)lds;
  STAMP(0);
  u32 vz; asm volatile("v_mov_b32 %0, 0" : "=v"(vz));
  const u64 epoch = flag_get(ws + vz, WS_EPOCH);
  const bool isB = ((bid >> 3) & 3) == 3;
  const int bj = ((bid >> 5) << 3) | (bid & 7);
  const int ai = (bid >> 5) * 24 + ((bid >> 3) & 3) * 8 + (bid & 7);
  const int qi = ((bid >> 4) << 4) | ((bid & 7) << 1) | ((bid >> 3) & 1);
  const int qmat = qi >> 7, qcol0 = (qi & 127) * 32;
  const u16* qW = qmat ? p.Bwq : p.Awq;
  const u16* qx = qmat ? p.Bx : p.Ax;

  if (!isB) {
    float qa0[8], qa1[8];
    {
      u32x4 qw[5]; u16 qx0[5], qx1[5];
      q_load<5, 128>(qW, qx, qcol0, t, qw, qx0, qx1);
      __builtin_amdgcn_sched_barrier(0);
      q_fma<5, 128>(t, qw, qx0, qx1, qa0, qa1);
    }
    const u64 tag = epoch + 1ull;
    const int pair = ai / ASPLIT, split = ai % ASPLIT;
    const int b = pair >> 4, hkv = pair & 15;
    const int ustart = split * 42 + (split < 4 ? split : 4), ucnt = split < 4 ? 43 : 42;
    const int nw = (ucnt - wave + 7) >> 3;
    const u16* Kb = p.Ak + (size_t)pair * 4096 * 128 + (size_t)(ustart + wave) * 2048 + (lane >> 4) * 128 + (lane & 15) * 8;
    const u16* Vb = p.Av + (size_t)pair * 4096 * 128 + (size_t)(ustart + wave) * 2048 + (lane >> 4) * 128 + (lane & 15) * 8;
    u32x4 K0[4], V0[4], K1[4], V1[4], K2[4], V2[4];
#define A_LOAD(KK, VV, it) do { _Pragma("unroll") for (int i_ = 0; i_ < 4; ++i_) { KK[i_] = ldnt(Kb + (size_t)(it) * 16384 + i_ * 512); } \
                                _Pragma("unroll") for (int i_ = 0; i_ < 4; ++i_) { VV[i_] = ldnt(Vb + (size_t)(it) * 16384 + i_ * 512); } } while (0)
    A_LOAD(K0, V0, 0); A_LOAD(K1, V1, 1); A_LOAD(K2, V2, 2);
    q_reduce(lds, wave, lane, qa0, qa1);
    BAR();
    q_publish<8>(lds, wsr, qmat, qcol0, t, tag);
    STAMP(1);
    if (ai < 64) {
      const unsigned char* src = (const unsigned char*)(p.Awo + (size_t)ai * 64 * 576);
#pragma unroll
      for (int i = 0; i < 9; ++i) {
        const int L = i * 512 + t, r = L / 72, pc = L - r * 72;
        const int c = (((pc >> 1) ^ ((r >> 1) & 3)) << 1) | (pc & 1);
        glds16(src + r * 1152 + c * 16, ldsb + (u32)(i * 512 + wave * 64) * 16);
      }
    }
    u32x4 q0, q1;
    {
      const u32 qb = WS_QG + ((0 * 2 + b) * 1024 + (2 * hkv) * 32 + (lane & 15) * 2) * 16;
      u32 off[4] = {qb, qb + 16, qb + 512, qb + 528};
      u32x4 g[4];
      gpoll<4>(wsr, off, g, true, tag);
      q0 = u32x4{g[0][0], g[0][1], g[1][0], g[1][1]}; q1 = u32x4{g[2][0], g[2][1], g[3][0], g[3][1]};
    }
    STAMP(2);
    float m0 = -INFINITY, m1 = -INFINITY, l0 = 0.f, l1 = 0.f;
    float acc0[8], acc1[8];
#pragma unroll
    for (int k = 0; k < 8; ++k) { acc0[k] = 0.f; acc1[k] = 0.f; }
#define A_COMPUTE(KK, VV) do { \
      float s0_[4], s1_[4]; \
      _Pragma("unroll") for (int i_ = 0; i_ < 4; ++i_) { \
        float d0_ = 0.f, d1_ = 0.f; \
        _Pragma("unroll") for (int k_ = 0; k_ < 4; ++k_) { d0_ = dot2(KK[i_][k_], q0[k_], d0_); d1_ = dot2(KK[i_][k_], q1[k_], d1_); } \
        d0_ = row16_sum(d0_); d1_ = row16_sum(d1_); \
        s0_[i_] = bfr(bfr(d0_) * SCALE); s1_[i_] = bfr(bfr(d1_) * SCALE); } \
      const float mx0_ = fmaxf(fmaxf(s0_[0], s0_[1]), fmaxf(s0_[2], s0_[3])), mx1_ = fmaxf(fmaxf(s1_[0], s1_[1]), fmaxf(s1_[2], s1_[3])); \
      if (__any(mx0_ > m0 || mx1_ > m1)) { \
        const float n0_ = fmaxf(m0, mx0_), n1_ = fmaxf(m1, mx1_); \
        const float al0_ = ex2((m0 - n0_) * L2E), al1_ = ex2((m1 - n1_) * L2E); \
        l0 *= al0_; l1 *= al1_; m0 = n0_; m1 = n1_; \
        _Pragma("unroll") for (int k_ = 0; k_ < 8; ++k_) { acc0[k_] *= al0_; acc1[k_] *= al1_; } } \
      float p0_[4], p1_[4]; \
      _Pragma("unroll") for (int i_ = 0; i_ < 4; ++i_) { p0_[i_] = ex2((s0_[i_] - m0) * L2E); p1_[i_] = ex2((s1_[i_] - m1) * L2E); } \
      l0 += (p0_[0] + p0_[1]) + (p0_[2] + p0_[3]); l1 += (p1_[0] + p1_[1]) + (p1_[2] + p1_[3]); \
      const u32 pa0_ = pack_bf16(p0_[0], p0_[1]), pb0_ = pack_bf16(p0_[2], p0_[3]), pa1_ = pack_bf16(p1_[0], p1_[1]), pb1_ = pack_bf16(p1_[2], p1_[3]); \
      _Pragma("unroll") for (int k_ = 0; k_ < 4; ++k_) { \
        const u32 lo01_ = __builtin_amdgcn_perm(VV[1][k_], VV[0][k_], 0x05040100u), hi01_ = __builtin_amdgcn_perm(VV[1][k_], VV[0][k_], 0x07060302u); \
        const u32 lo23_ = __builtin_amdgcn_perm(VV[3][k_], VV[2][k_], 0x05040100u), hi23_ = __builtin_amdgcn_perm(VV[3][k_], VV[2][k_], 0x07060302u); \
        acc0[2 * k_] = dot2(lo23_, pb0_, dot2(lo01_, pa0_, acc0[2 * k_])); acc0[2 * k_ + 1] = dot2(hi23_, pb0_, dot2(hi01_, pa0_, acc0[2 * k_ + 1])); \
        acc1[2 * k_] = dot2(lo23_, pb1_, dot2(lo01_, pa1_, acc1[2 * k_])); acc1[2 * k_ + 1] = dot2(hi23_, pb1_, dot2(hi01_, pa1_, acc1[2 * k_ + 1])); } \
    } while (0)
    A_COMPUTE(K0, V0); A_LOAD(K0, V0, 3);
    A_COMPUTE(K1, V1); A_LOAD(K1, V1, 4);
    if (nw == 6) {
      A_COMPUTE(K2, V2); A_LOAD(K2, V2, 5);
      A_COMPUTE(K0, V0);
      A_COMPUTE(K1, V1);
      A_COMPUTE(K2, V2);
    } else {
      A_COMPUTE(K2, V2);
      A_COMPUTE(K0, V0);
      A_COMPUTE(K1, V1);
    }
    STAMP(3);
    float* mrg = (float*)(lds + L_MRG);
    float* mml = (float*)(lds + L_ST);
    {
      const float M0 = xmax32(xmax16(m0)), M1 = xmax32(xmax16(m1));
      const float a0 = ex2((m0 - M0) * L2E), a1 = ex2((m1 - M1) * L2E);
      l0 = xsum32(xsum16(l0 * a0)); l1 = xsum32(xsum16(l1 * a1));
#pragma unroll
      for (int k = 0; k < 8; ++k) { acc0[k] = xsum32(xsum16(acc0[k] * a0)); acc1[k] = xsum32(xsum16(acc1[k] * a1)); }
      if (lane < 16) {
        float* r0 = mrg + (wave * 2 + 0) * 128 + lane * 8; float* r1 = mrg + (wave * 2 + 1) * 128 + lane * 8;
        *(f32x4*)r0 = f32x4{acc0[0], acc0[1], acc0[2], acc0[3]}; *(f32x4*)(r0 + 4) = f32x4{acc0[4], acc0[5], acc0[6], acc0[7]};
        *(f32x4*)r1 = f32x4{acc1[0], acc1[1], acc1[2], acc1[3]}; *(f32x4*)(r1 + 4) = f32x4{acc1[4], acc1[5], acc1[6], acc1[7]};
        if (lane == 0) *(f32x4*)(mml + wave * 4) = f32x4{M0, l0, M1, l1};
      }
    }
    BAR();
    if (t < 128) {
      const int g = t >> 6, k = t & 63;
      float M = -INFINITY;
#pragma unroll
      for (int r = 0; r < 8; ++r) M = fmaxf(M, mml[r * 4 + g * 2]);
      float L = 0.f, o0 = 0.f, o1 = 0.f;
#pragma unroll
      for (int r = 0; r < 8; ++r) {
        const float w = ex2((mml[r * 4 + g * 2] - M) * L2E); L = fmaf(w, mml[r * 4 + g * 2 + 1], L);
        const float2 v = *(const float2*)(mrg + (r * 2 + g) * 128 + 2 * k);
        o0 = fmaf(w, v.x, o0); o1 = fmaf(w, v.y, o1);
      }
      gst(wsr, WS_APG + ((ai * 2 + g) * 65 + k) * 16, __float_as_uint(o0), __float_as_uint(o1), tag);
      if (k == 0) gst(wsr, WS_APG + ((ai * 2 + g) * 65 + 64) * 16, __float_as_uint(M), __float_as_uint(L), tag);
    }
    STAMP(4);
    if (ai < 64) {
      const int hq = ai >> 1, dh = ai & 1, thkv = hq >> 1, g = hq & 1;
      if (wave == 0) {
        const int bb = lane >> 5, k = lane & 31;
        const int a0i = (bb * 16 + thkv) * 6;
        u32 off[12]; u32x4 gg[12];
#pragma unroll
        for (int s = 0; s < 6; ++s) { off[s] = WS_APG + (((a0i + s) * 2 + g) * 65 + 32 * dh + k) * 16; off[6 + s] = WS_APG + (((a0i + s) * 2 + g) * 65 + 64) * 16; }
        gpoll<12>(wsr, off, gg, true, tag);
        float M = __uint_as_float(gg[6][0]);
#pragma unroll
        for (int s = 1; s < 6; ++s) M = fmaxf(M, __uint_as_float(gg[6 + s][0]));
        float L = 0.f, o0 = 0.f, o1 = 0.f;
#pragma unroll
        for (int s = 0; s < 6; ++s) {
          const float w = ex2((__uint_as_float(gg[6 + s][0]) - M) * L2E);
          L = fmaf(w, __uint_as_float(gg[6 + s][1]), L); o0 = fmaf(w, __uint_as_float(gg[s][0]), o0); o1 = fmaf(w, __uint_as_float(gg[s][1]), o1);
        }
        *(u32*)(lds + L_SCR + 2048 + bb * 128 + k * 4) = pack_bf16(o0 / L, o1 / L);
      }
      DRAIN();
      BAR();
      STAMP(5);
      proj_mfma(lds, 0, wsr, WS_OUTA + ai * 9216, tag, wave, lane);
      STAMP(6);
    } else if (ai < 100) {
      final_reduce(lds, wsr, WS_OUTA, tag, ai - 64, t, p.out, 0);
      if (ai == 64) {
        if (wave == 0) { wait_flags(ws, WS_B3FLAG + lane * 8, true, tag); if (lane == 0) flag_set(ws, WS_EPOCH, tag); }
      }
      STAMP(7);
    }
    STAMP_FLUSH();
    return;
  }

  const int j = bj;
  const int hh = j >> 1, half = j & 1;
  const int b3b = j >> 5, b3ks = j & 31;
  const int kb = wave & 3, kh = wave >> 2;
  const u64 tag = epoch + 1ull;
  if (wave >= 4) {
    const unsigned char* src = (const unsigned char*)(p.Bc + ((size_t)b3b * 4096 + 128 * b3ks + 32 * (wave - 4)) * 512) + lane * 16;
#pragma unroll
    for (int r = 0; r < 32; ++r) glds16(src + r * 1024, ldsb + L_C + (u32)(32 * (wave - 4) + r) * CSTR);
    BAR();
    BAR();
    DRAIN();
  } else {
    u32x4 wk[16];
    float qa0[8], qa1[8];
    {
      u32x4 qw[9]; u16 qx0[9], qx1[9];
      q_load<9, 64>(qW, qx, qcol0, t, qw, qx0, qx1);
      __builtin_amdgcn_sched_barrier(0);
#pragma unroll
      for (int i = 0; i < 16; ++i) wk[i] = ldnt(p.Bwuk + ((size_t)(256 * half + (t >> 4) + 16 * i) * 32 + hh) * 128 + (t & 15) * 8);
      __builtin_amdgcn_sched_barrier(0);
      q_fma<9, 64>(t, qw, qx0, qx1, qa0, qa1);
    }
    q_reduce(lds, wave, lane, qa0, qa1);
    BAR();
    q_publish<4>(lds, wsr, qmat, qcol0, t, tag);
    STAMP(1);
    u32x4 qa, qb;
    {
      const u32 qo = WS_QG + ((1 * 2 + 0) * 1024 + hh * 32 + (t & 15) * 2) * 16;
      u32 off[4] = {qo, qo + 16, qo + 16384, qo + 16400};
      u32x4 g[4];
      gpoll<4>(wsr, off, g, true, tag);
      qa = u32x4{g[0][0], g[0][1], g[1][0], g[1][1]}; qb = u32x4{g[2][0], g[2][1], g[3][0], g[3][1]};
    }
    STAMP(2);
    float* qs = (float*)(lds + L_SCR + 2048);
#pragma unroll
    for (int i = 0; i < 16; ++i) {
      float d0 = 0.f, d1 = 0.f;
#pragma unroll
      for (int k = 0; k < 4; ++k) { d0 = dot2(wk[i][k], qa[k], d0); d1 = dot2(wk[i][k], qb[k], d1); }
      d0 = row16_sum(d0); d1 = row16_sum(d1);
      if ((t & 15) == 0) { qs[(t >> 4) + 16 * i] = d0; qs[256 + (t >> 4) + 16 * i] = d1; }
    }
    BAR();
    if (wave == 0) {
      const int bb = lane >> 5, c8 = lane & 31;
      u32x4 hi, lo;
#pragma unroll
      for (int k = 0; k < 4; ++k) {
        const float v0 = qs[bb * 256 + c8 * 8 + 2 * k], v1 = qs[bb * 256 + c8 * 8 + 2 * k + 1];
        const float h0 = bfr(v0), h1 = bfr(v1);
        hi[k] = pack_bf16(h0, h1); lo[k] = pack_bf16(v0 - h0, v1 - h1);
      }
      const u32 o = ((bb * 32 + hh) * 512 + 256 * half + c8 * 8) * 2;
      st128(wsr, WS_QH + o, hi); st128(wsr, WS_QL + o, lo);
      DRAIN();
      if (lane == 0) flag_set(ws, WS_B2FLAG + j * 8, tag);
    }
    STAMP(3);
  }
  BAR();
  STAMP(4);
  const int head = lane & 31, hi = lane >> 5;
  const int g4 = lane >> 4, i16 = lane & 15, qq = i16 >> 2, pp = i16 & 3;
  u32x4 cf[16];
  bf16x8 pv[2][8];
  {
    const int pi = 16 * (head >> 4) + 4 * (head & 3) + 2 * ((head >> 3) & 1) + ((head >> 2) & 1);
    const unsigned char* cr = lds + L_C + (32 * kb + pi) * CSTR + (256 * kh + 8 * hi) * 2;
#pragma unroll
    for (int s = 0; s < 16; ++s) cf[s] = *(const u32x4*)(cr + s * 32);
    const unsigned char* tb = lds + L_C + (4 * qq + 2 * hi) * CSTR + (64 * wave + 16 * (g4 & 1) + 4 * pp) * 2;
#pragma unroll
    for (int ks = 0; ks < 8; ++ks) {
      pv[0][ks] = tr_pair(tb + (16 * ks) * CSTR, tb + (16 * ks + 1) * CSTR);
      pv[1][ks] = tr_pair(tb + (16 * ks) * CSTR + 64, tb + (16 * ks + 1) * CSTR + 64);
    }
  }
  if (wave == 0) wait_flags(ws, WS_B2FLAG + lane * 8, true, tag);
  BAR();
  STAMP(5);
  if (wave < 4) {
    u32x4 vh[8], vl[8];
#pragma unroll
    for (int i = 0; i < 8; ++i) {
      const int idx = t + 256 * i;
      vh[i] = ld128(wsr, WS_QH + b3b * 32768 + idx * 16);
      vl[i] = ld128(wsr, WS_QL + b3b * 32768 + idx * 16);
    }
#pragma unroll
    for (int i = 0; i < 8; ++i) {
      const int idx = t + 256 * i;
      *(u32x4*)(lds + L_QH + (idx >> 6) * CSTR + (idx & 63) * 16) = vh[i];
      *(u32x4*)(lds + L_QL + (idx >> 6) * CSTR + (idx & 63) * 16) = vl[i];
    }
  }
  BAR();
  STAMP(6);
  if (wave >= 4) {
    const unsigned char* src = (const unsigned char*)(p.Bwuv + (size_t)hh * 128 + 64 * half);
#pragma unroll
    for (int i = 0; i < 16; ++i) {
      const int L = i * 256 + (t - 256), r = L >> 3, pc = L & 7;
      const int c = (((pc >> 1) ^ ((r >> 1) & 3)) << 1) | (pc & 1);
      glds16(src + (size_t)r * 8192 + c * 16, ldsb + L_WUV + (u32)(i * 256 + (wave - 4) * 64) * 16);
    }
  }
  {
    float* smax = (float*)(lds + L_ST);
    float* slsum = smax + 128;
    f32x16 sacc;
#pragma unroll
    for (int r = 0; r < 16; ++r) sacc[r] = 0.f;
    {
      const unsigned char* bhp = lds + L_QH + head * CSTR + (256 * kh + 8 * hi) * 2;
      const unsigned char* blp = lds + L_QL + head * CSTR + (256 * kh + 8 * hi) * 2;
#pragma unroll
      for (int s = 0; s < 16; ++s) {
        const bf16x8 bh = __builtin_bit_cast(bf16x8, *(const u32x4*)(bhp + s * 32));
        const bf16x8 bl = __builtin_bit_cast(bf16x8, *(const u32x4*)(blp + s * 32));
        sacc = __builtin_amdgcn_mfma_f32_32x32x16_bf16(__builtin_bit_cast(bf16x8, cf[s]), bh, sacc, 0, 0, 0);
        sacc = __builtin_amdgcn_mfma_f32_32x32x16_bf16(__builtin_bit_cast(bf16x8, cf[s]), bl, sacc, 0, 0, 0);
      }
    }
    if (kh == 1) {
#pragma unroll
      for (int r4 = 0; r4 < 4; ++r4)
        *(f32x4*)(lds + L_XB + ((kb * 4 + r4) * 64 + lane) * 16) = f32x4{sacc[4 * r4], sacc[4 * r4 + 1], sacc[4 * r4 + 2], sacc[4 * r4 + 3]};
    }
    BAR();
    float sv[16];
    if (kh == 0) {
      float mx = -INFINITY;
#pragma unroll
      for (int r4 = 0; r4 < 4; ++r4) {
        const f32x4 o = *(const f32x4*)(lds + L_XB + ((kb * 4 + r4) * 64 + lane) * 16);
#pragma unroll
        for (int e = 0; e < 4; ++e) { sv[4 * r4 + e] = bfr(bfr(sacc[4 * r4 + e] + o[e]) * SCALE); mx = fmaxf(mx, sv[4 * r4 + e]); }
      }
      mx = xmax32(mx);
      if (lane < 32) smax[kb * 32 + head] = mx;
    }
    BAR();
    if (kh == 0) {
      const float m = fmaxf(fmaxf(smax[head], smax[32 + head]), fmaxf(smax[64 + head], smax[96 + head]));
      float ls = 0.f;
#pragma unroll
      for (int r4 = 0; r4 < 4; ++r4) {
        float pe[4];
#pragma unroll
        for (int e = 0; e < 4; ++e) { pe[e] = ex2((sv[4 * r4 + e] - m) * L2E); ls += pe[e]; }
        *(u32x2*)(lds + L_P + head * 272 + (32 * kb + 8 * r4 + 4 * hi) * 2) = u32x2{pack_bf16(pe[0], pe[1]), pack_bf16(pe[2], pe[3])};
      }
      ls = xsum32(ls);
      if (lane < 32) slsum[kb * 32 + head] = ls;
    }
    BAR();
    f32x16 oacc0, oacc1;
#pragma unroll
    for (int r = 0; r < 16; ++r) { oacc0[r] = 0.f; oacc1[r] = 0.f; }
    {
      const unsigned char* pa = lds + L_P + head * 272 + hi * 16;
#pragma unroll
      for (int ks = 0; ks < 8; ++ks) {
        const bf16x8 a = __builtin_bit_cast(bf16x8, *(const u32x4*)(pa + ks * 32));
        oacc0 = __builtin_amdgcn_mfma_f32_32x32x16_bf16(pv[0][ks], a, oacc0, 0, 0, 0);
        oacc1 = __builtin_amdgcn_mfma_f32_32x32x16_bf16(pv[1][ks], a, oacc1, 0, 0, 0);
      }
    }
    STAMP(7);
    {
      unsigned char* ot = lds + L_OT + wave * 4608;
#pragma unroll
      for (int r4 = 0; r4 < 4; ++r4) {
        *(u32x2*)(ot + head * 144 + (8 * r4 + 4 * hi) * 2) = u32x2{pack_bf16(oacc0[4 * r4], oacc0[4 * r4 + 1]), pack_bf16(oacc0[4 * r4 + 2], oacc0[4 * r4 + 3])};
        *(u32x2*)(ot + head * 144 + (32 + 8 * r4 + 4 * hi) * 2) = u32x2{pack_bf16(oacc1[4 * r4], oacc1[4 * r4 + 1]), pack_bf16(oacc1[4 * r4 + 2], oacc1[4 * r4 + 3])};
      }
      LGKM0();
#pragma unroll
      for (int i = 0; i < 4; ++i) {
        const int hrow = (lane >> 3) + 8 * i, ch = lane & 7;
        const u32x4 v = *(const u32x4*)(ot + hrow * 144 + ch * 16);
        st128(wsr, WS_BPART + (((b3b * 32 + b3ks) * 32 + hrow) * 512 + 64 * wave + 8 * ch) * 2, v);
      }
    }
    if (t < 32) {
      const float L = (slsum[t] + slsum[32 + t]) + (slsum[64 + t] + slsum[96 + t]);
      const float m = fmaxf(fmaxf(smax[t], smax[32 + t]), fmaxf(smax[64 + t], smax[96 + t]));
      st64(wsr, WS_BML + ((b3b * 32 + b3ks) * 32 + t) * 8, u32x2{__float_as_uint(m), __float_as_uint(L)});
    }
    DRAIN();
    BAR();
    if (t == 0) flag_set(ws, WS_B3FLAG + j * 8, tag);
  }
  STAMP(8);
  if (wave >= 4) {
    const unsigned char* src = (const unsigned char*)(p.Bwo + (size_t)j * 64 * 576);
#pragma unroll
    for (int i = 0; i < 18; ++i) {
      const int L = i * 256 + (t - 256), r = L / 72, pc = L - r * 72;
      const int c = (((pc >> 1) ^ ((r >> 1) & 3)) << 1) | (pc & 1);
      glds16(src + r * 1152 + c * 16, ldsb + L_WO + (u32)(i * 256 + (wave - 4) * 64) * 16);
    }
  }
  {
    if (wave == 0) wait_flags(ws, WS_B3FLAG + lane * 8, true, tag);
    BAR();
    STAMP(9);
    const int rg = lane >> 3, ch = lane & 7;
    u32x4 g[8];
#pragma unroll
    for (int i = 0; i < 8; ++i) g[i] = ld128(wsr, WS_BPART + (((rg + 8 * i) * 32 + hh) * 512 + 64 * wave + 8 * ch) * 2);
    float wgt;
    {
      const u32x2 ml = ld64(wsr, WS_BML + (lane * 32 + hh) * 8);
      const float m = __uint_as_float(ml[0]), l = __uint_as_float(ml[1]);
      float M = m;
      M = fmaxf(M, dppf<0x128>(M)); M = fmaxf(M, dppf<0x124>(M)); M = fmaxf(M, dppf<0x122>(M)); M = fmaxf(M, dppf<0x121>(M)); M = xmax16(M);
      const float w = ex2((m - M) * L2E);
      const float L = xsum16(row16_sum(w * l));
      wgt = w / L;
    }
    float s0[8], s1[8];
#pragma unroll
    for (int k = 0; k < 8; ++k) { s0[k] = 0.f; s1[k] = 0.f; }
#pragma unroll
    for (int i = 0; i < 4; ++i) {
      const float w0 = __shfl(wgt, rg + 8 * i), w1 = __shfl(wgt, 32 + rg + 8 * i);
#pragma unroll
      for (int k = 0; k < 4; ++k) {
        s0[2 * k] = fmaf(w0, bflo(g[i][k]), s0[2 * k]); s0[2 * k + 1] = fmaf(w0, bfhi(g[i][k]), s0[2 * k + 1]);
        s1[2 * k] = fmaf(w1, bflo(g[i + 4][k]), s1[2 * k]); s1[2 * k + 1] = fmaf(w1, bfhi(g[i + 4][k]), s1[2 * k + 1]);
      }
    }
#pragma unroll
    for (int k = 0; k < 8; ++k) {
      float x = s0[k]; x += dppf<0x128>(x); x = xsum16(x); x = xsum32(x); s0[k] = x;
      float y = s1[k]; y += dppf<0x128>(y); y = xsum16(y); y = xsum32(y); s1[k] = y;
    }
    unsigned char* ca = lds + L_CA + wave * 512;
    if (lane < 8) {
      u32x4 h0, l0, h1, l1;
#pragma unroll
      for (int k = 0; k < 4; ++k) {
        const float a = bfr(s0[2 * k]), bq = bfr(s0[2 * k + 1]), c = bfr(s1[2 * k]), d = bfr(s1[2 * k + 1]);
        h0[k] = pack_bf16(a, bq); l0[k] = pack_bf16(s0[2 * k] - a, s0[2 * k + 1] - bq);
        h1[k] = pack_bf16(c, d); l1[k] = pack_bf16(s1[2 * k] - c, s1[2 * k + 1] - d);
      }
      *(u32x4*)(ca + 0 * 128 + lane * 16) = h0; *(u32x4*)(ca + 1 * 128 + lane * 16) = l0;
      *(u32x4*)(ca + 2 * 128 + lane * 16) = h1; *(u32x4*)(ca + 3 * 128 + lane * 16) = l1;
    }
    LGKM0();
    bf16x8 af[2];
#pragma unroll
    for (int ks = 0; ks < 2; ++ks) {
      u32x2 lo = {0u, 0u}, hq = {0u, 0u};
      if (i16 < 4) {
        lo = *(const u32x2*)(ca + i16 * 128 + (32 * ks + 4 * g4) * 2);
        hq = *(const u32x2*)(ca + i16 * 128 + (32 * ks + 16 + 4 * g4) * 2);
      }
      af[ks] = __builtin_bit_cast(bf16x8, u32x4{lo[0], lo[1], hq[0], hq[1]});
    }
    float* red2 = (float*)(lds + L_RED2);
    {
      const int r0 = 4 * g4 + qq, sw = (r0 >> 1) & 3;
#pragma unroll
      for (int nb = 0; nb < 4; ++nb) {
        f32x4 d = {0.f, 0.f, 0.f, 0.f};
        const unsigned char* base = lds + L_WUV + (64 * wave + r0) * 128 + ((nb ^ sw) * 32) + pp * 8;
#pragma unroll
        for (int ks = 0; ks < 2; ++ks) {
          const bf16x8 b = tr_pair(base + (32 * ks) * 128, base + (32 * ks + 16) * 128);
          d = __builtin_amdgcn_mfma_f32_16x16x32_bf16(af[ks], b, d, 0, 0, 0);
        }
        if (lane < 16) { red2[(wave * 2 + 0) * 64 + 16 * nb + lane] = d[0] + d[1]; red2[(wave * 2 + 1) * 64 + 16 * nb + lane] = d[2] + d[3]; }
      }
    }
    DRAIN();
    BAR();
    if (t < 64) {
      float2 s = *(const float2*)(red2 + t * 2);
#pragma unroll
      for (int w = 1; w < 8; ++w) { const float2 v = *(const float2*)(red2 + w * 128 + t * 2); s.x += v.x; s.y += v.y; }
      *(u32*)(lds + L_SCR + 2048 + t * 4) = pack_bf16(s.x, s.y);
    }
    BAR();
  }
  STAMP(10);
  proj_mfma(lds, L_WO, wsr, WS_OUTB + j * 9216, tag, wave, lane);
  STAMP(11);
  if (j < 36) final_reduce(lds, wsr, WS_OUTB, tag, j, t, p.out, 576);
  STAMP(12);
  STAMP_FLUSH();
}

extern "C" void kernel_launch(void* const* d_in, const int* in_sizes, int n_in, void* d_out, int out_size, void* d_ws, size_t ws_size,
                              hipStream_t stream) {
  Params p;
  p.Ax = (const u16*)d_in[0]; p.Ak = (const u16*)d_in[1]; p.Av = (const u16*)d_in[2]; p.Awq = (const u16*)d_in[3]; p.Awo = (const u16*)d_in[4];
  p.Bx = (const u16*)d_in[5]; p.Bc = (const u16*)d_in[6]; p.Bwq = (const u16*)d_in[7]; p.Bwuk = (const u16*)d_in[8]; p.Bwuv = (const u16*)d_in[9];
  p.Bwo = (const u16*)d_in[10];
  p.out = (u16*)d_out; p.ws = (unsigned char*)d_ws;
  fused_kernel<<<dim3(NWG), dim3(NT), 0, stream>>>(p);
}
```

```cpp
#include <hip/hip_runtime.h>
#include <stdint.h>

typedef unsigned short u16;
typedef unsigned int u32;
typedef unsigned long long u64;
typedef u32 u32x4 __attribute__((ext_vector_type(4)));
typedef u32 u32x2 __attribute__((ext_vector_type(2)));
typedef float f32x4 __attribute__((ext_vector_type(4)));
typedef float f32x16 __attribute__((ext_vector_type(16)));
typedef __bf16 bf16x2 __attribute__((ext_vector_type(2)));
typedef __bf16 bf16x8 __attribute__((ext_vector_type(8)));
typedef short s16x4 __attribute__((ext_vector_type(4)));
typedef short s16x8 __attribute__((ext_vector_type(8)));
#define GAS __attribute__((address_space(1)))
#define LAS __attribute__((address_space(3)))

constexpr int NT = 512;
constexpr int NWG = 256;
constexpr int ASPLIT = 6;
constexpr float SCALE = 0.08838834764831845f;
constexpr float L2E = 1.4426950408889634f;
constexpr int SPIN_MAX = 300000;

constexpr u32 WS_EPOCH = 0;
constexpr u32 WS_B2FLAG = 256;
constexpr u32 WS_B3FLAG = WS_B2FLAG + 512;
constexpr u32 WS_QG = 4096;
constexpr u32 WS_QH = WS_QG + 65536;
constexpr u32 WS_QL = WS_QH + 65536;
constexpr u32 WS_BML = WS_QL + 65536;
constexpr u32 WS_OUTB = WS_BML + 16384;
constexpr u32 WS_OUTA = WS_OUTB + 589824;
constexpr u32 WS_APG = WS_OUTA + 589824;
constexpr u32 WS_BPART = WS_APG + 401408;
constexpr u32 WS_END = WS_BPART + 2097152;

constexpr int CSTR = 1040;
constexpr int L_C = 0;
constexpr int L_WUV = 0;
constexpr int L_QH = 65536;
constexpr int L_QL = 98816;
constexpr int L_OT = 65536;
constexpr int L_WO = 65536;
constexpr int L_CA = 139264;
constexpr int L_RED2 = 143360;
constexpr int L_XB = 133120;
constexpr int L_P = 149504;
constexpr int L_ST = 158208;
constexpr int L_SCR = 159744;
constexpr int L_MRG = 81920;
constexpr int LDS_BYTES = 163840;

struct Params {
  const u16* Ax; const u16* Ak; const u16* Av; const u16* Awq; const u16* Awo;
  const u16* Bx; const u16* Bc; const u16* Bwq; const u16* Bwuk; const u16* Bwuv; const u16* Bwo;
  u16* out; unsigned char* ws;
};

__device__ __forceinline__ float bf2f(u16 v) { return __uint_as_float(((u32)v) << 16); }
__device__ __forceinline__ float bflo(u32 v) { return __uint_as_float(v << 16); }
__device__ __forceinline__ float bfhi(u32 v) { return __uint_as_float(v & 0xffff0000u); }
__device__ __forceinline__ float bfr(float f) { return (float)(__bf16)f; }
__device__ __forceinline__ u32 pack_bf16(float lo, float hi) {
  bf16x2 v = {(__bf16)lo, (__bf16)hi};
  return __builtin_bit_cast(u32, v);
}
__device__ __forceinline__ float dot2(u32 a, u32 b, float c) {
  return __builtin_amdgcn_fdot2_f32_bf16(__builtin_bit_cast(bf16x2, a), __builtin_bit_cast(bf16x2, b), c, false);
}
template <int CTRL> __device__ __forceinline__ float dppf(float x) {
  return __builtin_bit_cast(float, __builtin_amdgcn_mov_dpp(__builtin_bit_cast(int, x), CTRL, 0xf, 0xf, true));
}
__device__ __forceinline__ float row16_sum(float x) {
  x += dppf<0x128>(x); x += dppf<0x124>(x); x += dppf<0x122>(x); x += dppf<0x121>(x);
  return x;
}
__device__ __forceinline__ float xsum16(float x) { auto r = __builtin_amdgcn_permlane16_swap(__float_as_uint(x), __float_as_uint(x), false, false); return __uint_as_float(r[0]) + __uint_as_float(r[1]); }
__device__ __forceinline__ float xsum32(float x) { auto r = __builtin_amdgcn_permlane32_swap(__float_as_uint(x), __float_as_uint(x), false, false); return __uint_as_float(r[0]) + __uint_as_float(r[1]); }
__device__ __forceinline__ float xmax16(float x) { auto r = __builtin_amdgcn_permlane16_swap(__float_as_uint(x), __float_as_uint(x), false, false); return fmaxf(__uint_as_float(r[0]), __uint_as_float(r[1])); }
__device__ __forceinline__ float xmax32(float x) { auto r = __builtin_amdgcn_permlane32_swap(__float_as_uint(x), __float_as_uint(x), false, false); return fmaxf(__uint_as_float(r[0]), __uint_as_float(r[1])); }
__device__ __forceinline__ float ex2(float x) { return __builtin_amdgcn_exp2f(x); }

typedef __amdgpu_buffer_rsrc_t rsrc_t;
__device__ __forceinline__ u32x4 ld128(rsrc_t r, u32 off) { return __builtin_amdgcn_raw_buffer_load_b128(r, off, 0, 16); }
__device__ __forceinline__ u32x2 ld64(rsrc_t r, u32 off) { return __builtin_amdgcn_raw_buffer_load_b64(r, off, 0, 16); }
__device__ __forceinline__ void st128(rsrc_t r, u32 off, u32x4 v) { __builtin_amdgcn_raw_buffer_store_b128(v, r, off, 0, 16); }
__device__ __forceinline__ void st64(rsrc_t r, u32 off, u32x2 v) { __builtin_amdgcn_raw_buffer_store_b64(v, r, off, 0, 16); }
__device__ __forceinline__ u32x4 ldnt(const u16* p) { return __builtin_nontemporal_load((const u32x4*)p); }

__device__ __forceinline__ void flag_set(unsigned char* ws, u32 off, u64 tag) {
  __hip_atomic_store((GAS u64*)(ws + off), tag, __ATOMIC_RELAXED, __HIP_MEMORY_SCOPE_AGENT);
}
__device__ __forceinline__ u64 flag_get(unsigned char* ws, u32 off) {
  return __hip_atomic_load((GAS u64*)(ws + off), __ATOMIC_RELAXED, __HIP_MEMORY_SCOPE_AGENT);
}
#define DRAIN() asm volatile("s_waitcnt vmcnt(0)" ::: "memory")
#define BAR() asm volatile("s_waitcnt lgkmcnt(0)\n\ts_barrier" ::: "memory")
#define LGKM0() asm volatile("s_waitcnt lgkmcnt(0)" ::: "memory")

__device__ __forceinline__ void glds16(const void* gsrc, u32 lds_dst) {
  u32 keep;
  asm volatile("s_mov_b32 %0, m0\n\ts_mov_b32 m0, %2\n\ts_nop 0\n\tglobal_load_lds_dwordx4 %1, off nt\n\ts_mov_b32 m0, %0"
               : "=&s"(keep) : "v"(gsrc), "s"(lds_dst) : "memory");
}

constexpr int CODE_BYTES = 40960;
__device__ __forceinline__ void code_prefetch(u64 pc0, int lane, u32 lds_dummy) {
#pragma unroll
  for (int i = 0; i < (CODE_BYTES + 8191) / 8192; ++i) {
    int off = i * 8192 + lane * 128; off = off < CODE_BYTES - 4 ? off : CODE_BYTES - 4;
    const unsigned char* g = (const unsigned char*)pc0 + off;
    u32 keep;
    asm volatile("s_mov_b32 %0, m0\n\ts_mov_b32 m0, %2\n\ts_nop 0\n\tglobal_load_lds_dword %1, off\n\ts_mov_b32 m0, %0"
                 : "=&s"(keep) : "v"(g), "s"(lds_dummy) : "memory");
  }
}
__device__ __forceinline__ void wait_flags(unsigned char* ws, u32 off, bool active, u64 tag) {
  for (int spin = 0; spin < SPIN_MAX; ++spin) {
    u64 v = tag;
    if (active) v = flag_get(ws, off);
    if (__all(v == tag)) break;
    __builtin_amdgcn_s_sleep(1);
  }
  __builtin_amdgcn_fence(__ATOMIC_ACQUIRE, "wavefront");
}
__device__ __forceinline__ void gst(rsrc_t r, u32 off, u32 d0, u32 d1, u64 tag) { st128(r, off, u32x4{d0, d1, (u32)tag, (u32)(tag >> 32)}); }
__device__ __forceinline__ bool gok(u32x4 g, u64 tag) { return g[2] == (u32)tag && g[3] == (u32)(tag >> 32); }
template <int N> __device__ __forceinline__ void gpoll(rsrc_t r, const u32 (&off)[N], u32x4 (&g)[N], bool need, u64 tag) {
  for (int spin = 0; spin < SPIN_MAX; ++spin) {
    bool ok = true;
#pragma unroll
    for (int i = 0; i < N; ++i) { g[i] = ld128(r, off[i]); ok = ok && gok(g[i], tag); }
    if (__all(ok || !need)) break;
    __builtin_amdgcn_s_sleep(1);
  }
}

#define STAMP(i) do {} while (0)
#define STAMP_FLUSH() do {} while (0)

__device__ __forceinline__ bf16x8 tr_pair(const unsigned char* a0, const unsigned char* a1) {
  const s16x4 t0 = __builtin_amdgcn_ds_read_tr16_b64_v4i16((LAS s16x4*)a0);
  const s16x4 t1 = __builtin_amdgcn_ds_read_tr16_b64_v4i16((LAS s16x4*)a1);
  return __builtin_bit_cast(bf16x8, __builtin_shufflevector(t0, t1, 0, 1, 2, 3, 4, 5, 6, 7));
}

__device__ __forceinline__ void proj_mfma(unsigned char* lds, int wbase, rsrc_t wsr, u32 out_off, u64 tag, int wave, int lane) {
  const int g4 = lane >> 4, i16 = lane & 15, qq = i16 >> 2, pp = i16 & 3;
  bf16x8 af[2];
#pragma unroll
  for (int ks = 0; ks < 2; ++ks) {
    u32x2 lo = {0u, 0u}, hi = {0u, 0u};
    if (i16 < 2) {
      lo = *(const u32x2*)(lds + L_SCR + 2048 + i16 * 128 + (32 * ks + 4 * g4) * 2);
      hi = *(const u32x2*)(lds + L_SCR + 2048 + i16 * 128 + (32 * ks + 16 + 4 * g4) * 2);
    }
    af[ks] = __builtin_bit_cast(bf16x8, u32x4{lo[0], lo[1], hi[0], hi[1]});
  }
  const int r0 = 4 * g4 + qq;
  const int sw = (r0 >> 1) & 3;
#pragma unroll
  for (int i = 0; i < 5; ++i) {
    const int nb = wave + 8 * i;
    if (nb < 36) {
      f32x4 d = {0.f, 0.f, 0.f, 0.f};
      const unsigned char* base = lds + wbase + r0 * 1152 + ((nb ^ sw) * 32) + pp * 8;
#pragma unroll
      for (int ks = 0; ks < 2; ++ks) {
        const bf16x8 b = tr_pair(base + (32 * ks) * 1152, base + (32 * ks + 16) * 1152);
        d = __builtin_amdgcn_mfma_f32_16x16x32_bf16(af[ks], b, d, 0, 0, 0);
      }
      if (lane < 16) gst(wsr, out_off + (16 * nb + lane) * 16, __float_as_uint(d[0]), __float_as_uint(d[1]), tag);
    }
  }
}
__device__ __forceinline__ void final_reduce(unsigned char* lds, rsrc_t wsr, u32 part_off, u64 tag, int f, int t, u16* out, int base) {
  const int wave = t >> 6, lane = t & 63;
  const int pidx = t >> 3, sub = t & 7;
  u32 off[2] = {part_off + (u32)(pidx * 576 + 16 * f + sub) * 16, part_off + (u32)(pidx * 576 + 16 * f + 8 + sub) * 16};
  u32x4 g[2];
  gpoll<2>(wsr, off, g, true, tag);
  float v[4] = {__uint_as_float(g[0][0]), __uint_as_float(g[0][1]), __uint_as_float(g[1][0]), __uint_as_float(g[1][1])};
#pragma unroll
  for (int k = 0; k < 4; ++k) { float x = v[k]; x += dppf<0x128>(x); x = xsum16(x); x = xsum32(x); v[k] = x; }
  float* red = (float*)(lds + L_SCR);
  if (lane < 8) *(f32x4*)(red + wave * 32 + lane * 4) = f32x4{v[0], v[1], v[2], v[3]};
  BAR();
  if (t < 32) {
    float s = 0.f;
#pragma unroll
    for (int w = 0; w < 8; ++w) s += red[w * 32 + t];
    const int sb = t >> 2, k = t & 3, bb = k & 1, col = sb + 8 * (k >> 1);
    __bf16 o = (__bf16)s;
    out[bb * 1152 + base + 16 * f + col] = __builtin_bit_cast(u16, o);
  }
}

template <int NL, int RS>
__device__ __forceinline__ void q_load(const u16* qW, const u16* qx, int qcol0, int t, u32x4 (&qw)[NL], u16 (&qx0)[NL], u16 (&qx1)[NL]) {
  const int cch = t & 3, row0 = t >> 2;
#pragma unroll
  for (int i = 0; i < NL; ++i) {
    const int row = row0 + RS * i, rc = row < 576 ? row : 575;
    qx0[i] = qx[rc]; qx1[i] = qx[576 + rc];
  }
#pragma unroll
  for (int i = 0; i < NL; ++i) {
    const int row = row0 + RS * i, rc = row < 576 ? row : 575;
    qw[i] = ldnt(qW + (size_t)rc * 4096 + qcol0 + cch * 8);
  }
}
template <int NL, int RS>
__device__ __forceinline__ void q_fma(int t, const u32x4 (&qw)[NL], const u16 (&qx0)[NL], const u16 (&qx1)[NL], float (&qa0)[8], float (&qa1)[8]) {
#pragma unroll
  for (int k = 0; k < 8; ++k) { qa0[k] = 0.f; qa1[k] = 0.f; }
#pragma unroll
  for (int i = 0; i < NL; ++i) {
    const bool live = (t >> 2) + RS * i < 576;
    const float x0 = live ? bf2f(qx0[i]) : 0.f, x1 = live ? bf2f(qx1[i]) : 0.f;
#pragma unroll
    for (int k = 0; k < 4; ++k) {
      const float lo = bflo(qw[i][k]), hi = bfhi(qw[i][k]);
      qa0[2 * k] = fmaf(x0, lo, qa0[2 * k]); qa0[2 * k + 1] = fmaf(x0, hi, qa0[2 * k + 1]);
      qa1[2 * k] = fmaf(x1, lo, qa1[2 * k]); qa1[2 * k + 1] = fmaf(x1, hi, qa1[2 * k + 1]);
    }
  }
}
__device__ __forceinline__ void q_reduce(unsigned char* lds, int wave, int lane, float (&qa0)[8], float (&qa1)[8]) {
#pragma unroll
  for (int k = 0; k < 8; ++k) {
    float x = qa0[k]; x += dppf<0x124>(x); x += dppf<0x128>(x); x = xsum16(x); x = xsum32(x); qa0[k] = x;
    float y = qa1[k]; y += dppf<0x124>(y); y += dppf<0x128>(y); y = xsum16(y); y = xsum32(y); qa1[k] = y;
  }
  float* red_ = (float*)(lds + L_SCR);
  if (lane < 4) {
    float* r0 = red_ + wave * 64 + lane * 8;
    *(f32x4*)r0 = f32x4{qa0[0], qa0[1], qa0[2], qa0[3]}; *(f32x4*)(r0 + 4) = f32x4{qa0[4], qa0[5], qa0[6], qa0[7]};
    *(f32x4*)(r0 + 32) = f32x4{qa1[0], qa1[1], qa1[2], qa1[3]}; *(f32x4*)(r0 + 36) = f32x4{qa1[4], qa1[5], qa1[6], qa1[7]};
  }
}
template <int NW>
__device__ __forceinline__ void q_publish(unsigned char* lds, rsrc_t wsr, int qmat, int qcol0, int t, u64 tag) {
  if (t < 16) {
    const float* red_ = (const float*)(lds + L_SCR);
    f32x4 s_ = *(const f32x4*)(red_ + t * 4);
#pragma unroll
    for (int w = 1; w < NW; ++w) s_ += *(const f32x4*)(red_ + w * 64 + t * 4);
    gst(wsr, WS_QG + ((qmat * 2 + (t >> 3)) * 1024 + (qcol0 >> 2) + (t & 7)) * 16, pack_bf16(s_[0], s_[1]), pack_bf16(s_[2], s_[3]), tag);
  }
}

__global__ void __launch_bounds__(NT, 2) fused_kernel(Params p) {
  __shared__ __attribute__((aligned(16))) unsigned char lds[LDS_BYTES];
  u64 pc0; asm volatile("s_getpc_b64 %0" : "=s"(pc0)); pc0 &= ~255ull;
  const int bid = blockIdx.x, t = threadIdx.x, lane = t & 63;
  const int wave = __builtin_amdgcn_readfirstlane(t >> 6);
  unsigned char* ws = p.ws;
  const rsrc_t wsr = __builtin_amdgcn_make_buffer_rsrc(ws, 0, WS_END, 0x00020000);
  const u32 ldsb = (u32)(uintptr_t)(LAS unsigned char*)lds;
  STAMP(0);
  u32 vz; asm volatile("v_mov_b32 %0, 0" : "=v"(vz));
  const u64 epoch = flag_get(ws + vz, WS_EPOCH);
  const bool isB = ((bid >> 3) & 3) == 3;
  const int bj = ((bid >> 5) << 3) | (bid & 7);
  const int ai = (bid >> 5) * 24 + ((bid >> 3) & 3) * 8 + (bid & 7);
  const int qi = ((bid >> 4) << 4) | ((bid & 7) << 1) | ((bid >> 3) & 1);
  const int qmat = qi >> 7, qcol0 = (qi & 127) * 32;
  const u16* qW = qmat ? p.Bwq : p.Awq;
  const u16* qx = qmat ? p.Bx : p.Ax;

  if (!isB) {
    float qa0[8], qa1[8];
    {
      u32x4 qw[5]; u16 qx0[5], qx1[5];
      q_load<5, 128>(qW, qx, qcol0, t, qw, qx0, qx1);
      if (wave == 7) code_prefetch(pc0, lane, ldsb + L_ST + 1024);
      __builtin_amdgcn_sched_barrier(0);
      q_fma<5, 128>(t, qw, qx0, qx1, qa0, qa1);
    }
    const u64 tag = epoch + 1ull;
    const int pair = ai / ASPLIT, split = ai % ASPLIT;
    const int b = pair >> 4, hkv = pair & 15;
    const int ustart = split * 42 + (split < 4 ? split : 4), ucnt = split < 4 ? 43 : 42;
    const int nw = (ucnt - wave + 7) >> 3;
    const u16* Kb = p.Ak + (size_t)pair * 4096 * 128 + (size_t)(ustart + wave) * 2048 + (lane >> 4) * 128 + (lane & 15) * 8;
    const u16* Vb = p.Av + (size_t)pair * 4096 * 128 + (size_t)(ustart + wave) * 2048 + (lane >> 4) * 128 + (lane & 15) * 8;
    u32x4 K0[4], V0[4], K1[4], V1[4], K2[4], V2[4];
#define A_LOAD(KK, VV, it) do { _Pragma("unroll") for (int i_ = 0; i_ < 4; ++i_) { KK[i_] = ldnt(Kb + (size_t)(it) * 16384 + i_ * 512); } \
                                _Pragma("unroll") for (int i_ = 0; i_ < 4; ++i_) { VV[i_] = ldnt(Vb + (size_t)(it) * 16384 + i_ * 512); } } while (0)
    q_reduce(lds, wave, lane, qa0, qa1);
    BAR();
    q_publish<8>(lds, wsr, qmat, qcol0, t, tag);
    STAMP(1);
    if (wave != 0) A_LOAD(K0, V0, 0);
    if (wave == 0) {
      u32 off[1] = {WS_QG + (u32)((0 * 2 + b) * 1024 + (2 * hkv) * 32 + lane) * 16};
      u32x4 g[1];
      gpoll<1>(wsr, off, g, true, tag);
      *(u32x2*)(lds + L_SCR + 2048 + lane * 8) = u32x2{g[0][0], g[0][1]};
    }
    BAR();
    const u32x4 q0 = *(const u32x4*)(lds + L_SCR + 2048 + (lane & 15) * 16), q1 = *(const u32x4*)(lds + L_SCR + 2048 + 256 + (lane & 15) * 16);
    if (wave == 0) A_LOAD(K0, V0, 0);
    STAMP(2);
    float m0 = -INFINITY, m1 = -INFINITY, l0 = 0.f, l1 = 0.f;
    float acc0[8], acc1[8];
#pragma unroll
    for (int k = 0; k < 8; ++k) { acc0[k] = 0.f; acc1[k] = 0.f; }
#define A_COMPUTE(KK, VV) do { \
      float s0_[4], s1_[4]; \
      _Pragma("unroll") for (int i_ = 0; i_ < 4; ++i_) { \
        float d0_ = 0.f, d1_ = 0.f; \
        _Pragma("unroll") for (int k_ = 0; k_ < 4; ++k_) { d0_ = dot2(KK[i_][k_], q0[k_], d0_); d1_ = dot2(KK[i_][k_], q1[k_], d1_); } \
        d0_ = row16_sum(d0_); d1_ = row16_sum(d1_); \
        s0_[i_] = bfr(bfr(d0_) * SCALE); s1_[i_] = bfr(bfr(d1_) * SCALE); } \
      const float mx0_ = fmaxf(fmaxf(s0_[0], s0_[1]), fmaxf(s0_[2], s0_[3])), mx1_ = fmaxf(fmaxf(s1_[0], s1_[1]), fmaxf(s1_[2], s1_[3])); \
      if (__any(mx0_ > m0 || mx1_ > m1)) { \
        const float n0_ = fmaxf(m0, mx0_), n1_ = fmaxf(m1, mx1_); \
        const float al0_ = ex2((m0 - n0_) * L2E), al1_ = ex2((m1 - n1_) * L2E); \
        l0 *= al0_; l1 *= al1_; m0 = n0_; m1 = n1_; \
        _Pragma("unroll") for (int k_ = 0; k_ < 8; ++k_) { acc0[k_] *= al0_; acc1[k_] *= al1_; } } \
      float p0_[4], p1_[4]; \
      _Pragma("unroll") for (int i_ = 0; i_ < 4; ++i_) { p0_[i_] = ex2((s0_[i_] - m0) * L2E); p1_[i_] = ex2((s1_[i_] - m1) * L2E); } \
      l0 += (p0_[0] + p0_[1]) + (p0_[2] + p0_[3]); l1 += (p1_[0] + p1_[1]) + (p1_[2] + p1_[3]); \
      const u32 pa0_ = pack_bf16(p0_[0], p0_[1]), pb0_ = pack_bf16(p0_[2], p0_[3]), pa1_ = pack_bf16(p1_[0], p1_[1]), pb1_ = pack_bf16(p1_[2], p1_[3]); \
      _Pragma("unroll") for (int k_ = 0; k_ < 4; ++k_) { \
        const u32 lo01_ = __builtin_amdgcn_perm(VV[1][k_], VV[0][k_], 0x05040100u), hi01_ = __builtin_amdgcn_perm(VV[1][k_], VV[0][k_], 0x07060302u); \
        const u32 lo23_ = __builtin_amdgcn_perm(VV[3][k_], VV[2][k_], 0x05040100u), hi23_ = __builtin_amdgcn_perm(VV[3][k_], VV[2][k_], 0x07060302u); \
        acc0[2 * k_] = dot2(lo23_, pb0_, dot2(lo01_, pa0_, acc0[2 * k_])); acc0[2 * k_ + 1] = dot2(hi23_, pb0_, dot2(hi01_, pa0_, acc0[2 * k_ + 1])); \
        acc1[2 * k_] = dot2(lo23_, pb1_, dot2(lo01_, pa1_, acc1[2 * k_])); acc1[2 * k_ + 1] = dot2(hi23_, pb1_, dot2(hi01_, pa1_, acc1[2 * k_ + 1])); } \
    } while (0)
    DRAIN(); A_LOAD(K1, V1, 1); A_COMPUTE(K0, V0);
    DRAIN(); A_LOAD(K0, V0, 2); A_COMPUTE(K1, V1);
    DRAIN(); A_LOAD(K1, V1, 3); A_COMPUTE(K0, V0);
    DRAIN(); A_LOAD(K0, V0, 4); A_COMPUTE(K1, V1);
#define WO_DMA() do { \
      if (ai < 64) { \
        const unsigned char* src = (const unsigned char*)(p.Awo + (size_t)ai * 64 * 576); \
        for (int i = 0; i < 9; ++i) { \
          const int L = i * 512 + t, r = L / 72, pc = L - r * 72; \
          const int c = (((pc >> 1) ^ ((r >> 1) & 3)) << 1) | (pc & 1); \
          glds16(src + r * 1152 + c * 16, ldsb + (u32)(i * 512 + wave * 64) * 16); \
        } \
      } \
    } while (0)
    if (nw == 6) {
      DRAIN(); A_LOAD(K1, V1, 5); A_COMPUTE(K0, V0);
      A_COMPUTE(K1, V1);
    } else {
      A_COMPUTE(K0, V0);
    }
    WO_DMA();
    STAMP(3);
    float* mrg = (float*)(lds + L_MRG);
    float* mml = (float*)(lds + L_ST);
    {
      const float M0 = xmax32(xmax16(m0)), M1 = xmax32(xmax16(m1));
      const float a0 = ex2((m0 - M0) * L2E), a1 = ex2((m1 - M1) * L2E);
      l0 = xsum32(xsum16(l0 * a0)); l1 = xsum32(xsum16(l1 * a1));
#pragma unroll
      for (int k = 0; k < 8; ++k) { acc0[k] = xsum32(xsum16(acc0[k] * a0)); acc1[k] = xsum32(xsum16(acc1[k] * a1)); }
      if (lane < 16) {
        float* r0 = mrg + (wave * 2 + 0) * 128 + lane * 8; float* r1 = mrg + (wave * 2 + 1) * 128 + lane * 8;
        *(f32x4*)r0 = f32x4{acc0[0], acc0[1], acc0[2], acc0[3]}; *(f32x4*)(r0 + 4) = f32x4{acc0[4], acc0[5], acc0[6], acc0[7]};
        *(f32x4*)r1 = f32x4{acc1[0], acc1[1], acc1[2], acc1[3]}; *(f32x4*)(r1 + 4) = f32x4{acc1[4], acc1[5], acc1[6], acc1[7]};
        if (lane == 0) *(f32x4*)(mml + wave * 4) = f32x4{M0, l0, M1, l1};
      }
    }
    BAR();
    if (t < 128) {
      const int g = t >> 6, k = t & 63;
      float M = -INFINITY;
#pragma unroll
      for (int r = 0; r < 8; ++r) M = fmaxf(M, mml[r * 4 + g * 2]);
      float L = 0.f, o0 = 0.f, o1 = 0.f;
#pragma unroll
      for (int r = 0; r < 8; ++r) {
        const float w = ex2((mml[r * 4 + g * 2] - M) * L2E); L = fmaf(w, mml[r * 4 + g * 2 + 1], L);
        const float2 v = *(const float2*)(mrg + (r * 2 + g) * 128 + 2 * k);
        o0 = fmaf(w, v.x, o0); o1 = fmaf(w, v.y, o1);
      }
      gst(wsr, WS_APG + ((ai * 2 + g) * 65 + k) * 16, __float_as_uint(o0), __float_as_uint(o1), tag);
      if (k == 0) gst(wsr, WS_APG + ((ai * 2 + g) * 65 + 64) * 16, __float_as_uint(M), __float_as_uint(L), tag);
    }
    STAMP(4);
    if (ai < 64) {
      const int hq = ai >> 1, dh = ai & 1, thkv = hq >> 1, g = hq & 1;
      if (wave == 0) {
        const int bb = lane >> 5, k = lane & 31;
        const int a0i = (bb * 16 + thkv) * 6;
        u32 off[12]; u32x4 gg[12];
#pragma unroll
        for (int s = 0; s < 6; ++s) { off[s] = WS_APG + (((a0i + s) * 2 + g) * 65 + 32 * dh + k) * 16; off[6 + s] = WS_APG + (((a0i + s) * 2 + g) * 65 + 64) * 16; }
        gpoll<12>(wsr, off, gg, true, tag);
        float M = __uint_as_float(gg[6][0]);
#pragma unroll
        for (int s = 1; s < 6; ++s) M = fmaxf(M, __uint_as_float(gg[6 + s][0]));
        float L = 0.f, o0 = 0.f, o1 = 0.f;
#pragma unroll
        for (int s = 0; s < 6; ++s) {
          const float w = ex2((__uint_as_float(gg[6 + s][0]) - M) * L2E);
          L = fmaf(w, __uint_as_float(gg[6 + s][1]), L); o0 = fmaf(w, __uint_as_float(gg[s][0]), o0); o1 = fmaf(w, __uint_as_float(gg[s][1]), o1);
        }
        *(u32*)(lds + L_SCR + 2048 + bb * 128 + k * 4) = pack_bf16(o0 / L, o1 / L);
      }
      DRAIN();
      BAR();
      STAMP(5);
      proj_mfma(lds, 0, wsr, WS_OUTA + ai * 9216, tag, wave, lane);
      STAMP(6);
    } else if (ai < 100) {
      final_reduce(lds, wsr, WS_OUTA, tag, ai - 64, t, p.out, 0);
      if (ai == 64) {
        if (wave == 0) { wait_flags(ws, WS_B3FLAG + lane * 8, true, tag); if (lane == 0) flag_set(ws, WS_EPOCH, tag); }
      }
      STAMP(7);
    }
    STAMP_FLUSH();
    return;
  }

  const int j = bj;
  const int hh = j >> 1, half = j & 1;
  const int b3b = j >> 5, b3ks = j & 31;
  const int kb = wave & 3, kh = wave >> 2;
  if (wave >= 4) {
    if (wave == 4) code_prefetch(pc0, lane, ldsb + L_ST + 1024);
    BAR();
    const unsigned char* src = (const unsigned char*)(p.Bc + ((size_t)b3b * 4096 + 128 * b3ks + 32 * (wave - 4)) * 512) + lane * 16;
#pragma unroll
    for (int r = 0; r < 32; ++r) glds16(src + r * 1024, ldsb + L_C + (u32)(32 * (wave - 4) + r) * CSTR);
    BAR();
    BAR();
    DRAIN();
  } else {
    u32x4 wk[16];
    float qa0[8], qa1[8];
    {
      u32x4 qw[9]; u16 qx0[9], qx1[9];
      q_load<9, 64>(qW, qx, qcol0, t, qw, qx0, qx1);
      __builtin_amdgcn_sched_barrier(0);
#pragma unroll
      for (int i = 0; i < 16; ++i) wk[i] = ldnt(p.Bwuk + ((size_t)(256 * half + (t >> 4) + 16 * i) * 32 + hh) * 128 + (t & 15) * 8);
      __builtin_amdgcn_sched_barrier(0);
      q_fma<9, 64>(t, qw, qx0, qx1, qa0, qa1);
    }
    q_reduce(lds, wave, lane, qa0, qa1);
    const u64 tag = epoch + 1ull;
    BAR();
    q_publish<4>(lds, wsr, qmat, qcol0, t, tag);
  STAMP(1);
    if (wave == 0) {
      u32 off[1] = {WS_QG + (u32)((1 * 2 + (lane >> 5)) * 1024 + hh * 32 + (lane & 31)) * 16};
      u32x4 g[1];
      gpoll<1>(wsr, off, g, true, tag);
      *(u32x2*)(lds + L_ST + lane * 8) = u32x2{g[0][0], g[0][1]};
    }
    BAR();
    const u32x4 qa = *(const u32x4*)(lds + L_ST + (t & 15) * 16), qb = *(const u32x4*)(lds + L_ST + 256 + (t & 15) * 16);
  STAMP(2);
    float* qs = (float*)(lds + L_SCR + 2048);
#pragma unroll
    for (int i = 0; i < 16; ++i) {
      float d0 = 0.f, d1 = 0.f;
#pragma unroll
      for (int k = 0; k < 4; ++k) { d0 = dot2(wk[i][k], qa[k], d0); d1 = dot2(wk[i][k], qb[k], d1); }
      d0 = row16_sum(d0); d1 = row16_sum(d1);
      if ((t & 15) == 0) { qs[(t >> 4) + 16 * i] = d0; qs[256 + (t >> 4) + 16 * i] = d1; }
    }
  STAMP(3);
    BAR();
    if (wave == 0) {
      const int bb = lane >> 5, c8 = lane & 31;
      u32x4 hi, lo;
#pragma unroll
      for (int k = 0; k < 4; ++k) {
        const float v0 = qs[bb * 256 + c8 * 8 + 2 * k], v1 = qs[bb * 256 + c8 * 8 + 2 * k + 1];
        const float h0 = bfr(v0), h1 = bfr(v1);
        hi[k] = pack_bf16(h0, h1); lo[k] = pack_bf16(v0 - h0, v1 - h1);
      }
      const u32 o = ((bb * 32 + hh) * 512 + 256 * half + c8 * 8) * 2;
      st128(wsr, WS_QH + o, hi); st128(wsr, WS_QL + o, lo);
      DRAIN();
      if (lane == 0) flag_set(ws, WS_B2FLAG + j * 8, tag);
    }
  STAMP(4);
  }
  BAR();
  STAMP(5);
  const u64 tag = epoch + 1ull;
  const int head = lane & 31, hi = lane >> 5;
  const int g4 = lane >> 4, i16 = lane & 15, qq = i16 >> 2, pp = i16 & 3;
  u32x4 cf[16];
  bf16x8 pv[2][8];
  {
    const int pi = 16 * (head >> 4) + 4 * (head & 3) + 2 * ((head >> 3) & 1) + ((head >> 2) & 1);
    const unsigned char* cr = lds + L_C + (32 * kb + pi) * CSTR + (256 * kh + 8 * hi) * 2;
#pragma unroll
    for (int s = 0; s < 16; ++s) cf[s] = *(const u32x4*)(cr + s * 32);
    const unsigned char* tb = lds + L_C + (4 * qq + 2 * hi) * CSTR + (64 * wave + 16 * (g4 & 1) + 4 * pp) * 2;
#pragma unroll
    for (int ks = 0; ks < 8; ++ks) {
      pv[0][ks] = tr_pair(tb + (16 * ks) * CSTR, tb + (16 * ks + 1) * CSTR);
      pv[1][ks] = tr_pair(tb + (16 * ks) * CSTR + 64, tb + (16 * ks + 1) * CSTR + 64);
    }
  }
  if (wave == 0) wait_flags(ws, WS_B2FLAG + lane * 8, true, tag);
  BAR();
  STAMP(6);
  if (wave >= 4) {
    const unsigned char* src = (const unsigned char*)(p.Bwuv + (size_t)hh * 128 + 64 * half);
#pragma unroll
    for (int i = 0; i < 16; ++i) {
      const int L = i * 256 + (t - 256), r = L >> 3, pc = L & 7;
      const int c = (((pc >> 1) ^ ((r >> 1) & 3)) << 1) | (pc & 1);
      glds16(src + (size_t)r * 8192 + c * 16, ldsb + L_WUV + (u32)(i * 256 + (wave - 4) * 64) * 16);
    }
  }
  if (wave < 4) {
    u32x4 vh[8], vl[8];
#pragma unroll
    for (int i = 0; i < 8; ++i) {
      const int idx = t + 256 * i;
      vh[i] = ld128(wsr, WS_QH + b3b * 32768 + idx * 16);
      vl[i] = ld128(wsr, WS_QL + b3b * 32768 + idx * 16);
    }
#pragma unroll
    for (int i = 0; i < 8; ++i) {
      const int idx = t + 256 * i;
      *(u32x4*)(lds + L_QH + (idx >> 6) * CSTR + (idx & 63) * 16) = vh[i];
      *(u32x4*)(lds + L_QL + (idx >> 6) * CSTR + (idx & 63) * 16) = vl[i];
    }
  }
  BAR();
  STAMP(7);
  u32x4 wo[9];
#pragma unroll
  for (int i = 0; i < 9; ++i) wo[i] = ldnt(p.Bwo + (size_t)j * 64 * 576 + (size_t)(i * 512 + t) * 8);
  {
    float* smax = (float*)(lds + L_ST);
    float* slsum = smax + 128;
    f32x16 sacc;
#pragma unroll
    for (int r = 0; r < 16; ++r) sacc[r] = 0.f;
    {
      const unsigned char* bhp = lds + L_QH + head * CSTR + (256 * kh + 8 * hi) * 2;
      const unsigned char* blp = lds + L_QL + head * CSTR + (256 * kh + 8 * hi) * 2;
#pragma unroll
      for (int s = 0; s < 16; ++s) {
        const bf16x8 bh = __builtin_bit_cast(bf16x8, *(const u32x4*)(bhp + s * 32));
        const bf16x8 bl = __builtin_bit_cast(bf16x8, *(const u32x4*)(blp + s * 32));
        sacc = __builtin_amdgcn_mfma_f32_32x32x16_bf16(__builtin_bit_cast(bf16x8, cf[s]), bh, sacc, 0, 0, 0);
        sacc = __builtin_amdgcn_mfma_f32_32x32x16_bf16(__builtin_bit_cast(bf16x8, cf[s]), bl, sacc, 0, 0, 0);
      }
    }
  STAMP(8);
    if (kh == 1) {
#pragma unroll
      for (int r4 = 0; r4 < 4; ++r4)
        *(f32x4*)(lds + L_XB + ((kb * 4 + r4) * 64 + lane) * 16) = f32x4{sacc[4 * r4], sacc[4 * r4 + 1], sacc[4 * r4 + 2], sacc[4 * r4 + 3]};
    }
    BAR();
    float sv[16];
    if (kh == 0) {
      float mx = -INFINITY;
#pragma unroll
      for (int r4 = 0; r4 < 4; ++r4) {
        const f32x4 o = *(const f32x4*)(lds + L_XB + ((kb * 4 + r4) * 64 + lane) * 16);
#pragma unroll
        for (int e = 0; e < 4; ++e) { sv[4 * r4 + e] = bfr(bfr(sacc[4 * r4 + e] + o[e]) * SCALE); mx = fmaxf(mx, sv[4 * r4 + e]); }
      }
      mx = xmax32(mx);
      if (lane < 32) smax[kb * 32 + head] = mx;
    }
    BAR();
    if (kh == 0) {
      const float m = fmaxf(fmaxf(smax[head], smax[32 + head]), fmaxf(smax[64 + head], smax[96 + head]));
      float ls = 0.f;
#pragma unroll
      for (int r4 = 0; r4 < 4; ++r4) {
        float pe[4];
#pragma unroll
        for (int e = 0; e < 4; ++e) { pe[e] = ex2((sv[4 * r4 + e] - m) * L2E); ls += pe[e]; }
        *(u32x2*)(lds + L_P + head * 272 + (32 * kb + 8 * r4 + 4 * hi) * 2) = u32x2{pack_bf16(pe[0], pe[1]), pack_bf16(pe[2], pe[3])};
      }
      ls = xsum32(ls);
      if (lane < 32) slsum[kb * 32 + head] = ls;
    }
    BAR();
  STAMP(9);
    f32x16 oacc0, oacc1;
#pragma unroll
    for (int r = 0; r < 16; ++r) { oacc0[r] = 0.f; oacc1[r] = 0.f; }
    {
      const unsigned char* pa = lds + L_P + head * 272 + hi * 16;
#pragma unroll
      for (int ks = 0; ks < 8; ++ks) {
        const bf16x8 a = __builtin_bit_cast(bf16x8, *(const u32x4*)(pa + ks * 32));
        oacc0 = __builtin_amdgcn_mfma_f32_32x32x16_bf16(pv[0][ks], a, oacc0, 0, 0, 0);
        oacc1 = __builtin_amdgcn_mfma_f32_32x32x16_bf16(pv[1][ks], a, oacc1, 0, 0, 0);
      }
    }
  STAMP(10);
    {
      unsigned char* ot = lds + L_OT + wave * 4608;
#pragma unroll
      for (int r4 = 0; r4 < 4; ++r4) {
        *(u32x2*)(ot + head * 144 + (8 * r4 + 4 * hi) * 2) = u32x2{pack_bf16(oacc0[4 * r4], oacc0[4 * r4 + 1]), pack_bf16(oacc0[4 * r4 + 2], oacc0[4 * r4 + 3])};
        *(u32x2*)(ot + head * 144 + (32 + 8 * r4 + 4 * hi) * 2) = u32x2{pack_bf16(oacc1[4 * r4], oacc1[4 * r4 + 1]), pack_bf16(oacc1[4 * r4 + 2], oacc1[4 * r4 + 3])};
      }
      LGKM0();
#pragma unroll
      for (int i = 0; i < 4; ++i) {
        const int hrow = (lane >> 3) + 8 * i, ch = lane & 7;
        const u32x4 v = *(const u32x4*)(ot + hrow * 144 + ch * 16);
        st128(wsr, WS_BPART + (((b3b * 32 + b3ks) * 32 + hrow) * 512 + 64 * wave + 8 * ch) * 2, v);
      }
    }
    if (t < 32) {
      const float L = (slsum[t] + slsum[32 + t]) + (slsum[64 + t] + slsum[96 + t]);
      const float m = fmaxf(fmaxf(smax[t], smax[32 + t]), fmaxf(smax[64 + t], smax[96 + t]));
      st64(wsr, WS_BML + ((b3b * 32 + b3ks) * 32 + t) * 8, u32x2{__float_as_uint(m), __float_as_uint(L)});
    }
    DRAIN();
    BAR();
    if (t == 0) flag_set(ws, WS_B3FLAG + j * 8, tag);
  STAMP(11);
  }
  {
#pragma unroll
    for (int i = 0; i < 9; ++i) {
      const int L = i * 512 + t, r = L / 72, c = L - r * 72;
      const int pc = (((c >> 1) ^ ((r >> 1) & 3)) << 1) | (c & 1);
      *(u32x4*)(lds + L_WO + (r * 72 + pc) * 16) = wo[i];
    }
  }
  {
    if (wave == 0) wait_flags(ws, WS_B3FLAG + lane * 8, true, tag);
    BAR();
  STAMP(12);
    const int rg = lane >> 3, ch = lane & 7;
    u32x4 g[8];
#pragma unroll
    for (int i = 0; i < 8; ++i) g[i] = ld128(wsr, WS_BPART + (((rg + 8 * i) * 32 + hh) * 512 + 64 * wave + 8 * ch) * 2);
    float wgt;
    {
      const u32x2 ml = ld64(wsr, WS_BML + (lane * 32 + hh) * 8);
      const float m = __uint_as_float(ml[0]), l = __uint_as_float(ml[1]);
      float M = m;
      M = fmaxf(M, dppf<0x128>(M)); M = fmaxf(M, dppf<0x124>(M)); M = fmaxf(M, dppf<0x122>(M)); M = fmaxf(M, dppf<0x121>(M)); M = xmax16(M);
      const float w = ex2((m - M) * L2E);
      const float L = xsum16(row16_sum(w * l));
      wgt = w / L;
    }
    float s0[8], s1[8];
#pragma unroll
    for (int k = 0; k < 8; ++k) { s0[k] = 0.f; s1[k] = 0.f; }
#pragma unroll
    for (int i = 0; i < 4; ++i) {
      const float w0 = __shfl(wgt, rg + 8 * i), w1 = __shfl(wgt, 32 + rg + 8 * i);
#pragma unroll
      for (int k = 0; k < 4; ++k) {
        s0[2 * k] = fmaf(w0, bflo(g[i][k]), s0[2 * k]); s0[2 * k + 1] = fmaf(w0, bfhi(g[i][k]), s0[2 * k + 1]);
        s1[2 * k] = fmaf(w1, bflo(g[i + 4][k]), s1[2 * k]); s1[2 * k + 1] = fmaf(w1, bfhi(g[i + 4][k]), s1[2 * k + 1]);
      }
    }
#pragma unroll
    for (int k = 0; k < 8; ++k) {
      float x = s0[k]; x += dppf<0x128>(x); x = xsum16(x); x = xsum32(x); s0[k] = x;
      float y = s1[k]; y += dppf<0x128>(y); y = xsum16(y); y = xsum32(y); s1[k] = y;
    }
  STAMP(13);
    unsigned char* ca = lds + L_CA + wave * 512;
    if (lane < 8) {
      u32x4 h0, l0, h1, l1;
#pragma unroll
      for (int k = 0; k < 4; ++k) {
        const float a = bfr(s0[2 * k]), bq = bfr(s0[2 * k + 1]), c = bfr(s1[2 * k]), d = bfr(s1[2 * k + 1]);
        h0[k] = pack_bf16(a, bq); l0[k] = pack_bf16(s0[2 * k] - a, s0[2 * k + 1] - bq);
        h1[k] = pack_bf16(c, d); l1[k] = pack_bf16(s1[2 * k] - c, s1[2 * k + 1] - d);
      }
      *(u32x4*)(ca + 0 * 128 + lane * 16) = h0; *(u32x4*)(ca + 1 * 128 + lane * 16) = l0;
      *(u32x4*)(ca + 2 * 128 + lane * 16) = h1; *(u32x4*)(ca + 3 * 128 + lane * 16) = l1;
    }
    LGKM0();
    bf16x8 af[2];
#pragma unroll
    for (int ks = 0; ks < 2; ++ks) {
      u32x2 lo = {0u, 0u}, hq = {0u, 0u};
      if (i16 < 4) {
        lo = *(const u32x2*)(ca + i16 * 128 + (32 * ks + 4 * g4) * 2);
        hq = *(const u32x2*)(ca + i16 * 128 + (32 * ks + 16 + 4 * g4) * 2);
      }
      af[ks] = __builtin_bit_cast(bf16x8, u32x4{lo[0], lo[1], hq[0], hq[1]});
    }
    float* red2 = (float*)(lds + L_RED2);
    {
      const int r0 = 4 * g4 + qq, sw = (r0 >> 1) & 3;
#pragma unroll
      for (int nb = 0; nb < 4; ++nb) {
        f32x4 d = {0.f, 0.f, 0.f, 0.f};
        const unsigned char* base = lds + L_WUV + (64 * wave + r0) * 128 + ((nb ^ sw) * 32) + pp * 8;
#pragma unroll
        for (int ks = 0; ks < 2; ++ks) {
          const bf16x8 b = tr_pair(base + (32 * ks) * 128, base + (32 * ks + 16) * 128);
          d = __builtin_amdgcn_mfma_f32_16x16x32_bf16(af[ks], b, d, 0, 0, 0);
        }
        if (lane < 16) { red2[(wave * 2 + 0) * 64 + 16 * nb + lane] = d[0] + d[1]; red2[(wave * 2 + 1) * 64 + 16 * nb + lane] = d[2] + d[3]; }
      }
    }
    DRAIN();
    BAR();
    if (t < 64) {
      float2 s = *(const float2*)(red2 + t * 2);
#pragma unroll
      for (int w = 1; w < 8; ++w) { const float2 v = *(const float2*)(red2 + w * 128 + t * 2); s.x += v.x; s.y += v.y; }
      *(u32*)(lds + L_SCR + 2048 + t * 4) = pack_bf16(s.x, s.y);
    }
    BAR();
  }
  STAMP(14);
  proj_mfma(lds, L_WO, wsr, WS_OUTB + j * 9216, tag, wave, lane);
  if (j < 36) final_reduce(lds, wsr, WS_OUTB, tag, j, t, p.out, 576);
  STAMP(15);
  STAMP_FLUSH();
}

extern "C" void kernel_launch(void* const* d_in, const int* in_sizes, int n_in, void* d_out, int out_size, void* d_ws, size_t ws_size,
                              hipStream_t stream) {
  Params p;
  p.Ax = (const u16*)d_in[0]; p.Ak = (const u16*)d_in[1]; p.Av = (const u16*)d_in[2]; p.Awq = (const u16*)d_in[3]; p.Awo = (const u16*)d_in[4];
  p.Bx = (const u16*)d_in[5]; p.Bc = (const u16*)d_in[6]; p.Bwq = (const u16*)d_in[7]; p.Bwuk = (const u16*)d_in[8]; p.Bwuv = (const u16*)d_in[9];
  p.Bwo = (const u16*)d_in[10];
  p.out = (u16*)d_out; p.ws = (unsigned char*)d_ws;
  fused_kernel<<<dim3(NWG), dim3(NT), 0, stream>>>(p);
}
```

```cpp
#define PROBE_A 99
#define PROBE_B 99
#define PROBE_MUL 0
#include <hip/hip_runtime.h>
#include <stdint.h>

typedef unsigned short u16;
typedef unsigned int u32;
typedef unsigned long long u64;
typedef u32 u32x4 __attribute__((ext_vector_type(4)));
typedef u32 u32x2 __attribute__((ext_vector_type(2)));
typedef float f32x4 __attribute__((ext_vector_type(4)));
typedef float f32x16 __attribute__((ext_vector_type(16)));
typedef __bf16 bf16x2 __attribute__((ext_vector_type(2)));
typedef __bf16 bf16x8 __attribute__((ext_vector_type(8)));
typedef short s16x4 __attribute__((ext_vector_type(4)));
typedef short s16x8 __attribute__((ext_vector_type(8)));
#define GAS __attribute__((address_space(1)))
#define LAS __attribute__((address_space(3)))

constexpr int NT = 512;
constexpr int NWG = 256;
constexpr int ASPLIT = 6;
constexpr float SCALE = 0.08838834764831845f;
constexpr float L2E = 1.4426950408889634f;
constexpr int SPIN_MAX = 300000;

constexpr u32 WS_EPOCH = 0;
constexpr u32 WS_B2FLAG = 256;
constexpr u32 WS_B3FLAG = WS_B2FLAG + 512;
constexpr u32 WS_QG = 4096;
constexpr u32 WS_QH = WS_QG + 65536;
constexpr u32 WS_QL = WS_QH + 65536;
constexpr u32 WS_BML = WS_QL + 65536;
constexpr u32 WS_OUTB = WS_BML + 16384;
constexpr u32 WS_OUTA = WS_OUTB + 589824;
constexpr u32 WS_APG = WS_OUTA + 589824;
constexpr u32 WS_BPART = WS_APG + 401408;
constexpr u32 WS_PROBE = WS_BPART + 2097152;
constexpr u32 WS_END = WS_PROBE + 4096;

constexpr int CSTR = 1040;
constexpr int L_C = 0;
constexpr int L_WUV = 0;
constexpr int L_QH = 65536;
constexpr int L_QL = 98816;
constexpr int L_OT = 65536;
constexpr int L_WO = 65536;
constexpr int L_CA = 139264;
constexpr int L_RED2 = 143360;
constexpr int L_XB = 133120;
constexpr int L_P = 149504;
constexpr int L_ST = 158208;
constexpr int L_SCR = 159744;
constexpr int L_MRG = 81920;
constexpr int LDS_BYTES = 163840;

struct Params {
  const u16* Ax; const u16* Ak; const u16* Av; const u16* Awq; const u16* Awo;
  const u16* Bx; const u16* Bc; const u16* Bwq; const u16* Bwuk; const u16* Bwuv; const u16* Bwo;
  u16* out; unsigned char* ws;
};

__device__ __forceinline__ float bf2f(u16 v) { return __uint_as_float(((u32)v) << 16); }
__device__ __forceinline__ float bflo(u32 v) { return __uint_as_float(v << 16); }
__device__ __forceinline__ float bfhi(u32 v) { return __uint_as_float(v & 0xffff0000u); }
__device__ __forceinline__ float bfr(float f) { return (float)(__bf16)f; }
__device__ __forceinline__ u32 pack_bf16(float lo, float hi) {
  bf16x2 v = {(__bf16)lo, (__bf16)hi};
  return __builtin_bit_cast(u32, v);
}
__device__ __forceinline__ float dot2(u32 a, u32 b, float c) {
  return __builtin_amdgcn_fdot2_f32_bf16(__builtin_bit_cast(bf16x2, a), __builtin_bit_cast(bf16x2, b), c, false);
}
template <int CTRL> __device__ __forceinline__ float dppf(float x) {
  return __builtin_bit_cast(float, __builtin_amdgcn_mov_dpp(__builtin_bit_cast(int, x), CTRL, 0xf, 0xf, true));
}
__device__ __forceinline__ float row16_sum(float x) {
  x += dppf<0x128>(x); x += dppf<0x124>(x); x += dppf<0x122>(x); x += dppf<0x121>(x);
  return x;
}
__device__ __forceinline__ float xsum16(float x) { auto r = __builtin_amdgcn_permlane16_swap(__float_as_uint(x), __float_as_uint(x), false, false); return __uint_as_float(r[0]) + __uint_as_float(r[1]); }
__device__ __forceinline__ float xsum32(float x) { auto r = __builtin_amdgcn_permlane32_swap(__float_as_uint(x), __float_as_uint(x), false, false); return __uint_as_float(r[0]) + __uint_as_float(r[1]); }
__device__ __forceinline__ float xmax16(float x) { auto r = __builtin_amdgcn_permlane16_swap(__float_as_uint(x), __float_as_uint(x), false, false); return fmaxf(__uint_as_float(r[0]), __uint_as_float(r[1])); }
__device__ __forceinline__ float xmax32(float x) { auto r = __builtin_amdgcn_permlane32_swap(__float_as_uint(x), __float_as_uint(x), false, false); return fmaxf(__uint_as_float(r[0]), __uint_as_float(r[1])); }
__device__ __forceinline__ float ex2(float x) { return __builtin_amdgcn_exp2f(x); }

typedef __amdgpu_buffer_rsrc_t rsrc_t;
__device__ __forceinline__ u32x4 ld128(rsrc_t r, u32 off) { return __builtin_amdgcn_raw_buffer_load_b128(r, off, 0, 16); }
__device__ __forceinline__ u32x2 ld64(rsrc_t r, u32 off) { return __builtin_amdgcn_raw_buffer_load_b64(r, off, 0, 16); }
__device__ __forceinline__ void st128(rsrc_t r, u32 off, u32x4 v) { __builtin_amdgcn_raw_buffer_store_b128(v, r, off, 0, 16); }
__device__ __forceinline__ void st64(rsrc_t r, u32 off, u32x2 v) { __builtin_amdgcn_raw_buffer_store_b64(v, r, off, 0, 16); }
__device__ __forceinline__ u32x4 ldnt(const u16* p) { return __builtin_nontemporal_load((const u32x4*)p); }

__device__ __forceinline__ void flag_set(unsigned char* ws, u32 off, u64 tag) {
  __hip_atomic_store((GAS u64*)(ws + off), tag, __ATOMIC_RELAXED, __HIP_MEMORY_SCOPE_AGENT);
}
__device__ __forceinline__ u64 flag_get(unsigned char* ws, u32 off) {
  return __hip_atomic_load((GAS u64*)(ws + off), __ATOMIC_RELAXED, __HIP_MEMORY_SCOPE_AGENT);
}
#define DRAIN() asm volatile("s_waitcnt vmcnt(0)" ::: "memory")
#define BAR() asm volatile("s_waitcnt lgkmcnt(0)\n\ts_barrier" ::: "memory")
#define LGKM0() asm volatile("s_waitcnt lgkmcnt(0)" ::: "memory")

__device__ __forceinline__ void glds16(const void* gsrc, u32 lds_dst) {
  u32 keep;
  asm volatile("s_mov_b32 %0, m0\n\ts_mov_b32 m0, %2\n\ts_nop 0\n\tglobal_load_lds_dwordx4 %1, off nt\n\ts_mov_b32 m0, %0"
               : "=&s"(keep) : "v"(gsrc), "s"(lds_dst) : "memory");
}

constexpr int CODE_BYTES = 40960;
__device__ __forceinline__ void code_prefetch(u64 pc0, int lane, u32 lds_dummy) {
#pragma unroll
  for (int i = 0; i < (CODE_BYTES + 8191) / 8192; ++i) {
    int off = i * 8192 + lane * 128; off = off < CODE_BYTES - 4 ? off : CODE_BYTES - 4;
    const unsigned char* g = (const unsigned char*)pc0 + off;
    u32 keep;
    asm volatile("s_mov_b32 %0, m0\n\ts_mov_b32 m0, %2\n\ts_nop 0\n\tglobal_load_lds_dword %1, off\n\ts_mov_b32 m0, %0"
                 : "=&s"(keep) : "v"(g), "s"(lds_dummy) : "memory");
  }
}
__device__ __forceinline__ void wait_flags(unsigned char* ws, u32 off, bool active, u64 tag) {
  for (int spin = 0; spin < SPIN_MAX; ++spin) {
    u64 v = tag;
    if (active) v = flag_get(ws, off);
    if (__all(v == tag)) break;
    __builtin_amdgcn_s_sleep(1);
  }
  __builtin_amdgcn_fence(__ATOMIC_ACQUIRE, "wavefront");
}
__device__ __forceinline__ void gst(rsrc_t r, u32 off, u32 d0, u32 d1, u64 tag) { st128(r, off, u32x4{d0, d1, (u32)tag, (u32)(tag >> 32)}); }
__device__ __forceinline__ bool gok(u32x4 g, u64 tag) { return g[2] == (u32)tag && g[3] == (u32)(tag >> 32); }
template <int N> __device__ __forceinline__ void gpoll(rsrc_t r, const u32 (&off)[N], u32x4 (&g)[N], bool need, u64 tag) {
  for (int spin = 0; spin < SPIN_MAX; ++spin) {
    bool ok = true;
#pragma unroll
    for (int i = 0; i < N; ++i) { g[i] = ld128(r, off[i]); ok = ok && gok(g[i], tag); }
    if (__all(ok || !need)) break;
    __builtin_amdgcn_s_sleep(1);
  }
}

#define STAMPS
#define STAMP(i) do { if ((threadIdx.x & 63) == 0) { stamps[(i)] = __builtin_amdgcn_s_memrealtime(); } } while (0)
#define STAMP_FLUSH() do { \
  const u64 tg_ = epoch + 1ull; \
  u64 sel_ = isB ? stamps[PROBE_B] : stamps[PROBE_A]; if (PROBE_A == 99) sel_ = __builtin_amdgcn_s_memrealtime(); if (sel_ == 0) sel_ = stamps[0]; \
  if (threadIdx.x == 0) gst(wsr, WS_PROBE + blockIdx.x * 16, (u32)stamps[0], (u32)sel_, tg_); \
  if (blockIdx.x == 0 && threadIdx.x < 64) { \
    u32 off_[4] = {WS_PROBE + lane * 16u, WS_PROBE + (64u + lane) * 16u, WS_PROBE + (128u + lane) * 16u, WS_PROBE + (192u + lane) * 16u}; u32x4 g_[4]; \
    gpoll<4>(wsr, off_, g_, true, tg_); \
    const u32 ref_ = __builtin_amdgcn_readfirstlane(g_[0][0]); \
    int mn_ = 0x7fffffff, mx_ = -0x7fffffff; \
    for (int i_ = 0; i_ < 4; ++i_) { mn_ = min(mn_, (int)(g_[i_][0] - ref_)); mx_ = max(mx_, (int)(g_[i_][1] - ref_)); } \
    for (int o_ = 32; o_ >= 1; o_ >>= 1) { mn_ = min(mn_, __shfl_xor(mn_, o_)); mx_ = max(mx_, __shfl_xor(mx_, o_)); } \
    int d_ = (mx_ - mn_) * PROBE_MUL; if (d_ > 20000) d_ = 20000; \
    const u64 s0_ = __builtin_amdgcn_s_memrealtime(); \
    while ((long long)(__builtin_amdgcn_s_memrealtime() - s0_) < (long long)d_) __builtin_amdgcn_s_sleep(1); \
  } } while (0)

__device__ __forceinline__ bf16x8 tr_pair(const unsigned char* a0, const unsigned char* a1) {
  const s16x4 t0 = __builtin_amdgcn_ds_read_tr16_b64_v4i16((LAS s16x4*)a0);
  const s16x4 t1 = __builtin_amdgcn_ds_read_tr16_b64_v4i16((LAS s16x4*)a1);
  return __builtin_bit_cast(bf16x8, __builtin_shufflevector(t0, t1, 0, 1, 2, 3, 4, 5, 6, 7));
}

__device__ __forceinline__ void proj_mfma(unsigned char* lds, int wbase, rsrc_t wsr, u32 out_off, u64 tag, int wave, int lane) {
  const int g4 = lane >> 4, i16 = lane & 15, qq = i16 >> 2, pp = i16 & 3;
  bf16x8 af[2];
#pragma unroll
  for (int ks = 0; ks < 2; ++ks) {
    u32x2 lo = {0u, 0u}, hi = {0u, 0u};
    if (i16 < 2) {
      lo = *(const u32x2*)(lds + L_SCR + 2048 + i16 * 128 + (32 * ks + 4 * g4) * 2);
      hi = *(const u32x2*)(lds + L_SCR + 2048 + i16 * 128 + (32 * ks + 16 + 4 * g4) * 2);
    }
    af[ks] = __builtin_bit_cast(bf16x8, u32x4{lo[0], lo[1], hi[0], hi[1]});
  }
  const int r0 = 4 * g4 + qq;
  const int sw = (r0 >> 1) & 3;
#pragma unroll
  for (int i = 0; i < 5; ++i) {
    const int nb = wave + 8 * i;
    if (nb < 36) {
      f32x4 d = {0.f, 0.f, 0.f, 0.f};
      const unsigned char* base = lds + wbase + r0 * 1152 + ((nb ^ sw) * 32) + pp * 8;
#pragma unroll
      for (int ks = 0; ks < 2; ++ks) {
        const bf16x8 b = tr_pair(base + (32 * ks) * 1152, base + (32 * ks + 16) * 1152);
        d = __builtin_amdgcn_mfma_f32_16x16x32_bf16(af[ks], b, d, 0, 0, 0);
      }
      if (lane < 16) gst(wsr, out_off + (16 * nb + lane) * 16, __float_as_uint(d[0]), __float_as_uint(d[1]), tag);
    }
  }
}
__device__ __forceinline__ void final_reduce(unsigned char* lds, rsrc_t wsr, u32 part_off, u64 tag, int f, int t, u16* out, int base) {
  const int wave = t >> 6, lane = t & 63;
  const int pidx = t >> 3, sub = t & 7;
  u32 off[2] = {part_off + (u32)(pidx * 576 + 16 * f + sub) * 16, part_off + (u32)(pidx * 576 + 16 * f + 8 + sub) * 16};
  u32x4 g[2];
  gpoll<2>(wsr, off, g, true, tag);
  float v[4] = {__uint_as_float(g[0][0]), __uint_as_float(g[0][1]), __uint_as_float(g[1][0]), __uint_as_float(g[1][1])};
#pragma unroll
  for (int k = 0; k < 4; ++k) { float x = v[k]; x += dppf<0x128>(x); x = xsum16(x); x = xsum32(x); v[k] = x; }
  float* red = (float*)(lds + L_SCR);
  if (lane < 8) *(f32x4*)(red + wave * 32 + lane * 4) = f32x4{v[0], v[1], v[2], v[3]};
  BAR();
  if (t < 32) {
    float s = 0.f;
#pragma unroll
    for (int w = 0; w < 8; ++w) s += red[w * 32 + t];
    const int sb = t >> 2, k = t & 3, bb = k & 1, col = sb + 8 * (k >> 1);
    __bf16 o = (__bf16)s;
    out[bb * 1152 + base + 16 * f + col] = __builtin_bit_cast(u16, o);
  }
}

template <int NL, int RS>
__device__ __forceinline__ void q_load(const u16* qW, const u16* qx, int qcol0, int t, u32x4 (&qw)[NL], u16 (&qx0)[NL], u16 (&qx1)[NL]) {
  const int cch = t & 3, row0 = t >> 2;
#pragma unroll
  for (int i = 0; i < NL; ++i) {
    const int row = row0 + RS * i, rc = row < 576 ? row : 575;
    qx0[i] = qx[rc]; qx1[i] = qx[576 + rc];
  }
#pragma unroll
  for (int i = 0; i < NL; ++i) {
    const int row = row0 + RS * i, rc = row < 576 ? row : 575;
    qw[i] = ldnt(qW + (size_t)rc * 4096 + qcol0 + cch * 8);
  }
}
template <int NL, int RS>
__device__ __forceinline__ void q_fma(int t, const u32x4 (&qw)[NL], const u16 (&qx0)[NL], const u16 (&qx1)[NL], float (&qa0)[8], float (&qa1)[8]) {
#pragma unroll
  for (int k = 0; k < 8; ++k) { qa0[k] = 0.f; qa1[k] = 0.f; }
#pragma unroll
  for (int i = 0; i < NL; ++i) {
    const bool live = (t >> 2) + RS * i < 576;
    const float x0 = live ? bf2f(qx0[i]) : 0.f, x1 = live ? bf2f(qx1[i]) : 0.f;
#pragma unroll
    for (int k = 0; k < 4; ++k) {
      const float lo = bflo(qw[i][k]), hi = bfhi(qw[i][k]);
      qa0[2 * k] = fmaf(x0, lo, qa0[2 * k]); qa0[2 * k + 1] = fmaf(x0, hi, qa0[2 * k + 1]);
      qa1[2 * k] = fmaf(x1, lo, qa1[2 * k]); qa1[2 * k + 1] = fmaf(x1, hi, qa1[2 * k + 1]);
    }
  }
}
__device__ __forceinline__ void q_reduce(unsigned char* lds, int wave, int lane, float (&qa0)[8], float (&qa1)[8]) {
#pragma unroll
  for (int k = 0; k < 8; ++k) {
    float x = qa0[k]; x += dppf<0x124>(x); x += dppf<0x128>(x); x = xsum16(x); x = xsum32(x); qa0[k] = x;
    float y = qa1[k]; y += dppf<0x124>(y); y += dppf<0x128>(y); y = xsum16(y); y = xsum32(y); qa1[k] = y;
  }
  float* red_ = (float*)(lds + L_SCR);
  if (lane < 4) {
    float* r0 = red_ + wave * 64 + lane * 8;
    *(f32x4*)r0 = f32x4{qa0[0], qa0[1], qa0[2], qa0[3]}; *(f32x4*)(r0 + 4) = f32x4{qa0[4], qa0[5], qa0[6], qa0[7]};
    *(f32x4*)(r0 + 32) = f32x4{qa1[0], qa1[1], qa1[2], qa1[3]}; *(f32x4*)(r0 + 36) = f32x4{qa1[4], qa1[5], qa1[6], qa1[7]};
  }
}
template <int NW>
__device__ __forceinline__ void q_publish(unsigned char* lds, rsrc_t wsr, int qmat, int qcol0, int t, u64 tag) {
  if (t < 16) {
    const float* red_ = (const float*)(lds + L_SCR);
    f32x4 s_ = *(const f32x4*)(red_ + t * 4);
#pragma unroll
    for (int w = 1; w < NW; ++w) s_ += *(const f32x4*)(red_ + w * 64 + t * 4);
    gst(wsr, WS_QG + ((qmat * 2 + (t >> 3)) * 1024 + (qcol0 >> 2) + (t & 7)) * 16, pack_bf16(s_[0], s_[1]), pack_bf16(s_[2], s_[3]), tag);
  }
}

__global__ void __launch_bounds__(NT, 2) fused_kernel(Params p) {
  __shared__ __attribute__((aligned(16))) unsigned char lds[LDS_BYTES];
  u64 pc0; asm volatile("s_getpc_b64 %0" : "=s"(pc0)); pc0 &= ~255ull;
  const int bid = blockIdx.x, t = threadIdx.x, lane = t & 63;
  const int wave = __builtin_amdgcn_readfirstlane(t >> 6);
  unsigned char* ws = p.ws;
  const rsrc_t wsr = __builtin_amdgcn_make_buffer_rsrc(ws, 0, WS_END, 0x00020000);
  const u32 ldsb = (u32)(uintptr_t)(LAS unsigned char*)lds;
  u64 stamps[100];
#pragma unroll
  for (int i = 0; i < 100; ++i) stamps[i] = 0;
  STAMP(0);
  u32 vz; asm volatile("v_mov_b32 %0, 0" : "=v"(vz));
  const u64 epoch = flag_get(ws + vz, WS_EPOCH);
  const bool isB = ((bid >> 3) & 3) == 3;
  const int bj = ((bid >> 5) << 3) | (bid & 7);
  const int ai = (bid >> 5) * 24 + ((bid >> 3) & 3) * 8 + (bid & 7);
  const int qi = ((bid >> 4) << 4) | ((bid & 7) << 1) | ((bid >> 3) & 1);
  const int qmat = qi >> 7, qcol0 = (qi & 127) * 32;
  const u16* qW = qmat ? p.Bwq : p.Awq;
  const u16* qx = qmat ? p.Bx : p.Ax;

  if (!isB) {
    float qa0[8], qa1[8];
    {
      u32x4 qw[5]; u16 qx0[5], qx1[5];
      q_load<5, 128>(qW, qx, qcol0, t, qw, qx0, qx1);
      if (wave == 7) code_prefetch(pc0, lane, ldsb + L_ST + 1024);
      __builtin_amdgcn_sched_barrier(0);
      q_fma<5, 128>(t, qw, qx0, qx1, qa0, qa1);
    }
    const u64 tag = epoch + 1ull;
    const int pair = ai / ASPLIT, split = ai % ASPLIT;
    const int b = pair >> 4, hkv = pair & 15;
    const int ustart = split * 42 + (split < 4 ? split : 4), ucnt = split < 4 ? 43 : 42;
    const int nw = (ucnt - wave + 7) >> 3;
    const u16* Kb = p.Ak + (size_t)pair * 4096 * 128 + (size_t)(ustart + wave) * 2048 + (lane >> 4) * 128 + (lane & 15) * 8;
    const u16* Vb = p.Av + (size_t)pair * 4096 * 128 + (size_t)(ustart + wave) * 2048 + (lane >> 4) * 128 + (lane & 15) * 8;
    u32x4 K0[4], V0[4], K1[4], V1[4], K2[4], V2[4];
#define A_LOAD(KK, VV, it) do { _Pragma("unroll") for (int i_ = 0; i_ < 4; ++i_) { KK[i_] = ldnt(Kb + (size_t)(it) * 16384 + i_ * 512); } \
                                _Pragma("unroll") for (int i_ = 0; i_ < 4; ++i_) { VV[i_] = ldnt(Vb + (size_t)(it) * 16384 + i_ * 512); } } while (0)
    q_reduce(lds, wave, lane, qa0, qa1);
    BAR();
    q_publish<8>(lds, wsr, qmat, qcol0, t, tag);
    STAMP(1);
    if (wave != 0) A_LOAD(K0, V0, 0);
    if (wave == 0) {
      u32 off[1] = {WS_QG + (u32)((0 * 2 + b) * 1024 + (2 * hkv) * 32 + lane) * 16};
      u32x4 g[1];
      gpoll<1>(wsr, off, g, true, tag);
      *(u32x2*)(lds + L_SCR + 2048 + lane * 8) = u32x2{g[0][0], g[0][1]};
    }
    BAR();
    const u32x4 q0 = *(const u32x4*)(lds + L_SCR + 2048 + (lane & 15) * 16), q1 = *(const u32x4*)(lds + L_SCR + 2048 + 256 + (lane & 15) * 16);
    if (wave == 0) A_LOAD(K0, V0, 0);
    STAMP(2);
    float m0 = -INFINITY, m1 = -INFINITY, l0 = 0.f, l1 = 0.f;
    float acc0[8], acc1[8];
#pragma unroll
    for (int k = 0; k < 8; ++k) { acc0[k] = 0.f; acc1[k] = 0.f; }
#define A_COMPUTE(KK, VV) do { \
      float s0_[4], s1_[4]; \
      _Pragma("unroll") for (int i_ = 0; i_ < 4; ++i_) { \
        float d0_ = 0.f, d1_ = 0.f; \
        _Pragma("unroll") for (int k_ = 0; k_ < 4; ++k_) { d0_ = dot2(KK[i_][k_], q0[k_], d0_); d1_ = dot2(KK[i_][k_], q1[k_], d1_); } \
        d0_ = row16_sum(d0_); d1_ = row16_sum(d1_); \
        s0_[i_] = bfr(bfr(d0_) * SCALE); s1_[i_] = bfr(bfr(d1_) * SCALE); } \
      const float mx0_ = fmaxf(fmaxf(s0_[0], s0_[1]), fmaxf(s0_[2], s0_[3])), mx1_ = fmaxf(fmaxf(s1_[0], s1_[1]), fmaxf(s1_[2], s1_[3])); \
      if (__any(mx0_ > m0 || mx1_ > m1)) { \
        const float n0_ = fmaxf(m0, mx0_), n1_ = fmaxf(m1, mx1_); \
        const float al0_ = ex2((m0 - n0_) * L2E), al1_ = ex2((m1 - n1_) * L2E); \
        l0 *= al0_; l1 *= al1_; m0 = n0_; m1 = n1_; \
        _Pragma("unroll") for (int k_ = 0; k_ < 8; ++k_) { acc0[k_] *= al0_; acc1[k_] *= al1_; } } \
      float p0_[4], p1_[4]; \
      _Pragma("unroll") for (int i_ = 0; i_ < 4; ++i_) { p0_[i_] = ex2((s0_[i_] - m0) * L2E); p1_[i_] = ex2((s1_[i_] - m1) * L2E); } \
      l0 += (p0_[0] + p0_[1]) + (p0_[2] + p0_[3]); l1 += (p1_[0] + p1_[1]) + (p1_[2] + p1_[3]); \
      const u32 pa0_ = pack_bf16(p0_[0], p0_[1]), pb0_ = pack_bf16(p0_[2], p0_[3]), pa1_ = pack_bf16(p1_[0], p1_[1]), pb1_ = pack_bf16(p1_[2], p1_[3]); \
      _Pragma("unroll") for (int k_ = 0; k_ < 4; ++k_) { \
        const u32 lo01_ = __builtin_amdgcn_perm(VV[1][k_], VV[0][k_], 0x05040100u), hi01_ = __builtin_amdgcn_perm(VV[1][k_], VV[0][k_], 0x07060302u); \
        const u32 lo23_ = __builtin_amdgcn_perm(VV[3][k_], VV[2][k_], 0x05040100u), hi23_ = __builtin_amdgcn_perm(VV[3][k_], VV[2][k_], 0x07060302u); \
        acc0[2 * k_] = dot2(lo23_, pb0_, dot2(lo01_, pa0_, acc0[2 * k_])); acc0[2 * k_ + 1] = dot2(hi23_, pb0_, dot2(hi01_, pa0_, acc0[2 * k_ + 1])); \
        acc1[2 * k_] = dot2(lo23_, pb1_, dot2(lo01_, pa1_, acc1[2 * k_])); acc1[2 * k_ + 1] = dot2(hi23_, pb1_, dot2(hi01_, pa1_, acc1[2 * k_ + 1])); } \
    } while (0)
    DRAIN(); A_LOAD(K1, V1, 1); A_COMPUTE(K0, V0);
    DRAIN(); A_LOAD(K0, V0, 2); A_COMPUTE(K1, V1);
    DRAIN(); A_LOAD(K1, V1, 3); A_COMPUTE(K0, V0);
    DRAIN(); A_LOAD(K0, V0, 4); A_COMPUTE(K1, V1);
#define WO_DMA() do { \
      if (ai < 64) { \
        const unsigned char* src = (const unsigned char*)(p.Awo + (size_t)ai * 64 * 576); \
        for (int i = 0; i < 9; ++i) { \
          const int L = i * 512 + t, r = L / 72, pc = L - r * 72; \
          const int c = (((pc >> 1) ^ ((r >> 1) & 3)) << 1) | (pc & 1); \
          glds16(src + r * 1152 + c * 16, ldsb + (u32)(i * 512 + wave * 64) * 16); \
        } \
      } \
    } while (0)
    if (nw == 6) {
      DRAIN(); A_LOAD(K1, V1, 5); A_COMPUTE(K0, V0);
      A_COMPUTE(K1, V1);
    } else {
      A_COMPUTE(K0, V0);
    }
    WO_DMA();
    STAMP(3);
    float* mrg = (float*)(lds + L_MRG);
    float* mml = (float*)(lds + L_ST);
    {
      const float M0 = xmax32(xmax16(m0)), M1 = xmax32(xmax16(m1));
      const float a0 = ex2((m0 - M0) * L2E), a1 = ex2((m1 - M1) * L2E);
      l0 = xsum32(xsum16(l0 * a0)); l1 = xsum32(xsum16(l1 * a1));
#pragma unroll
      for (int k = 0; k < 8; ++k) { acc0[k] = xsum32(xsum16(acc0[k] * a0)); acc1[k] = xsum32(xsum16(acc1[k] * a1)); }
      if (lane < 16) {
        float* r0 = mrg + (wave * 2 + 0) * 128 + lane * 8; float* r1 = mrg + (wave * 2 + 1) * 128 + lane * 8;
        *(f32x4*)r0 = f32x4{acc0[0], acc0[1], acc0[2], acc0[3]}; *(f32x4*)(r0 + 4) = f32x4{acc0[4], acc0[5], acc0[6], acc0[7]};
        *(f32x4*)r1 = f32x4{acc1[0], acc1[1], acc1[2], acc1[3]}; *(f32x4*)(r1 + 4) = f32x4{acc1[4], acc1[5], acc1[6], acc1[7]};
        if (lane == 0) *(f32x4*)(mml + wave * 4) = f32x4{M0, l0, M1, l1};
      }
    }
    BAR();
    if (t < 128) {
      const int g = t >> 6, k = t & 63;
      float M = -INFINITY;
#pragma unroll
      for (int r = 0; r < 8; ++r) M = fmaxf(M, mml[r * 4 + g * 2]);
      float L = 0.f, o0 = 0.f, o1 = 0.f;
#pragma unroll
      for (int r = 0; r < 8; ++r) {
        const float w = ex2((mml[r * 4 + g * 2] - M) * L2E); L = fmaf(w, mml[r * 4 + g * 2 + 1], L);
        const float2 v = *(const float2*)(mrg + (r * 2 + g) * 128 + 2 * k);
        o0 = fmaf(w, v.x, o0); o1 = fmaf(w, v.y, o1);
      }
      gst(wsr, WS_APG + ((ai * 2 + g) * 65 + k) * 16, __float_as_uint(o0), __float_as_uint(o1), tag);
      if (k == 0) gst(wsr, WS_APG + ((ai * 2 + g) * 65 + 64) * 16, __float_as_uint(M), __float_as_uint(L), tag);
    }
    STAMP(4);
    if (ai < 64) {
      const int hq = ai >> 1, dh = ai & 1, thkv = hq >> 1, g = hq & 1;
      if (wave == 0) {
        const int bb = lane >> 5, k = lane & 31;
        const int a0i = (bb * 16 + thkv) * 6;
        u32 off[12]; u32x4 gg[12];
#pragma unroll
        for (int s = 0; s < 6; ++s) { off[s] = WS_APG + (((a0i + s) * 2 + g) * 65 + 32 * dh + k) * 16; off[6 + s] = WS_APG + (((a0i + s) * 2 + g) * 65 + 64) * 16; }
        gpoll<12>(wsr, off, gg, true, tag);
        float M = __uint_as_float(gg[6][0]);
#pragma unroll
        for (int s = 1; s < 6; ++s) M = fmaxf(M, __uint_as_float(gg[6 + s][0]));
        float L = 0.f, o0 = 0.f, o1 = 0.f;
#pragma unroll
        for (int s = 0; s < 6; ++s) {
          const float w = ex2((__uint_as_float(gg[6 + s][0]) - M) * L2E);
          L = fmaf(w, __uint_as_float(gg[6 + s][1]), L); o0 = fmaf(w, __uint_as_float(gg[s][0]), o0); o1 = fmaf(w, __uint_as_float(gg[s][1]), o1);
        }
        *(u32*)(lds + L_SCR + 2048 + bb * 128 + k * 4) = pack_bf16(o0 / L, o1 / L);
      }
      DRAIN();
      BAR();
      STAMP(5);
      proj_mfma(lds, 0, wsr, WS_OUTA + ai * 9216, tag, wave, lane);
      STAMP(6);
    } else if (ai < 100) {
      final_reduce(lds, wsr, WS_OUTA, tag, ai - 64, t, p.out, 0);
      if (ai == 64) {
        if (wave == 0) { wait_flags(ws, WS_B3FLAG + lane * 8, true, tag); if (lane == 0) flag_set(ws, WS_EPOCH, tag); }
      }
      STAMP(7);
    }
    STAMP_FLUSH();
    return;
  }

  const int j = bj;
  const int hh = j >> 1, half = j & 1;
  const int b3b = j >> 5, b3ks = j & 31;
  const int kb = wave & 3, kh = wave >> 2;
  if (wave >= 4) {
    if (wave == 4) code_prefetch(pc0, lane, ldsb + L_ST + 1024);
    BAR();
    const unsigned char* src = (const unsigned char*)(p.Bc + ((size_t)b3b * 4096 + 128 * b3ks + 32 * (wave - 4)) * 512) + lane * 16;
#pragma unroll
    for (int r = 0; r < 32; ++r) glds16(src + r * 1024, ldsb + L_C + (u32)(32 * (wave - 4) + r) * CSTR);
    BAR();
    BAR();
    DRAIN();
  } else {
    u32x4 wk[16];
    float qa0[8], qa1[8];
    {
      u32x4 qw[9]; u16 qx0[9], qx1[9];
      q_load<9, 64>(qW, qx, qcol0, t, qw, qx0, qx1);
      __builtin_amdgcn_sched_barrier(0);
#pragma unroll
      for (int i = 0; i < 16; ++i) wk[i] = ldnt(p.Bwuk + ((size_t)(256 * half + (t >> 4) + 16 * i) * 32 + hh) * 128 + (t & 15) * 8);
      __builtin_amdgcn_sched_barrier(0);
      q_fma<9, 64>(t, qw, qx0, qx1, qa0, qa1);
    }
    q_reduce(lds, wave, lane, qa0, qa1);
    const u64 tag = epoch + 1ull;
    BAR();
    q_publish<4>(lds, wsr, qmat, qcol0, t, tag);
  STAMP(1);
    if (wave == 0) {
      u32 off[1] = {WS_QG + (u32)((1 * 2 + (lane >> 5)) * 1024 + hh * 32 + (lane & 31)) * 16};
      u32x4 g[1];
      gpoll<1>(wsr, off, g, true, tag);
      *(u32x2*)(lds + L_ST + lane * 8) = u32x2{g[0][0], g[0][1]};
    }
    BAR();
    const u32x4 qa = *(const u32x4*)(lds + L_ST + (t & 15) * 16), qb = *(const u32x4*)(lds + L_ST + 256 + (t & 15) * 16);
  STAMP(2);
    float* qs = (float*)(lds + L_SCR + 2048);
#pragma unroll
    for (int i = 0; i < 16; ++i) {
      float d0 = 0.f, d1 = 0.f;
#pragma unroll
      for (int k = 0; k < 4; ++k) { d0 = dot2(wk[i][k], qa[k], d0); d1 = dot2(wk[i][k], qb[k], d1); }
      d0 = row16_sum(d0); d1 = row16_sum(d1);
      if ((t & 15) == 0) { qs[(t >> 4) + 16 * i] = d0; qs[256 + (t >> 4) + 16 * i] = d1; }
    }
  STAMP(3);
    BAR();
    if (wave == 0) {
      const int bb = lane >> 5, c8 = lane & 31;
      u32x4 hi, lo;
#pragma unroll
      for (int k = 0; k < 4; ++k) {
        const float v0 = qs[bb * 256 + c8 * 8 + 2 * k], v1 = qs[bb * 256 + c8 * 8 + 2 * k + 1];
        const float h0 = bfr(v0), h1 = bfr(v1);
        hi[k] = pack_bf16(h0, h1); lo[k] = pack_bf16(v0 - h0, v1 - h1);
      }
      const u32 o = ((bb * 32 + hh) * 512 + 256 * half + c8 * 8) * 2;
      st128(wsr, WS_QH + o, hi); st128(wsr, WS_QL + o, lo);
      DRAIN();
      if (lane == 0) flag_set(ws, WS_B2FLAG + j * 8, tag);
    }
  STAMP(4);
  }
  BAR();
  STAMP(5);
  const u64 tag = epoch + 1ull;
  const int head = lane & 31, hi = lane >> 5;
  const int g4 = lane >> 4, i16 = lane & 15, qq = i16 >> 2, pp = i16 & 3;
  u32x4 cf[16];
  bf16x8 pv[2][8];
  {
    const int pi = 16 * (head >> 4) + 4 * (head & 3) + 2 * ((head >> 3) & 1) + ((head >> 2) & 1);
    const unsigned char* cr = lds + L_C + (32 * kb + pi) * CSTR + (256 * kh + 8 * hi) * 2;
#pragma unroll
    for (int s = 0; s < 16; ++s) cf[s] = *(const u32x4*)(cr + s * 32);
    const unsigned char* tb = lds + L_C + (4 * qq + 2 * hi) * CSTR + (64 * wave + 16 * (g4 & 1) + 4 * pp) * 2;
#pragma unroll
    for (int ks = 0; ks < 8; ++ks) {
      pv[0][ks] = tr_pair(tb + (16 * ks) * CSTR, tb + (16 * ks + 1) * CSTR);
      pv[1][ks] = tr_pair(tb + (16 * ks) * CSTR + 64, tb + (16 * ks + 1) * CSTR + 64);
    }
  }
  if (wave == 0) wait_flags(ws, WS_B2FLAG + lane * 8, true, tag);
  BAR();
  STAMP(6);
  if (wave >= 4) {
    const unsigned char* src = (const unsigned char*)(p.Bwuv + (size_t)hh * 128 + 64 * half);
#pragma unroll
    for (int i = 0; i < 16; ++i) {
      const int L = i * 256 + (t - 256), r = L >> 3, pc = L & 7;
      const int c = (((pc >> 1) ^ ((r >> 1) & 3)) << 1) | (pc & 1);
      glds16(src + (size_t)r * 8192 + c * 16, ldsb + L_WUV + (u32)(i * 256 + (wave - 4) * 64) * 16);
    }
  }
  if (wave < 4) {
    u32x4 vh[8], vl[8];
#pragma unroll
    for (int i = 0; i < 8; ++i) {
      const int idx = t + 256 * i;
      vh[i] = ld128(wsr, WS_QH + b3b * 32768 + idx * 16);
      vl[i] = ld128(wsr, WS_QL + b3b * 32768 + idx * 16);
    }
#pragma unroll
    for (int i = 0; i < 8; ++i) {
      const int idx = t + 256 * i;
      *(u32x4*)(lds + L_QH + (idx >> 6) * CSTR + (idx & 63) * 16) = vh[i];
      *(u32x4*)(lds + L_QL + (idx >> 6) * CSTR + (idx & 63) * 16) = vl[i];
    }
  }
  BAR();
  STAMP(7);
  u32x4 wo[9];
#pragma unroll
  for (int i = 0; i < 9; ++i) wo[i] = ldnt(p.Bwo + (size_t)j * 64 * 576 + (size_t)(i * 512 + t) * 8);
  {
    float* smax = (float*)(lds + L_ST);
    float* slsum = smax + 128;
    f32x16 sacc;
#pragma unroll
    for (int r = 0; r < 16; ++r) sacc[r] = 0.f;
    {
      const unsigned char* bhp = lds + L_QH + head * CSTR + (256 * kh + 8 * hi) * 2;
      const unsigned char* blp = lds + L_QL + head * CSTR + (256 * kh + 8 * hi) * 2;
#pragma unroll
      for (int s = 0; s < 16; ++s) {
        const bf16x8 bh = __builtin_bit_cast(bf16x8, *(const u32x4*)(bhp + s * 32));
        const bf16x8 bl = __builtin_bit_cast(bf16x8, *(const u32x4*)(blp + s * 32));
        sacc = __builtin_amdgcn_mfma_f32_32x32x16_bf16(__builtin_bit_cast(bf16x8, cf[s]), bh, sacc, 0, 0, 0);
        sacc = __builtin_amdgcn_mfma_f32_32x32x16_bf16(__builtin_bit_cast(bf16x8, cf[s]), bl, sacc, 0, 0, 0);
      }
    }
  STAMP(8);
    if (kh == 1) {
#pragma unroll
      for (int r4 = 0; r4 < 4; ++r4)
        *(f32x4*)(lds + L_XB + ((kb * 4 + r4) * 64 + lane) * 16) = f32x4{sacc[4 * r4], sacc[4 * r4 + 1], sacc[4 * r4 + 2], sacc[4 * r4 + 3]};
    }
    BAR();
    float sv[16];
    if (kh == 0) {
      float mx = -INFINITY;
#pragma unroll
      for (int r4 = 0; r4 < 4; ++r4) {
        const f32x4 o = *(const f32x4*)(lds + L_XB + ((kb * 4 + r4) * 64 + lane) * 16);
#pragma unroll
        for (int e = 0; e < 4; ++e) { sv[4 * r4 + e] = bfr(bfr(sacc[4 * r4 + e] + o[e]) * SCALE); mx = fmaxf(mx, sv[4 * r4 + e]); }
      }
      mx = xmax32(mx);
      if (lane < 32) smax[kb * 32 + head] = mx;
    }
    BAR();
    if (kh == 0) {
      const float m = fmaxf(fmaxf(smax[head], smax[32 + head]), fmaxf(smax[64 + head], smax[96 + head]));
      float ls = 0.f;
#pragma unroll
      for (int r4 = 0; r4 < 4; ++r4) {
        float pe[4];
#pragma unroll
        for (int e = 0; e < 4; ++e) { pe[e] = ex2((sv[4 * r4 + e] - m) * L2E); ls += pe[e]; }
        *(u32x2*)(lds + L_P + head * 272 + (32 * kb + 8 * r4 + 4 * hi) * 2) = u32x2{pack_bf16(pe[0], pe[1]), pack_bf16(pe[2], pe[3])};
      }
      ls = xsum32(ls);
      if (lane < 32) slsum[kb * 32 + head] = ls;
    }
    BAR();
  STAMP(9);
    f32x16 oacc0, oacc1;
#pragma unroll
    for (int r = 0; r < 16; ++r) { oacc0[r] = 0.f; oacc1[r] = 0.f; }
    {
      const unsigned char* pa = lds + L_P + head * 272 + hi * 16;
#pragma unroll
      for (int ks = 0; ks < 8; ++ks) {
        const bf16x8 a = __builtin_bit_cast(bf16x8, *(const u32x4*)(pa + ks * 32));
        oacc0 = __builtin_amdgcn_mfma_f32_32x32x16_bf16(pv[0][ks], a, oacc0, 0, 0, 0);
        oacc1 = __builtin_amdgcn_mfma_f32_32x32x16_bf16(pv[1][ks], a, oacc1, 0, 0, 0);
      }
    }
  STAMP(10);
    {
      unsigned char* ot = lds + L_OT + wave * 4608;
#pragma unroll
      for (int r4 = 0; r4 < 4; ++r4) {
        *(u32x2*)(ot + head * 144 + (8 * r4 + 4 * hi) * 2) = u32x2{pack_bf16(oacc0[4 * r4], oacc0[4 * r4 + 1]), pack_bf16(oacc0[4 * r4 + 2], oacc0[4 * r4 + 3])};
        *(u32x2*)(ot + head * 144 + (32 + 8 * r4 + 4 * hi) * 2) = u32x2{pack_bf16(oacc1[4 * r4], oacc1[4 * r4 + 1]), pack_bf16(oacc1[4 * r4 + 2], oacc1[4 * r4 + 3])};
      }
      LGKM0();
#pragma unroll
      for (int i = 0; i < 4; ++i) {
        const int hrow = (lane >> 3) + 8 * i, ch = lane & 7;
        const u32x4 v = *(const u32x4*)(ot + hrow * 144 + ch * 16);
        st128(wsr, WS_BPART + (((b3b * 32 + b3ks) * 32 + hrow) * 512 + 64 * wave + 8 * ch) * 2, v);
      }
    }
    if (t < 32) {
      const float L = (slsum[t] + slsum[32 + t]) + (slsum[64 + t] + slsum[96 + t]);
      const float m = fmaxf(fmaxf(smax[t], smax[32 + t]), fmaxf(smax[64 + t], smax[96 + t]));
      st64(wsr, WS_BML + ((b3b * 32 + b3ks) * 32 + t) * 8, u32x2{__float_as_uint(m), __float_as_uint(L)});
    }
    DRAIN();
    BAR();
    if (t == 0) flag_set(ws, WS_B3FLAG + j * 8, tag);
  STAMP(11);
  }
  {
#pragma unroll
    for (int i = 0; i < 9; ++i) {
      const int L = i * 512 + t, r = L / 72, c = L - r * 72;
      const int pc = (((c >> 1) ^ ((r >> 1) & 3)) << 1) | (c & 1);
      *(u32x4*)(lds + L_WO + (r * 72 + pc) * 16) = wo[i];
    }
  }
  {
    if (wave == 0) wait_flags(ws, WS_B3FLAG + lane * 8, true, tag);
    BAR();
  STAMP(12);
    const int rg = lane >> 3, ch = lane & 7;
    u32x4 g[8];
#pragma unroll
    for (int i = 0; i < 8; ++i) g[i] = ld128(wsr, WS_BPART + (((rg + 8 * i) * 32 + hh) * 512 + 64 * wave + 8 * ch) * 2);
    float wgt;
    {
      const u32x2 ml = ld64(wsr, WS_BML + (lane * 32 + hh) * 8);
      const float m = __uint_as_float(ml[0]), l = __uint_as_float(ml[1]);
      float M = m;
      M = fmaxf(M, dppf<0x128>(M)); M = fmaxf(M, dppf<0x124>(M)); M = fmaxf(M, dppf<0x122>(M)); M = fmaxf(M, dppf<0x121>(M)); M = xmax16(M);
      const float w = ex2((m - M) * L2E);
      const float L = xsum16(row16_sum(w * l));
      wgt = w / L;
    }
    float s0[8], s1[8];
#pragma unroll
    for (int k = 0; k < 8; ++k) { s0[k] = 0.f; s1[k] = 0.f; }
#pragma unroll
    for (int i = 0; i < 4; ++i) {
      const float w0 = __shfl(wgt, rg + 8 * i), w1 = __shfl(wgt, 32 + rg + 8 * i);
#pragma unroll
      for (int k = 0; k < 4; ++k) {
        s0[2 * k] = fmaf(w0, bflo(g[i][k]), s0[2 * k]); s0[2 * k + 1] = fmaf(w0, bfhi(g[i][k]), s0[2 * k + 1]);
        s1[2 * k] = fmaf(w1, bflo(g[i + 4][k]), s1[2 * k]); s1[2 * k + 1] = fmaf(w1, bfhi(g[i + 4][k]), s1[2 * k + 1]);
      }
    }
#pragma unroll
    for (int k = 0; k < 8; ++k) {
      float x = s0[k]; x += dppf<0x128>(x); x = xsum16(x); x = xsum32(x); s0[k] = x;
      float y = s1[k]; y += dppf<0x128>(y); y = xsum16(y); y = xsum32(y); s1[k] = y;
    }
  STAMP(13);
    unsigned char* ca = lds + L_CA + wave * 512;
    if (lane < 8) {
      u32x4 h0, l0, h1, l1;
#pragma unroll
      for (int k = 0; k < 4; ++k) {
        const float a = bfr(s0[2 * k]), bq = bfr(s0[2 * k + 1]), c = bfr(s1[2 * k]), d = bfr(s1[2 * k + 1]);
        h0[k] = pack_bf16(a, bq); l0[k] = pack_bf16(s0[2 * k] - a, s0[2 * k + 1] - bq);
        h1[k] = pack_bf16(c, d); l1[k] = pack_bf16(s1[2 * k] - c, s1[2 * k + 1] - d);
      }
      *(u32x4*)(ca + 0 * 128 + lane * 16) = h0; *(u32x4*)(ca + 1 * 128 + lane * 16) = l0;
      *(u32x4*)(ca + 2 * 128 + lane * 16) = h1; *(u32x4*)(ca + 3 * 128 + lane * 16) = l1;
    }
    LGKM0();
    bf16x8 af[2];
#pragma unroll
    for (int ks = 0; ks < 2; ++ks) {
      u32x2 lo = {0u, 0u}, hq = {0u, 0u};
      if (i16 < 4) {
        lo = *(const u32x2*)(ca + i16 * 128 + (32 * ks + 4 * g4) * 2);
        hq = *(const u32x2*)(ca + i16 * 128 + (32 * ks + 16 + 4 * g4) * 2);
      }
      af[ks] = __builtin_bit_cast(bf16x8, u32x4{lo[0], lo[1], hq[0], hq[1]});
    }
    float* red2 = (float*)(lds + L_RED2);
    {
      const int r0 = 4 * g4 + qq, sw = (r0 >> 1) & 3;
#pragma unroll
      for (int nb = 0; nb < 4; ++nb) {
        f32x4 d = {0.f, 0.f, 0.f, 0.f};
        const unsigned char* base = lds + L_WUV + (64 * wave + r0) * 128 + ((nb ^ sw) * 32) + pp * 8;
#pragma unroll
        for (int ks = 0; ks < 2; ++ks) {
          const bf16x8 b = tr_pair(base + (32 * ks) * 128, base + (32 * ks + 16) * 128);
          d = __builtin_amdgcn_mfma_f32_16x16x32_bf16(af[ks], b, d, 0, 0, 0);
        }
        if (lane < 16) { red2[(wave * 2 + 0) * 64 + 16 * nb + lane] = d[0] + d[1]; red2[(wave * 2 + 1) * 64 + 16 * nb + lane] = d[2] + d[3]; }
      }
    }
    DRAIN();
    BAR();
    if (t < 64) {
      float2 s = *(const float2*)(red2 + t * 2);
#pragma unroll
      for (int w = 1; w < 8; ++w) { const float2 v = *(const float2*)(red2 + w * 128 + t * 2); s.x += v.x; s.y += v.y; }
      *(u32*)(lds + L_SCR + 2048 + t * 4) = pack_bf16(s.x, s.y);
    }
    BAR();
  }
  STAMP(14);
  proj_mfma(lds, L_WO, wsr, WS_OUTB + j * 9216, tag, wave, lane);
  if (j < 36) final_reduce(lds, wsr, WS_OUTB, tag, j, t, p.out, 576);
  STAMP(15);
  STAMP_FLUSH();
}

extern "C" void kernel_launch(void* const* d_in, const int* in_sizes, int n_in, void* d_out, int out_size, void* d_ws, size_t ws_size,
                              hipStream_t stream) {
  Params p;
  p.Ax = (const u16*)d_in[0]; p.Ak = (const u16*)d_in[1]; p.Av = (const u16*)d_in[2]; p.Awq = (const u16*)d_in[3]; p.Awo = (const u16*)d_in[4];
  p.Bx = (const u16*)d_in[5]; p.Bc = (const u16*)d_in[6]; p.Bwq = (const u16*)d_in[7]; p.Bwuk = (const u16*)d_in[8]; p.Bwuv = (const u16*)d_in[9];
  p.Bwo = (const u16*)d_in[10];
  p.out = (u16*)d_out; p.ws = (unsigned char*)d_ws;
  fused_kernel<<<dim3(NWG), dim3(NT), 0, stream>>>(p);
}
```

```cpp
#include <hip/hip_runtime.h>
#include <stdint.h>

typedef unsigned short u16;
typedef unsigned int u32;
typedef unsigned long long u64;
typedef u32 u32x4 __attribute__((ext_vector_type(4)));
typedef u32 u32x2 __attribute__((ext_vector_type(2)));
typedef float f32x4 __attribute__((ext_vector_type(4)));
typedef float f32x16 __attribute__((ext_vector_type(16)));
typedef __bf16 bf16x2 __attribute__((ext_vector_type(2)));
typedef __bf16 bf16x8 __attribute__((ext_vector_type(8)));
typedef short s16x4 __attribute__((ext_vector_type(4)));
typedef short s16x8 __attribute__((ext_vector_type(8)));
#define GAS __attribute__((address_space(1)))
#define LAS __attribute__((address_space(3)))

constexpr int NT = 512;
constexpr int NWG = 256;
constexpr int ASPLIT = 6;
constexpr float SCALE = 0.08838834764831845f;
constexpr float L2E = 1.4426950408889634f;
constexpr int SPIN_MAX = 300000;

constexpr u32 WS_EPOCH = 0;
constexpr u32 WS_B2FLAG = 256;
constexpr u32 WS_B3FLAG = WS_B2FLAG + 512;
constexpr u32 WS_QG = 4096;
constexpr u32 WS_QH = WS_QG + 65536;
constexpr u32 WS_QL = WS_QH + 65536;
constexpr u32 WS_BML = WS_QL + 65536;
constexpr u32 WS_OUTB = WS_BML + 16384;
constexpr u32 WS_OUTA = WS_OUTB + 589824;
constexpr u32 WS_APG = WS_OUTA + 589824;
constexpr u32 WS_BPART = WS_APG + 401408;
constexpr u32 WS_END = WS_BPART + 2097152;

constexpr int CSTR = 1040;
constexpr int L_C = 0;
constexpr int L_WUV = 0;
constexpr int L_QH = 65536;
constexpr int L_QL = 98816;
constexpr int L_OT = 65536;
constexpr int L_WO = 65536;
constexpr int L_CA = 139264;
constexpr int L_RED2 = 143360;
constexpr int L_XB = 133120;
constexpr int L_P = 149504;
constexpr int L_ST = 158208;
constexpr int L_SCR = 159744;
constexpr int L_MRG = 81920;
constexpr int LDS_BYTES = 163840;

struct Params {
  const u16* Ax; const u16* Ak; const u16* Av; const u16* Awq; const u16* Awo;
  const u16* Bx; const u16* Bc; const u16* Bwq; const u16* Bwuk; const u16* Bwuv; const u16* Bwo;
  u16* out; unsigned char* ws;
};

__device__ __forceinline__ float bf2f(u16 v) { return __uint_as_float(((u32)v) << 16); }
__device__ __forceinline__ float bflo(u32 v) { return __uint_as_float(v << 16); }
__device__ __forceinline__ float bfhi(u32 v) { return __uint_as_float(v & 0xffff0000u); }
__device__ __forceinline__ float bfr(float f) { return (float)(__bf16)f; }
__device__ __forceinline__ u32 pack_bf16(float lo, float hi) {
  bf16x2 v = {(__bf16)lo, (__bf16)hi};
  return __builtin_bit_cast(u32, v);
}
__device__ __forceinline__ float dot2(u32 a, u32 b, float c) {
  return __builtin_amdgcn_fdot2_f32_bf16(__builtin_bit_cast(bf16x2, a), __builtin_bit_cast(bf16x2, b), c, false);
}
template <int CTRL> __device__ __forceinline__ float dppf(float x) {
  return __builtin_bit_cast(float, __builtin_amdgcn_mov_dpp(__builtin_bit_cast(int, x), CTRL, 0xf, 0xf, true));
}
__device__ __forceinline__ float row16_sum(float x) {
  x += dppf<0x128>(x); x += dppf<0x124>(x); x += dppf<0x122>(x); x += dppf<0x121>(x);
  return x;
}
__device__ __forceinline__ float xsum16(float x) { auto r = __builtin_amdgcn_permlane16_swap(__float_as_uint(x), __float_as_uint(x), false, false); return __uint_as_float(r[0]) + __uint_as_float(r[1]); }
__device__ __forceinline__ float xsum32(float x) { auto r = __builtin_amdgcn_permlane32_swap(__float_as_uint(x), __float_as_uint(x), false, false); return __uint_as_float(r[0]) + __uint_as_float(r[1]); }
__device__ __forceinline__ float xmax16(float x) { auto r = __builtin_amdgcn_permlane16_swap(__float_as_uint(x), __float_as_uint(x), false, false); return fmaxf(__uint_as_float(r[0]), __uint_as_float(r[1])); }
__device__ __forceinline__ float xmax32(float x) { auto r = __builtin_amdgcn_permlane32_swap(__float_as_uint(x), __float_as_uint(x), false, false); return fmaxf(__uint_as_float(r[0]), __uint_as_float(r[1])); }
__device__ __forceinline__ float ex2(float x) { return __builtin_amdgcn_exp2f(x); }

typedef __amdgpu_buffer_rsrc_t rsrc_t;
__device__ __forceinline__ u32x4 ld128(rsrc_t r, u32 off) { return __builtin_amdgcn_raw_buffer_load_b128(r, off, 0, 16); }
__device__ __forceinline__ u32x2 ld64(rsrc_t r, u32 off) { return __builtin_amdgcn_raw_buffer_load_b64(r, off, 0, 16); }
__device__ __forceinline__ void st128(rsrc_t r, u32 off, u32x4 v) { __builtin_amdgcn_raw_buffer_store_b128(v, r, off, 0, 16); }
__device__ __forceinline__ void st64(rsrc_t r, u32 off, u32x2 v) { __builtin_amdgcn_raw_buffer_store_b64(v, r, off, 0, 16); }
__device__ __forceinline__ u32x4 ldnt(const u16* p) { return __builtin_nontemporal_load((const u32x4*)p); }

__device__ __forceinline__ void flag_set(unsigned char* ws, u32 off, u64 tag) {
  __hip_atomic_store((GAS u64*)(ws + off), tag, __ATOMIC_RELAXED, __HIP_MEMORY_SCOPE_AGENT);
}
__device__ __forceinline__ u64 flag_get(unsigned char* ws, u32 off) {
  return __hip_atomic_load((GAS u64*)(ws + off), __ATOMIC_RELAXED, __HIP_MEMORY_SCOPE_AGENT);
}
#define DRAIN() asm volatile("s_waitcnt vmcnt(0)" ::: "memory")
#define BAR() asm volatile("s_waitcnt lgkmcnt(0)\n\ts_barrier" ::: "memory")
#define LGKM0() asm volatile("s_waitcnt lgkmcnt(0)" ::: "memory")

__device__ __forceinline__ void glds16(const void* gsrc, u32 lds_dst) {
  u32 keep;
  asm volatile("s_mov_b32 %0, m0\n\ts_mov_b32 m0, %2\n\ts_nop 0\n\tglobal_load_lds_dwordx4 %1, off nt\n\ts_mov_b32 m0, %0"
               : "=&s"(keep) : "v"(gsrc), "s"(lds_dst) : "memory");
}

constexpr int CODE_BYTES = 40960;
__device__ __forceinline__ void code_prefetch(u64 pc0, int lane, u32 lds_dummy) {
#pragma unroll
  for (int i = 0; i < (CODE_BYTES + 8191) / 8192; ++i) {
    int off = i * 8192 + lane * 128; off = off < CODE_BYTES - 4 ? off : CODE_BYTES - 4;
    const unsigned char* g = (const unsigned char*)pc0 + off;
    u32 keep;
    asm volatile("s_mov_b32 %0, m0\n\ts_mov_b32 m0, %2\n\ts_nop 0\n\tglobal_load_lds_dword %1, off\n\ts_mov_b32 m0, %0"
                 : "=&s"(keep) : "v"(g), "s"(lds_dummy) : "memory");
  }
}
__device__ __forceinline__ void wait_flags(unsigned char* ws, u32 off, bool active, u64 tag) {
  for (int spin = 0; spin < SPIN_MAX; ++spin) {
    u64 v = tag;
    if (active) v = flag_get(ws, off);
    if (__all(v == tag)) break;
    __builtin_amdgcn_s_sleep(1);
  }
  __builtin_amdgcn_fence(__ATOMIC_ACQUIRE, "wavefront");
}
__device__ __forceinline__ void gst(rsrc_t r, u32 off, u32 d0, u32 d1, u64 tag) { st128(r, off, u32x4{d0, d1, (u32)tag, (u32)(tag >> 32)}); }
__device__ __forceinline__ bool gok(u32x4 g, u64 tag) { return g[2] == (u32)tag && g[3] == (u32)(tag >> 32); }
template <int N> __device__ __forceinline__ void gpoll(rsrc_t r, const u32 (&off)[N], u32x4 (&g)[N], bool need, u64 tag) {
  for (int spin = 0; spin < SPIN_MAX; ++spin) {
    bool ok = true;
#pragma unroll
    for (int i = 0; i < N; ++i) { g[i] = ld128(r, off[i]); ok = ok && gok(g[i], tag); }
    if (__all(ok || !need)) break;
    __builtin_amdgcn_s_sleep(1);
  }
}

#define STAMP(i) do {} while (0)
#define STAMP_FLUSH() do {} while (0)

__device__ __forceinline__ bf16x8 tr_pair(const unsigned char* a0, const unsigned char* a1) {
  const s16x4 t0 = __builtin_amdgcn_ds_read_tr16_b64_v4i16((LAS s16x4*)a0);
  const s16x4 t1 = __builtin_amdgcn_ds_read_tr16_b64_v4i16((LAS s16x4*)a1);
  return __builtin_bit_cast(bf16x8, __builtin_shufflevector(t0, t1, 0, 1, 2, 3, 4, 5, 6, 7));
}

__device__ __forceinline__ void proj_mfma(unsigned char* lds, int wbase, rsrc_t wsr, u32 out_off, u64 tag, int wave, int lane) {
  const int g4 = lane >> 4, i16 = lane & 15, qq = i16 >> 2, pp = i16 & 3;
  bf16x8 af[2];
#pragma unroll
  for (int ks = 0; ks < 2; ++ks) {
    u32x2 lo = {0u, 0u}, hi = {0u, 0u};
    if (i16 < 2) {
      lo = *(const u32x2*)(lds + L_SCR + 2048 + i16 * 128 + (32 * ks + 4 * g4) * 2);
      hi = *(const u32x2*)(lds + L_SCR + 2048 + i16 * 128 + (32 * ks + 16 + 4 * g4) * 2);
    }
    af[ks] = __builtin_bit_cast(bf16x8, u32x4{lo[0], lo[1], hi[0], hi[1]});
  }
  const int r0 = 4 * g4 + qq;
  const int sw = (r0 >> 1) & 3;
#pragma unroll
  for (int i = 0; i < 5; ++i) {
    const int nb = wave + 8 * i;
    if (nb < 36) {
      f32x4 d = {0.f, 0.f, 0.f, 0.f};
      const unsigned char* base = lds + wbase + r0 * 1152 + ((nb ^ sw) * 32) + pp * 8;
#pragma unroll
      for (int ks = 0; ks < 2; ++ks) {
        const bf16x8 b = tr_pair(base + (32 * ks) * 1152, base + (32 * ks + 16) * 1152);
        d = __builtin_amdgcn_mfma_f32_16x16x32_bf16(af[ks], b, d, 0, 0, 0);
      }
      if (lane < 16) gst(wsr, out_off + (16 * nb + lane) * 16, __float_as_uint(d[0]), __float_as_uint(d[1]), tag);
    }
  }
}
__device__ __forceinline__ void final_reduce(unsigned char* lds, rsrc_t wsr, u32 part_off, u64 tag, int f, int t, u16* out, int base) {
  const int wave = t >> 6, lane = t & 63;
  const int pidx = t >> 3, sub = t & 7;
  u32 off[2] = {part_off + (u32)(pidx * 576 + 16 * f + sub) * 16, part_off + (u32)(pidx * 576 + 16 * f + 8 + sub) * 16};
  u32x4 g[2];
  gpoll<2>(wsr, off, g, true, tag);
  float v[4] = {__uint_as_float(g[0][0]), __uint_as_float(g[0][1]), __uint_as_float(g[1][0]), __uint_as_float(g[1][1])};
#pragma unroll
  for (int k = 0; k < 4; ++k) { float x = v[k]; x += dppf<0x128>(x); x = xsum16(x); x = xsum32(x); v[k] = x; }
  float* red = (float*)(lds + L_SCR);
  if (lane < 8) *(f32x4*)(red + wave * 32 + lane * 4) = f32x4{v[0], v[1], v[2], v[3]};
  BAR();
  if (t < 32) {
    float s = 0.f;
#pragma unroll
    for (int w = 0; w < 8; ++w) s += red[w * 32 + t];
    const int sb = t >> 2, k = t & 3, bb = k & 1, col = sb + 8 * (k >> 1);
    __bf16 o = (__bf16)s;
    out[bb * 1152 + base + 16 * f + col] = __builtin_bit_cast(u16, o);
  }
}

template <int NL, int RS>
__device__ __forceinline__ void q_load(const u16* qW, const u16* qx, int qcol0, int t, u32x4 (&qw)[NL], u16 (&qx0)[NL], u16 (&qx1)[NL]) {
  const int cch = t & 3, row0 = t >> 2;
#pragma unroll
  for (int i = 0; i < NL; ++i) {
    const int row = row0 + RS * i, rc = row < 576 ? row : 575;
    qx0[i] = qx[rc]; qx1[i] = qx[576 + rc];
  }
#pragma unroll
  for (int i = 0; i < NL; ++i) {
    const int row = row0 + RS * i, rc = row < 576 ? row : 575;
    qw[i] = ldnt(qW + (size_t)rc * 4096 + qcol0 + cch * 8);
  }
}
template <int NL, int RS>
__device__ __forceinline__ void q_fma(int t, const u32x4 (&qw)[NL], const u16 (&qx0)[NL], const u16 (&qx1)[NL], float (&qa0)[8], float (&qa1)[8]) {
#pragma unroll
  for (int k = 0; k < 8; ++k) { qa0[k] = 0.f; qa1[k] = 0.f; }
#pragma unroll
  for (int i = 0; i < NL; ++i) {
    const bool live = (t >> 2) + RS * i < 576;
    const float x0 = live ? bf2f(qx0[i]) : 0.f, x1 = live ? bf2f(qx1[i]) : 0.f;
#pragma unroll
    for (int k = 0; k < 4; ++k) {
      const float lo = bflo(qw[i][k]), hi = bfhi(qw[i][k]);
      qa0[2 * k] = fmaf(x0, lo, qa0[2 * k]); qa0[2 * k + 1] = fmaf(x0, hi, qa0[2 * k + 1]);
      qa1[2 * k] = fmaf(x1, lo, qa1[2 * k]); qa1[2 * k + 1] = fmaf(x1, hi, qa1[2 * k + 1]);
    }
  }
}
__device__ __forceinline__ void q_reduce(unsigned char* lds, int wave, int lane, float (&qa0)[8], float (&qa1)[8]) {
#pragma unroll
  for (int k = 0; k < 8; ++k) {
    float x = qa0[k]; x += dppf<0x124>(x); x += dppf<0x128>(x); x = xsum16(x); x = xsum32(x); qa0[k] = x;
    float y = qa1[k]; y += dppf<0x124>(y); y += dppf<0x128>(y); y = xsum16(y); y = xsum32(y); qa1[k] = y;
  }
  float* red_ = (float*)(lds + L_SCR);
  if (lane < 4) {
    float* r0 = red_ + wave * 64 + lane * 8;
    *(f32x4*)r0 = f32x4{qa0[0], qa0[1], qa0[2], qa0[3]}; *(f32x4*)(r0 + 4) = f32x4{qa0[4], qa0[5], qa0[6], qa0[7]};
    *(f32x4*)(r0 + 32) = f32x4{qa1[0], qa1[1], qa1[2], qa1[3]}; *(f32x4*)(r0 + 36) = f32x4{qa1[4], qa1[5], qa1[6], qa1[7]};
  }
}
template <int NW>
__device__ __forceinline__ void q_publish(unsigned char* lds, rsrc_t wsr, int qmat, int qcol0, int t, u64 tag) {
  if (t < 16) {
    const float* red_ = (const float*)(lds + L_SCR);
    f32x4 s_ = *(const f32x4*)(red_ + t * 4);
#pragma unroll
    for (int w = 1; w < NW; ++w) s_ += *(const f32x4*)(red_ + w * 64 + t * 4);
    gst(wsr, WS_QG + ((qmat * 2 + (t >> 3)) * 1024 + (qcol0 >> 2) + (t & 7)) * 16, pack_bf16(s_[0], s_[1]), pack_bf16(s_[2], s_[3]), tag);
  }
}

#define U3 48
#define ADEPTH 1
#define PREMODE 1
#define NPRE3 0
#define D1 0
#define D2 0
#define DELAY(n) do { _Pragma("unroll") for (int d_ = 0; d_ < (n); ++d_) __builtin_amdgcn_s_sleep(8); } while (0)
constexpr int U2 = (256 - 4 * U3) / 2;

#define A_LOAD(KK, VV, it) do { _Pragma("unroll") for (int i_ = 0; i_ < 4; ++i_) { KK[i_] = ldnt(Kb + (size_t)(it) * 16384 + i_ * 512); } \
                                _Pragma("unroll") for (int i_ = 0; i_ < 4; ++i_) { VV[i_] = ldnt(Vb + (size_t)(it) * 16384 + i_ * 512); } } while (0)
#define A_COMPUTE(KK, VV) do { \
      float s0_[4], s1_[4]; \
      _Pragma("unroll") for (int i_ = 0; i_ < 4; ++i_) { \
        float d0_ = 0.f, d1_ = 0.f; \
        _Pragma("unroll") for (int k_ = 0; k_ < 4; ++k_) { d0_ = dot2(KK[i_][k_], q0[k_], d0_); d1_ = dot2(KK[i_][k_], q1[k_], d1_); } \
        d0_ = row16_sum(d0_); d1_ = row16_sum(d1_); \
        s0_[i_] = bfr(bfr(d0_) * SCALE); s1_[i_] = bfr(bfr(d1_) * SCALE); } \
      const float mx0_ = fmaxf(fmaxf(s0_[0], s0_[1]), fmaxf(s0_[2], s0_[3])), mx1_ = fmaxf(fmaxf(s1_[0], s1_[1]), fmaxf(s1_[2], s1_[3])); \
      if (__any(mx0_ > m0 || mx1_ > m1)) { \
        const float n0_ = fmaxf(m0, mx0_), n1_ = fmaxf(m1, mx1_); \
        const float al0_ = ex2((m0 - n0_) * L2E), al1_ = ex2((m1 - n1_) * L2E); \
        l0 *= al0_; l1 *= al1_; m0 = n0_; m1 = n1_; \
        _Pragma("unroll") for (int k_ = 0; k_ < 8; ++k_) { acc0[k_] *= al0_; acc1[k_] *= al1_; } } \
      float p0_[4], p1_[4]; \
      _Pragma("unroll") for (int i_ = 0; i_ < 4; ++i_) { p0_[i_] = ex2((s0_[i_] - m0) * L2E); p1_[i_] = ex2((s1_[i_] - m1) * L2E); } \
      l0 += (p0_[0] + p0_[1]) + (p0_[2] + p0_[3]); l1 += (p1_[0] + p1_[1]) + (p1_[2] + p1_[3]); \
      const u32 pa0_ = pack_bf16(p0_[0], p0_[1]), pb0_ = pack_bf16(p0_[2], p0_[3]), pa1_ = pack_bf16(p1_[0], p1_[1]), pb1_ = pack_bf16(p1_[2], p1_[3]); \
      _Pragma("unroll") for (int k_ = 0; k_ < 4; ++k_) { \
        const u32 lo01_ = __builtin_amdgcn_perm(VV[1][k_], VV[0][k_], 0x05040100u), hi01_ = __builtin_amdgcn_perm(VV[1][k_], VV[0][k_], 0x07060302u); \
        const u32 lo23_ = __builtin_amdgcn_perm(VV[3][k_], VV[2][k_], 0x05040100u), hi23_ = __builtin_amdgcn_perm(VV[3][k_], VV[2][k_], 0x07060302u); \
        acc0[2 * k_] = dot2(lo23_, pb0_, dot2(lo01_, pa0_, acc0[2 * k_])); acc0[2 * k_ + 1] = dot2(hi23_, pb0_, dot2(hi01_, pa0_, acc0[2 * k_ + 1])); \
        acc1[2 * k_] = dot2(lo23_, pb1_, dot2(lo01_, pa1_, acc1[2 * k_])); acc1[2 * k_ + 1] = dot2(hi23_, pb1_, dot2(hi01_, pa1_, acc1[2 * k_ + 1])); } \
    } while (0)

#define WAITD() asm volatile("s_waitcnt vmcnt(0)" ::: "memory")
#define A_STEP(KK, VV, KN, VN, u) do { \
    if ((u) + ADEPTH < NU) { WAITD(); A_LOAD(KN, VN, (u) + ADEPTH); } else if ((u) + ADEPTH == NU) { if (extra) { WAITD(); A_LOAD(KN, VN, (u) + ADEPTH); } } \
    __builtin_amdgcn_sched_barrier(0); \
    if ((u) < NU) { A_COMPUTE(KK, VV); } else if ((u) == NU) { if (extra) A_COMPUTE(KK, VV); } \
    __builtin_amdgcn_sched_barrier(0); } while (0)
template <int NU, int NPRE>
__device__ __forceinline__ void a_stream(bool extra, bool poll, const u16* Kb, const u16* Vb, unsigned char* lds, rsrc_t wsr, u32 qoff, u64 tag, int lane,
                                         float& m0, float& m1, float& l0, float& l1, float (&acc0)[8], float (&acc1)[8],
                                         u32x4 (&K0)[4], u32x4 (&V0)[4], u32x4 (&K1)[4], u32x4 (&V1)[4]) {
  u32x4 K2[4], V2[4];
  if (poll) {
    u32 off[1] = {qoff};
    u32x4 g[1];
    gpoll<1>(wsr, off, g, true, tag);
    *(u32x2*)(lds + L_SCR + 2048 + lane * 8) = u32x2{g[0][0], g[0][1]};
  }
  A_LOAD(K0, V0, 0);
  if (ADEPTH == 2) A_LOAD(K1, V1, 1);
  BAR();
  const u32x4 q0 = *(const u32x4*)(lds + L_SCR + 2048 + (lane & 15) * 16), q1 = *(const u32x4*)(lds + L_SCR + 2048 + 256 + (lane & 15) * 16);
  A_STEP(K0, V0, K1, V1, 0); A_STEP(K1, V1, K0, V0, 1); A_STEP(K0, V0, K1, V1, 2); A_STEP(K1, V1, K0, V0, 3);
  A_STEP(K0, V0, K1, V1, 4); A_STEP(K1, V1, K0, V0, 5); A_STEP(K0, V0, K1, V1, 6); A_STEP(K1, V1, K0, V0, 7);
}

__global__ void __launch_bounds__(NT, 2) fused_kernel(Params p) {
  __shared__ __attribute__((aligned(16))) unsigned char lds[LDS_BYTES];
  u64 pc0; asm volatile("s_getpc_b64 %0" : "=s"(pc0)); pc0 &= ~255ull;
  const int bid = blockIdx.x, t = threadIdx.x, lane = t & 63;
  const int wave = __builtin_amdgcn_readfirstlane(t >> 6);
  unsigned char* ws = p.ws;
  const rsrc_t wsr = __builtin_amdgcn_make_buffer_rsrc(ws, 0, WS_END, 0x00020000);
  const u32 ldsb = (u32)(uintptr_t)(LAS unsigned char*)lds;
  STAMP(0);
  u32 vz; asm volatile("v_mov_b32 %0, 0" : "=v"(vz));
  const u64 epoch = flag_get(ws + vz, WS_EPOCH);
  const int xcd = bid & 7, slot = bid >> 3;
  const bool isB = slot < 8;

  if (!isB) {
    const bool isR2 = slot < 16;
    const int r2 = (slot - 8) * 8 + xcd, r3 = (slot - 16) * 8 + xcd;
    const int pair = isR2 ? (r2 >> 1) : (r3 >> 2), sub = isR2 ? 4 + (r2 & 1) : (r3 & 3);
    const int ai = pair * 6 + sub;
    const int b = pair >> 4, hkv = pair & 15;
    const int ustart = (isR2 ? 4 * U3 + (sub - 4) * U2 : sub * U3) + (7 - wave);
    const u16* Kb = p.Ak + (size_t)pair * 4096 * 128 + (size_t)ustart * 2048 + (lane >> 4) * 128 + (lane & 15) * 8;
    const u16* Vb = p.Av + (size_t)pair * 4096 * 128 + (size_t)ustart * 2048 + (lane >> 4) * 128 + (lane & 15) * 8;
    const u32 qoff = WS_QG + (u32)((0 * 2 + b) * 1024 + (2 * hkv) * 32 + lane) * 16;
    float m0 = -INFINITY, m1 = -INFINITY, l0 = 0.f, l1 = 0.f;
    float acc0[8], acc1[8];
#pragma unroll
    for (int k = 0; k < 8; ++k) { acc0[k] = 0.f; acc1[k] = 0.f; }
    u64 tag;
    u32x4 K0[4], V0[4], K1[4], V1[4];
    if (!isR2) {
      const int qc = (((slot - 16) >> 1) << 4) | (xcd << 1) | ((slot - 16) & 1);
      const int qcol0 = qc * 32;
      u32x4 qwB[5], qwA[5]; u16 bx0[5], bx1[5], ax0[5], ax1[5];
      q_load<5, 128>(p.Bwq, p.Bx, qcol0, t, qwB, bx0, bx1);
      __builtin_amdgcn_sched_barrier(0);
      float qa0[8], qa1[8];
      q_fma<5, 128>(t, qwB, bx0, bx1, qa0, qa1);
      __builtin_amdgcn_sched_barrier(0);
      q_load<5, 128>(p.Awq, p.Ax, qcol0, t, qwA, ax0, ax1);
      if (NPRE3 > 0) A_LOAD(K0, V0, 0);
      if (NPRE3 > 1) A_LOAD(K1, V1, 1);
      __builtin_amdgcn_sched_barrier(0);
      q_reduce(lds, wave, lane, qa0, qa1);
      tag = epoch + 1ull;
      BAR();
      q_publish<8>(lds, wsr, 1, qcol0, t, tag);
      STAMP(1);
      q_fma<5, 128>(t, qwA, ax0, ax1, qa0, qa1);
      q_reduce(lds - 4096, wave, lane, qa0, qa1);
      BAR();
      q_publish<8>(lds - 4096, wsr, 0, qcol0, t, tag);
      STAMP(2);
      constexpr int NLO = U3 / 8, WSPLIT = 8 - (U3 - NLO * 8);
      a_stream<NLO, NPRE3>(wave >= WSPLIT, wave == 0, Kb, Vb, lds, wsr, qoff, tag, lane, m0, m1, l0, l1, acc0, acc1, K0, V0, K1, V1);
    } else {
      const int hh = r2 >> 1, half = r2 & 1;
      u32x4 wk[8];
      DELAY(D2);
#pragma unroll
      for (int i = 0; i < 8; ++i) wk[i] = ldnt(p.Bwuk + ((size_t)(256 * half + (t >> 4) + 32 * i) * 32 + hh) * 128 + (t & 15) * 8);
      tag = epoch + 1ull;
      if (wave == 0) {
        u32 off[1] = {WS_QG + (u32)((1 * 2 + (lane >> 5)) * 1024 + hh * 32 + (lane & 31)) * 16};
        u32x4 g[1];
        gpoll<1>(wsr, off, g, true, tag);
        *(u32x2*)(lds + L_ST + lane * 8) = u32x2{g[0][0], g[0][1]};
      }
      BAR();
      STAMP(1);
      const u32x4 qa = *(const u32x4*)(lds + L_ST + (t & 15) * 16), qb = *(const u32x4*)(lds + L_ST + 256 + (t & 15) * 16);
      float* qs = (float*)(lds + L_SCR);
#pragma unroll
      for (int i = 0; i < 8; ++i) {
        float d0 = 0.f, d1 = 0.f;
#pragma unroll
        for (int k = 0; k < 4; ++k) { d0 = dot2(wk[i][k], qa[k], d0); d1 = dot2(wk[i][k], qb[k], d1); }
        d0 = row16_sum(d0); d1 = row16_sum(d1);
        if ((t & 15) == 0) { qs[(t >> 4) + 32 * i] = d0; qs[256 + (t >> 4) + 32 * i] = d1; }
      }
      BAR();
      if (wave == 0) {
        const int bb = lane >> 5, c8 = lane & 31;
        u32x4 hi, lo;
#pragma unroll
        for (int k = 0; k < 4; ++k) {
          const float v0 = qs[bb * 256 + c8 * 8 + 2 * k], v1 = qs[bb * 256 + c8 * 8 + 2 * k + 1];
          const float h0 = bfr(v0), h1 = bfr(v1);
          hi[k] = pack_bf16(h0, h1); lo[k] = pack_bf16(v0 - h0, v1 - h1);
        }
        const u32 o = ((bb * 32 + hh) * 512 + 256 * half + c8 * 8) * 2;
        st128(wsr, WS_QH + o, hi); st128(wsr, WS_QL + o, lo);
        DRAIN();
        if (lane == 0) flag_set(ws, WS_B2FLAG + r2 * 8, tag);
      }
      BAR();
      STAMP(2);
      {
        const unsigned char* src = (const unsigned char*)(p.Awo + (size_t)r2 * 64 * 576);
        for (int i = 0; i < 9; ++i) {
          const int L = i * 512 + t, r = L / 72, pc = L - r * 72;
          const int c = (((pc >> 1) ^ ((r >> 1) & 3)) << 1) | (pc & 1);
          glds16(src + r * 1152 + c * 16, ldsb + (u32)(i * 512 + wave * 64) * 16);
        }
      }
      constexpr int NLO = U2 / 8, WSPLIT = 8 - (U2 - NLO * 8);
      a_stream<NLO, 0>(wave >= WSPLIT, wave == 1, Kb, Vb, lds, wsr, qoff, tag, lane, m0, m1, l0, l1, acc0, acc1, K0, V0, K1, V1);
    }
    STAMP(3);
    float* mrg = (float*)(lds + L_MRG);
    float* mml = (float*)(lds + L_ST);
    {
      const float M0 = xmax32(xmax16(m0)), M1 = xmax32(xmax16(m1));
      const float a0 = ex2((m0 - M0) * L2E), a1 = ex2((m1 - M1) * L2E);
      l0 = xsum32(xsum16(l0 * a0)); l1 = xsum32(xsum16(l1 * a1));
#pragma unroll
      for (int k = 0; k < 8; ++k) { acc0[k] = xsum32(xsum16(acc0[k] * a0)); acc1[k] = xsum32(xsum16(acc1[k] * a1)); }
      if (lane < 16) {
        float* r0 = mrg + (wave * 2 + 0) * 128 + lane * 8; float* r1 = mrg + (wave * 2 + 1) * 128 + lane * 8;
        *(f32x4*)r0 = f32x4{acc0[0], acc0[1], acc0[2], acc0[3]}; *(f32x4*)(r0 + 4) = f32x4{acc0[4], acc0[5], acc0[6], acc0[7]};
        *(f32x4*)r1 = f32x4{acc1[0], acc1[1], acc1[2], acc1[3]}; *(f32x4*)(r1 + 4) = f32x4{acc1[4], acc1[5], acc1[6], acc1[7]};
        if (lane == 0) *(f32x4*)(mml + wave * 4) = f32x4{M0, l0, M1, l1};
      }
    }
    BAR();
    if (t < 128) {
      const int g = t >> 6, k = t & 63;
      float M = -INFINITY;
#pragma unroll
      for (int r = 0; r < 8; ++r) M = fmaxf(M, mml[r * 4 + g * 2]);
      float L = 0.f, o0 = 0.f, o1 = 0.f;
#pragma unroll
      for (int r = 0; r < 8; ++r) {
        const float w = ex2((mml[r * 4 + g * 2] - M) * L2E); L = fmaf(w, mml[r * 4 + g * 2 + 1], L);
        const float2 v = *(const float2*)(mrg + (r * 2 + g) * 128 + 2 * k);
        o0 = fmaf(w, v.x, o0); o1 = fmaf(w, v.y, o1);
      }
      gst(wsr, WS_APG + ((ai * 2 + g) * 65 + k) * 16, __float_as_uint(o0), __float_as_uint(o1), tag);
      if (k == 0) gst(wsr, WS_APG + ((ai * 2 + g) * 65 + 64) * 16, __float_as_uint(M), __float_as_uint(L), tag);
    }
    STAMP(4);
    if (isR2) {
      const int hq = r2 >> 1, dh = r2 & 1, thkv = hq >> 1, g = hq & 1;
      if (wave == 0) {
        const int bb = lane >> 5, k = lane & 31;
        const int a0i = (bb * 16 + thkv) * 6;
        u32 off[12]; u32x4 gg[12];
#pragma unroll
        for (int s = 0; s < 6; ++s) { off[s] = WS_APG + (((a0i + s) * 2 + g) * 65 + 32 * dh + k) * 16; off[6 + s] = WS_APG + (((a0i + s) * 2 + g) * 65 + 64) * 16; }
        gpoll<12>(wsr, off, gg, true, tag);
        float M = __uint_as_float(gg[6][0]);
#pragma unroll
        for (int s = 1; s < 6; ++s) M = fmaxf(M, __uint_as_float(gg[6 + s][0]));
        float L = 0.f, o0 = 0.f, o1 = 0.f;
#pragma unroll
        for (int s = 0; s < 6; ++s) {
          const float w = ex2((__uint_as_float(gg[6 + s][0]) - M) * L2E);
          L = fmaf(w, __uint_as_float(gg[6 + s][1]), L); o0 = fmaf(w, __uint_as_float(gg[s][0]), o0); o1 = fmaf(w, __uint_as_float(gg[s][1]), o1);
        }
        *(u32*)(lds + L_SCR + 2048 + bb * 128 + k * 4) = pack_bf16(o0 / L, o1 / L);
      }
      DRAIN();
      BAR();
      STAMP(5);
      proj_mfma(lds, 0, wsr, WS_OUTA + r2 * 9216, tag, wave, lane);
      STAMP(6);
    } else if (r3 < 36) {
      final_reduce(lds, wsr, WS_OUTA, tag, r3, t, p.out, 0);
      if (r3 == 0) {
        if (wave == 0) { wait_flags(ws, WS_B3FLAG + lane * 8, true, tag); if (lane == 0) flag_set(ws, WS_EPOCH, tag); }
      }
      STAMP(7);
    }
    STAMP_FLUSH();
    return;
  }

  const int j = slot * 8 + xcd;
  const int hh = j >> 1, half = j & 1;
  const int b3b = j >> 5, b3ks = j & 31;
  const int kb = wave & 3, kh = wave >> 2;
  {
    DELAY(D1);
    const unsigned char* src = (const unsigned char*)(p.Bc + ((size_t)b3b * 4096 + 128 * b3ks + 16 * wave) * 512) + lane * 16;
#pragma unroll
    for (int r = 0; r < 16; ++r) glds16(src + r * 1024, ldsb + L_C + (u32)(16 * wave + r) * CSTR);
    if (wave == 7) code_prefetch(pc0, lane, ldsb + L_ST + 1024);
    DRAIN();
  }
  BAR();
  STAMP(5);
  const u64 tag = epoch + 1ull;
  const int head = lane & 31, hi = lane >> 5;
  const int g4 = lane >> 4, i16 = lane & 15, qq = i16 >> 2, pp = i16 & 3;
  u32x4 cf[16];
  bf16x8 pv[2][8];
  {
    const int pi = 16 * (head >> 4) + 4 * (head & 3) + 2 * ((head >> 3) & 1) + ((head >> 2) & 1);
    const unsigned char* cr = lds + L_C + (32 * kb + pi) * CSTR + (256 * kh + 8 * hi) * 2;
#pragma unroll
    for (int s = 0; s < 16; ++s) cf[s] = *(const u32x4*)(cr + s * 32);
    const unsigned char* tb = lds + L_C + (4 * qq + 2 * hi) * CSTR + (64 * wave + 16 * (g4 & 1) + 4 * pp) * 2;
#pragma unroll
    for (int ks = 0; ks < 8; ++ks) {
      pv[0][ks] = tr_pair(tb + (16 * ks) * CSTR, tb + (16 * ks + 1) * CSTR);
      pv[1][ks] = tr_pair(tb + (16 * ks) * CSTR + 64, tb + (16 * ks + 1) * CSTR + 64);
    }
  }
  STAMP(4);
  BAR();
  {
    const unsigned char* src = (const unsigned char*)(p.Bwuv + (size_t)hh * 128 + 64 * half);
#pragma unroll
    for (int i = 0; i < 8; ++i) {
      const int L = i * 512 + t, r = L >> 3, pc = L & 7;
      const int c = (((pc >> 1) ^ ((r >> 1) & 3)) << 1) | (pc & 1);
      glds16(src + (size_t)r * 8192 + c * 16, ldsb + L_WUV + (u32)(i * 512 + wave * 64) * 16);
    }
  }
  u32x4 wo[9];
#pragma unroll
  for (int i = 0; i < 9; ++i) wo[i] = ldnt(p.Bwo + (size_t)j * 64 * 576 + (size_t)(i * 512 + t) * 8);
  __builtin_amdgcn_sched_barrier(0);
  wait_flags(ws, WS_B2FLAG + (wave * 8 + (lane & 7)) * 8, true, tag);
  STAMP(6);
  {
    u32x4 vh[4], vl[4];
#pragma unroll
    for (int i = 0; i < 4; ++i) {
      vh[i] = ld128(wsr, WS_QH + b3b * 32768 + ((wave * 4 + i) * 64 + lane) * 16);
      vl[i] = ld128(wsr, WS_QL + b3b * 32768 + ((wave * 4 + i) * 64 + lane) * 16);
    }
#pragma unroll
    for (int i = 0; i < 4; ++i) {
      *(u32x4*)(lds + L_QH + (wave * 4 + i) * CSTR + lane * 16) = vh[i];
      *(u32x4*)(lds + L_QL + (wave * 4 + i) * CSTR + lane * 16) = vl[i];
    }
  }
  BAR();
  STAMP(7);
  {
    float* smax = (float*)(lds + L_ST);
    float* slsum = smax + 128;
    f32x16 sacc;
#pragma unroll
    for (int r = 0; r < 16; ++r) sacc[r] = 0.f;
    {
      const unsigned char* bhp = lds + L_QH + head * CSTR + (256 * kh + 8 * hi) * 2;
      const unsigned char* blp = lds + L_QL + head * CSTR + (256 * kh + 8 * hi) * 2;
#pragma unroll
      for (int s = 0; s < 16; ++s) {
        const bf16x8 bh = __builtin_bit_cast(bf16x8, *(const u32x4*)(bhp + s * 32));
        const bf16x8 bl = __builtin_bit_cast(bf16x8, *(const u32x4*)(blp + s * 32));
        sacc = __builtin_amdgcn_mfma_f32_32x32x16_bf16(__builtin_bit_cast(bf16x8, cf[s]), bh, sacc, 0, 0, 0);
        sacc = __builtin_amdgcn_mfma_f32_32x32x16_bf16(__builtin_bit_cast(bf16x8, cf[s]), bl, sacc, 0, 0, 0);
      }
    }
  STAMP(8);
    if (kh == 1) {
#pragma unroll
      for (int r4 = 0; r4 < 4; ++r4)
        *(f32x4*)(lds + L_XB + ((kb * 4 + r4) * 64 + lane) * 16) = f32x4{sacc[4 * r4], sacc[4 * r4 + 1], sacc[4 * r4 + 2], sacc[4 * r4 + 3]};
    }
    BAR();
    float sv[16];
    if (kh == 0) {
      float mx = -INFINITY;
#pragma unroll
      for (int r4 = 0; r4 < 4; ++r4) {
        const f32x4 o = *(const f32x4*)(lds + L_XB + ((kb * 4 + r4) * 64 + lane) * 16);
#pragma unroll
        for (int e = 0; e < 4; ++e) { sv[4 * r4 + e] = bfr(bfr(sacc[4 * r4 + e] + o[e]) * SCALE); mx = fmaxf(mx, sv[4 * r4 + e]); }
      }
      mx = xmax32(mx);
      if (lane < 32) smax[kb * 32 + head] = mx;
    }
    BAR();
    if (kh == 0) {
      const float m = fmaxf(fmaxf(smax[head], smax[32 + head]), fmaxf(smax[64 + head], smax[96 + head]));
      float ls = 0.f;
#pragma unroll
      for (int r4 = 0; r4 < 4; ++r4) {
        float pe[4];
#pragma unroll
        for (int e = 0; e < 4; ++e) { pe[e] = ex2((sv[4 * r4 + e] - m) * L2E); ls += pe[e]; }
        *(u32x2*)(lds + L_P + head * 272 + (32 * kb + 8 * r4 + 4 * hi) * 2) = u32x2{pack_bf16(pe[0], pe[1]), pack_bf16(pe[2], pe[3])};
      }
      ls = xsum32(ls);
      if (lane < 32) slsum[kb * 32 + head] = ls;
    }
    BAR();
  STAMP(9);
    f32x16 oacc0, oacc1;
#pragma unroll
    for (int r = 0; r < 16; ++r) { oacc0[r] = 0.f; oacc1[r] = 0.f; }
    {
      const unsigned char* pa = lds + L_P + head * 272 + hi * 16;
#pragma unroll
      for (int ks = 0; ks < 8; ++ks) {
        const bf16x8 a = __builtin_bit_cast(bf16x8, *(const u32x4*)(pa + ks * 32));
        oacc0 = __builtin_amdgcn_mfma_f32_32x32x16_bf16(pv[0][ks], a, oacc0, 0, 0, 0);
        oacc1 = __builtin_amdgcn_mfma_f32_32x32x16_bf16(pv[1][ks], a, oacc1, 0, 0, 0);
      }
    }
  STAMP(10);
    {
      unsigned char* ot = lds + L_OT + wave * 4608;
#pragma unroll
      for (int r4 = 0; r4 < 4; ++r4) {
        *(u32x2*)(ot + head * 144 + (8 * r4 + 4 * hi) * 2) = u32x2{pack_bf16(oacc0[4 * r4], oacc0[4 * r4 + 1]), pack_bf16(oacc0[4 * r4 + 2], oacc0[4 * r4 + 3])};
        *(u32x2*)(ot + head * 144 + (32 + 8 * r4 + 4 * hi) * 2) = u32x2{pack_bf16(oacc1[4 * r4], oacc1[4 * r4 + 1]), pack_bf16(oacc1[4 * r4 + 2], oacc1[4 * r4 + 3])};
      }
      LGKM0();
#pragma unroll
      for (int i = 0; i < 4; ++i) {
        const int hrow = (lane >> 3) + 8 * i, ch = lane & 7;
        const u32x4 v = *(const u32x4*)(ot + hrow * 144 + ch * 16);
        st128(wsr, WS_BPART + (((b3b * 32 + b3ks) * 32 + hrow) * 512 + 64 * wave + 8 * ch) * 2, v);
      }
    }
    if (t < 32) {
      const float L = (slsum[t] + slsum[32 + t]) + (slsum[64 + t] + slsum[96 + t]);
      const float m = fmaxf(fmaxf(smax[t], smax[32 + t]), fmaxf(smax[64 + t], smax[96 + t]));
      st64(wsr, WS_BML + ((b3b * 32 + b3ks) * 32 + t) * 8, u32x2{__float_as_uint(m), __float_as_uint(L)});
    }
    DRAIN();
    BAR();
    if (t == 0) flag_set(ws, WS_B3FLAG + j * 8, tag);
  STAMP(11);
  }
  {
#pragma unroll
    for (int i = 0; i < 9; ++i) {
      const int L = i * 512 + t, r = L / 72, c = L - r * 72;
      const int pc = (((c >> 1) ^ ((r >> 1) & 3)) << 1) | (c & 1);
      *(u32x4*)(lds + L_WO + (r * 72 + pc) * 16) = wo[i];
    }
  }
  {
    if (wave == 0) wait_flags(ws, WS_B3FLAG + lane * 8, true, tag);
    BAR();
  STAMP(12);
    const int rg = lane >> 3, ch = lane & 7;
    u32x4 g[8];
#pragma unroll
    for (int i = 0; i < 8; ++i) g[i] = ld128(wsr, WS_BPART + (((rg + 8 * i) * 32 + hh) * 512 + 64 * wave + 8 * ch) * 2);
    float wgt;
    {
      const u32x2 ml = ld64(wsr, WS_BML + (lane * 32 + hh) * 8);
      const float m = __uint_as_float(ml[0]), l = __uint_as_float(ml[1]);
      float M = m;
      M = fmaxf(M, dppf<0x128>(M)); M = fmaxf(M, dppf<0x124>(M)); M = fmaxf(M, dppf<0x122>(M)); M = fmaxf(M, dppf<0x121>(M)); M = xmax16(M);
      const float w = ex2((m - M) * L2E);
      const float L = xsum16(row16_sum(w * l));
      wgt = w / L;
    }
    float s0[8], s1[8];
#pragma unroll
    for (int k = 0; k < 8; ++k) { s0[k] = 0.f; s1[k] = 0.f; }
#pragma unroll
    for (int i = 0; i < 4; ++i) {
      const float w0 = __shfl(wgt, rg + 8 * i), w1 = __shfl(wgt, 32 + rg + 8 * i);
#pragma unroll
      for (int k = 0; k < 4; ++k) {
        s0[2 * k] = fmaf(w0, bflo(g[i][k]), s0[2 * k]); s0[2 * k + 1] = fmaf(w0, bfhi(g[i][k]), s0[2 * k + 1]);
        s1[2 * k] = fmaf(w1, bflo(g[i + 4][k]), s1[2 * k]); s1[2 * k + 1] = fmaf(w1, bfhi(g[i + 4][k]), s1[2 * k + 1]);
      }
    }
#pragma unroll
    for (int k = 0; k < 8; ++k) {
      float x = s0[k]; x += dppf<0x128>(x); x = xsum16(x); x = xsum32(x); s0[k] = x;
      float y = s1[k]; y += dppf<0x128>(y); y = xsum16(y); y = xsum32(y); s1[k] = y;
    }
  STAMP(13);
    unsigned char* ca = lds + L_CA + wave * 512;
    if (lane < 8) {
      u32x4 h0, l0, h1, l1;
#pragma unroll
      for (int k = 0; k < 4; ++k) {
        const float a = bfr(s0[2 * k]), bq = bfr(s0[2 * k + 1]), c = bfr(s1[2 * k]), d = bfr(s1[2 * k + 1]);
        h0[k] = pack_bf16(a, bq); l0[k] = pack_bf16(s0[2 * k] - a, s0[2 * k + 1] - bq);
        h1[k] = pack_bf16(c, d); l1[k] = pack_bf16(s1[2 * k] - c, s1[2 * k + 1] - d);
      }
      *(u32x4*)(ca + 0 * 128 + lane * 16) = h0; *(u32x4*)(ca + 1 * 128 + lane * 16) = l0;
      *(u32x4*)(ca + 2 * 128 + lane * 16) = h1; *(u32x4*)(ca + 3 * 128 + lane * 16) = l1;
    }
    LGKM0();
    bf16x8 af[2];
#pragma unroll
    for (int ks = 0; ks < 2; ++ks) {
      u32x2 lo = {0u, 0u}, hq = {0u, 0u};
      if (i16 < 4) {
        lo = *(const u32x2*)(ca + i16 * 128 + (32 * ks + 4 * g4) * 2);
        hq = *(const u32x2*)(ca + i16 * 128 + (32 * ks + 16 + 4 * g4) * 2);
      }
      af[ks] = __builtin_bit_cast(bf16x8, u32x4{lo[0], lo[1], hq[0], hq[1]});
    }
    float* red2 = (float*)(lds + L_RED2);
    {
      const int r0 = 4 * g4 + qq, sw = (r0 >> 1) & 3;
#pragma unroll
      for (int nb = 0; nb < 4; ++nb) {
        f32x4 d = {0.f, 0.f, 0.f, 0.f};
        const unsigned char* base = lds + L_WUV + (64 * wave + r0) * 128 + ((nb ^ sw) * 32) + pp * 8;
#pragma unroll
        for (int ks = 0; ks < 2; ++ks) {
          const bf16x8 b = tr_pair(base + (32 * ks) * 128, base + (32 * ks + 16) * 128);
          d = __builtin_amdgcn_mfma_f32_16x16x32_bf16(af[ks], b, d, 0, 0, 0);
        }
        if (lane < 16) { red2[(wave * 2 + 0) * 64 + 16 * nb + lane] = d[0] + d[1]; red2[(wave * 2 + 1) * 64 + 16 * nb + lane] = d[2] + d[3]; }
      }
    }
    DRAIN();
    BAR();
    if (t < 64) {
      float2 s = *(const float2*)(red2 + t * 2);
#pragma unroll
      for (int w = 1; w < 8; ++w) { const float2 v = *(const float2*)(red2 + w * 128 + t * 2); s.x += v.x; s.y += v.y; }
      *(u32*)(lds + L_SCR + 2048 + t * 4) = pack_bf16(s.x, s.y);
    }
    BAR();
  }
  STAMP(14);
  proj_mfma(lds, L_WO, wsr, WS_OUTB + j * 9216, tag, wave, lane);
  if (j < 36) final_reduce(lds, wsr, WS_OUTB, tag, j, t, p.out, 576);
  STAMP(15);
  STAMP_FLUSH();
}

extern "C" void kernel_launch(void* const* d_in, const int* in_sizes, int n_in, void* d_out, int out_size, void* d_ws, size_t ws_size,
                              hipStream_t stream) {
  Params p;
  p.Ax = (const u16*)d_in[0]; p.Ak = (const u16*)d_in[1]; p.Av = (const u16*)d_in[2]; p.Awq = (const u16*)d_in[3]; p.Awo = (const u16*)d_in[4];
  p.Bx = (const u16*)d_in[5]; p.Bc = (const u16*)d_in[6]; p.Bwq = (const u16*)d_in[7]; p.Bwuk = (const u16*)d_in[8]; p.Bwuv = (const u16*)d_in[9];
  p.Bwo = (const u16*)d_in[10];
  p.out = (u16*)d_out; p.ws = (unsigned char*)d_ws;
  fused_kernel<<<dim3(NWG), dim3(NT), 0, stream>>>(p);
}
```

```cpp
#include <hip/hip_runtime.h>
#include <stdint.h>

typedef unsigned short u16;
typedef unsigned int u32;
typedef unsigned long long u64;
typedef u32 u32x4 __attribute__((ext_vector_type(4)));
typedef u32 u32x2 __attribute__((ext_vector_type(2)));
typedef float f32x4 __attribute__((ext_vector_type(4)));
typedef float f32x16 __attribute__((ext_vector_type(16)));
typedef __bf16 bf16x2 __attribute__((ext_vector_type(2)));
typedef __bf16 bf16x8 __attribute__((ext_vector_type(8)));
typedef short s16x4 __attribute__((ext_vector_type(4)));
typedef short s16x8 __attribute__((ext_vector_type(8)));
#define GAS __attribute__((address_space(1)))
#define LAS __attribute__((address_space(3)))

constexpr int NT = 512;
constexpr int NWG = 256;
constexpr int ASPLIT = 6;
constexpr float SCALE = 0.08838834764831845f;
constexpr float L2E = 1.4426950408889634f;
constexpr int SPIN_MAX = 300000;

constexpr u32 WS_EPOCH = 0;
constexpr u32 WS_B2FLAG = 256;
constexpr u32 WS_B3FLAG = WS_B2FLAG + 512;
constexpr u32 WS_QG = 4096;
constexpr u32 WS_QH = WS_QG + 65536;
constexpr u32 WS_QL = WS_QH + 65536;
constexpr u32 WS_BML = WS_QL + 65536;
constexpr u32 WS_OUTB = WS_BML + 16384;
constexpr u32 WS_OUTA = WS_OUTB + 589824;
constexpr u32 WS_APG = WS_OUTA + 589824;
constexpr u32 WS_BPART = WS_APG + 401408;
constexpr u32 WS_END = WS_BPART + 2097152;

constexpr int CSTR = 1040;
constexpr int L_C = 0;
constexpr int L_WUV = 0;
constexpr int L_QH = 65536;
constexpr int L_QL = 98816;
constexpr int L_OT = 65536;
constexpr int L_WO = 65536;
constexpr int L_CA = 139264;
constexpr int L_RED2 = 143360;
constexpr int L_XB = 133120;
constexpr int L_P = 149504;
constexpr int L_ST = 158208;
constexpr int L_SCR = 159744;
constexpr int L_MRG = 81920;
constexpr int LDS_BYTES = 163840;

struct Params {
  const u16* Ax; const u16* Ak; const u16* Av; const u16* Awq; const u16* Awo;
  const u16* Bx; const u16* Bc; const u16* Bwq; const u16* Bwuk; const u16* Bwuv; const u16* Bwo;
  u16* out; unsigned char* ws;
};

__device__ __forceinline__ float bf2f(u16 v) { return __uint_as_float(((u32)v) << 16); }
__device__ __forceinline__ float bflo(u32 v) { return __uint_as_float(v << 16); }
__device__ __forceinline__ float bfhi(u32 v) { return __uint_as_float(v & 0xffff0000u); }
__device__ __forceinline__ float bfr(float f) { return (float)(__bf16)f; }
__device__ __forceinline__ u32 pack_bf16(float lo, float hi) {
  bf16x2 v = {(__bf16)lo, (__bf16)hi};
  return __builtin_bit_cast(u32, v);
}
__device__ __forceinline__ float dot2(u32 a, u32 b, float c) {
  return __builtin_amdgcn_fdot2_f32_bf16(__builtin_bit_cast(bf16x2, a), __builtin_bit_cast(bf16x2, b), c, false);
}
template <int CTRL> __device__ __forceinline__ float dppf(float x) {
  return __builtin_bit_cast(float, __builtin_amdgcn_mov_dpp(__builtin_bit_cast(int, x), CTRL, 0xf, 0xf, true));
}
__device__ __forceinline__ float row16_sum(float x) {
  x += dppf<0x128>(x); x += dppf<0x124>(x); x += dppf<0x122>(x); x += dppf<0x121>(x);
  return x;
}
__device__ __forceinline__ float xsum16(float x) { auto r = __builtin_amdgcn_permlane16_swap(__float_as_uint(x), __float_as_uint(x), false, false); return __uint_as_float(r[0]) + __uint_as_float(r[1]); }
__device__ __forceinline__ float xsum32(float x) { auto r = __builtin_amdgcn_permlane32_swap(__float_as_uint(x), __float_as_uint(x), false, false); return __uint_as_float(r[0]) + __uint_as_float(r[1]); }
__device__ __forceinline__ float xmax16(float x) { auto r = __builtin_amdgcn_permlane16_swap(__float_as_uint(x), __float_as_uint(x), false, false); return fmaxf(__uint_as_float(r[0]), __uint_as_float(r[1])); }
__device__ __forceinline__ float xmax32(float x) { auto r = __builtin_amdgcn_permlane32_swap(__float_as_uint(x), __float_as_uint(x), false, false); return fmaxf(__uint_as_float(r[0]), __uint_as_float(r[1])); }
__device__ __forceinline__ float ex2(float x) { return __builtin_amdgcn_exp2f(x); }

typedef __amdgpu_buffer_rsrc_t rsrc_t;
__device__ __forceinline__ u32x4 ld128(rsrc_t r, u32 off) { return __builtin_amdgcn_raw_buffer_load_b128(r, off, 0, 16); }
__device__ __forceinline__ u32x2 ld64(rsrc_t r, u32 off) { return __builtin_amdgcn_raw_buffer_load_b64(r, off, 0, 16); }
__device__ __forceinline__ void st128(rsrc_t r, u32 off, u32x4 v) { __builtin_amdgcn_raw_buffer_store_b128(v, r, off, 0, 16); }
__device__ __forceinline__ void st64(rsrc_t r, u32 off, u32x2 v) { __builtin_amdgcn_raw_buffer_store_b64(v, r, off, 0, 16); }
__device__ __forceinline__ u32x4 ldnt(const u16* p) { return __builtin_nontemporal_load((const u32x4*)p); }

__device__ __forceinline__ void flag_set(unsigned char* ws, u32 off, u64 tag) {
  __hip_atomic_store((GAS u64*)(ws + off), tag, __ATOMIC_RELAXED, __HIP_MEMORY_SCOPE_AGENT);
}
__device__ __forceinline__ u64 flag_get(unsigned char* ws, u32 off) {
  return __hip_atomic_load((GAS u64*)(ws + off), __ATOMIC_RELAXED, __HIP_MEMORY_SCOPE_AGENT);
}
#define DRAIN() asm volatile("s_waitcnt vmcnt(0)" ::: "memory")
#define BAR() asm volatile("s_waitcnt lgkmcnt(0)\n\ts_barrier" ::: "memory")
#define LGKM0() asm volatile("s_waitcnt lgkmcnt(0)" ::: "memory")

__device__ __forceinline__ void glds16(const void* gsrc, u32 lds_dst) {
  u32 keep;
  asm volatile("s_mov_b32 %0, m0\n\ts_mov_b32 m0, %2\n\ts_nop 0\n\tglobal_load_lds_dwordx4 %1, off nt\n\ts_mov_b32 m0, %0"
               : "=&s"(keep) : "v"(gsrc), "s"(lds_dst) : "memory");
}

constexpr int CODE_BYTES = 40960;
__device__ __forceinline__ void code_prefetch(u64 pc0, int lane, u32 lds_dummy) {
#pragma unroll
  for (int i = 0; i < (CODE_BYTES + 8191) / 8192; ++i) {
    int off = i * 8192 + lane * 128; off = off < CODE_BYTES - 4 ? off : CODE_BYTES - 4;
    const unsigned char* g = (const unsigned char*)pc0 + off;
    u32 keep;
    asm volatile("s_mov_b32 %0, m0\n\ts_mov_b32 m0, %2\n\ts_nop 0\n\tglobal_load_lds_dword %1, off\n\ts_mov_b32 m0, %0"
                 : "=&s"(keep) : "v"(g), "s"(lds_dummy) : "memory");
  }
}
__device__ __forceinline__ void wait_flags(unsigned char* ws, u32 off, bool active, u64 tag) {
  for (int spin = 0; spin < SPIN_MAX; ++spin) {
    u64 v = tag;
    if (active) v = flag_get(ws, off);
    if (__all(v == tag)) break;
    __builtin_amdgcn_s_sleep(1);
  }
  __builtin_amdgcn_fence(__ATOMIC_ACQUIRE, "wavefront");
}
__device__ __forceinline__ void gst(rsrc_t r, u32 off, u32 d0, u32 d1, u64 tag) { st128(r, off, u32x4{d0, d1, (u32)tag, (u32)(tag >> 32)}); }
__device__ __forceinline__ bool gok(u32x4 g, u64 tag) { return g[2] == (u32)tag && g[3] == (u32)(tag >> 32); }
template <int N> __device__ __forceinline__ void gpoll(rsrc_t r, const u32 (&off)[N], u32x4 (&g)[N], bool need, u64 tag) {
  for (int spin = 0; spin < SPIN_MAX; ++spin) {
    bool ok = true;
#pragma unroll
    for (int i = 0; i < N; ++i) { g[i] = ld128(r, off[i]); ok = ok && gok(g[i], tag); }
    if (__all(ok || !need)) break;
    __builtin_amdgcn_s_sleep(1);
  }
}

#define STAMP(i) do {} while (0)
#define STAMP_FLUSH() do {} while (0)

__device__ __forceinline__ bf16x8 tr_pair(const unsigned char* a0, const unsigned char* a1) {
  const s16x4 t0 = __builtin_amdgcn_ds_read_tr16_b64_v4i16((LAS s16x4*)a0);
  const s16x4 t1 = __builtin_amdgcn_ds_read_tr16_b64_v4i16((LAS s16x4*)a1);
  return __builtin_bit_cast(bf16x8, __builtin_shufflevector(t0, t1, 0, 1, 2, 3, 4, 5, 6, 7));
}

__device__ __forceinline__ void proj_mfma(unsigned char* lds, int wbase, rsrc_t wsr, u32 out_off, u64 tag, int wave, int lane) {
  const int g4 = lane >> 4, i16 = lane & 15, qq = i16 >> 2, pp = i16 & 3;
  bf16x8 af[2];
#pragma unroll
  for (int ks = 0; ks < 2; ++ks) {
    u32x2 lo = {0u, 0u}, hi = {0u, 0u};
    if (i16 < 2) {
      lo = *(const u32x2*)(lds + L_SCR + 2048 + i16 * 128 + (32 * ks + 4 * g4) * 2);
      hi = *(const u32x2*)(lds + L_SCR + 2048 + i16 * 128 + (32 * ks + 16 + 4 * g4) * 2);
    }
    af[ks] = __builtin_bit_cast(bf16x8, u32x4{lo[0], lo[1], hi[0], hi[1]});
  }
  const int r0 = 4 * g4 + qq;
  const int sw = (r0 >> 1) & 3;
#pragma unroll
  for (int i = 0; i < 5; ++i) {
    const int nb = wave + 8 * i;
    if (nb < 36) {
      f32x4 d = {0.f, 0.f, 0.f, 0.f};
      const unsigned char* base = lds + wbase + r0 * 1152 + ((nb ^ sw) * 32) + pp * 8;
#pragma unroll
      for (int ks = 0; ks < 2; ++ks) {
        const bf16x8 b = tr_pair(base + (32 * ks) * 1152, base + (32 * ks + 16) * 1152);
        d = __builtin_amdgcn_mfma_f32_16x16x32_bf16(af[ks], b, d, 0, 0, 0);
      }
      if (lane < 16) gst(wsr, out_off + (16 * nb + lane) * 16, __float_as_uint(d[0]), __float_as_uint(d[1]), tag);
    }
  }
}
__device__ __forceinline__ void final_reduce(unsigned char* lds, rsrc_t wsr, u32 part_off, u64 tag, int f, int t, u16* out, int base) {
  const int wave = t >> 6, lane = t & 63;
  const int pidx = t >> 3, sub = t & 7;
  u32 off[2] = {part_off + (u32)(pidx * 576 + 16 * f + sub) * 16, part_off + (u32)(pidx * 576 + 16 * f + 8 + sub) * 16};
  u32x4 g[2];
  gpoll<2>(wsr, off, g, true, tag);
  float v[4] = {__uint_as_float(g[0][0]), __uint_as_float(g[0][1]), __uint_as_float(g[1][0]), __uint_as_float(g[1][1])};
#pragma unroll
  for (int k = 0; k < 4; ++k) { float x = v[k]; x += dppf<0x128>(x); x = xsum16(x); x = xsum32(x); v[k] = x; }
  float* red = (float*)(lds + L_SCR);
  if (lane < 8) *(f32x4*)(red + wave * 32 + lane * 4) = f32x4{v[0], v[1], v[2], v[3]};
  BAR();
  if (t < 32) {
    float s = 0.f;
#pragma unroll
    for (int w = 0; w < 8; ++w) s += red[w * 32 + t];
    const int sb = t >> 2, k = t & 3, bb = k & 1, col = sb + 8 * (k >> 1);
    __bf16 o = (__bf16)s;
    out[bb * 1152 + base + 16 * f + col] = __builtin_bit_cast(u16, o);
  }
}

template <int NL, int RS>
__device__ __forceinline__ void q_load(const u16* qW, const u16* qx, int qcol0, int t, u32x4 (&qw)[NL], u16 (&qx0)[NL], u16 (&qx1)[NL]) {
  const int cch = t & 3, row0 = t >> 2;
#pragma unroll
  for (int i = 0; i < NL; ++i) {
    const int row = row0 + RS * i, rc = row < 576 ? row : 575;
    qx0[i] = qx[rc]; qx1[i] = qx[576 + rc];
  }
#pragma unroll
  for (int i = 0; i < NL; ++i) {
    const int row = row0 + RS * i, rc = row < 576 ? row : 575;
    qw[i] = ldnt(qW + (size_t)rc * 4096 + qcol0 + cch * 8);
  }
}
template <int NL, int RS>
__device__ __forceinline__ void q_fma(int t, const u32x4 (&qw)[NL], const u16 (&qx0)[NL], const u16 (&qx1)[NL], float (&qa0)[8], float (&qa1)[8]) {
#pragma unroll
  for (int k = 0; k < 8; ++k) { qa0[k] = 0.f; qa1[k] = 0.f; }
#pragma unroll
  for (int i = 0; i < NL; ++i) {
    const bool live = (t >> 2) + RS * i < 576;
    const float x0 = live ? bf2f(qx0[i]) : 0.f, x1 = live ? bf2f(qx1[i]) : 0.f;
#pragma unroll
    for (int k = 0; k < 4; ++k) {
      const float lo = bflo(qw[i][k]), hi = bfhi(qw[i][k]);
      qa0[2 * k] = fmaf(x0, lo, qa0[2 * k]); qa0[2 * k + 1] = fmaf(x0, hi, qa0[2 * k + 1]);
      qa1[2 * k] = fmaf(x1, lo, qa1[2 * k]); qa1[2 * k + 1] = fmaf(x1, hi, qa1[2 * k + 1]);
    }
  }
}
__device__ __forceinline__ void q_reduce(unsigned char* lds, int wave, int lane, float (&qa0)[8], float (&qa1)[8]) {
#pragma unroll
  for (int k = 0; k < 8; ++k) {
    float x = qa0[k]; x += dppf<0x124>(x); x += dppf<0x128>(x); x = xsum16(x); x = xsum32(x); qa0[k] = x;
    float y = qa1[k]; y += dppf<0x124>(y); y += dppf<0x128>(y); y = xsum16(y); y = xsum32(y); qa1[k] = y;
  }
  float* red_ = (float*)(lds + L_SCR);
  if (lane < 4) {
    float* r0 = red_ + wave * 64 + lane * 8;
    *(f32x4*)r0 = f32x4{qa0[0], qa0[1], qa0[2], qa0[3]}; *(f32x4*)(r0 + 4) = f32x4{qa0[4], qa0[5], qa0[6], qa0[7]};
    *(f32x4*)(r0 + 32) = f32x4{qa1[0], qa1[1], qa1[2], qa1[3]}; *(f32x4*)(r0 + 36) = f32x4{qa1[4], qa1[5], qa1[6], qa1[7]};
  }
}
template <int NW>
__device__ __forceinline__ void q_publish(unsigned char* lds, rsrc_t wsr, int qmat, int qcol0, int t, u64 tag) {
  if (t < 16) {
    const float* red_ = (const float*)(lds + L_SCR);
    f32x4 s_ = *(const f32x4*)(red_ + t * 4);
#pragma unroll
    for (int w = 1; w < NW; ++w) s_ += *(const f32x4*)(red_ + w * 64 + t * 4);
    gst(wsr, WS_QG + ((qmat * 2 + (t >> 3)) * 1024 + (qcol0 >> 2) + (t & 7)) * 16, pack_bf16(s_[0], s_[1]), pack_bf16(s_[2], s_[3]), tag);
  }
}

#define U3 48
#define ADEPTH 1
#define PREMODE 1
#define NPRE3 0
#define D1 0
#define D2 0
#define DELAY(n) do { _Pragma("unroll") for (int d_ = 0; d_ < (n); ++d_) __builtin_amdgcn_s_sleep(8); } while (0)
constexpr int U2 = (256 - 4 * U3) / 2;

#define A_LOAD(KK, VV, it) do { _Pragma("unroll") for (int i_ = 0; i_ < 4; ++i_) { KK[i_] = ldnt(Kb + (size_t)(it) * 16384 + i_ * 512); } \
                                _Pragma("unroll") for (int i_ = 0; i_ < 4; ++i_) { VV[i_] = ldnt(Vb + (size_t)(it) * 16384 + i_ * 512); } } while (0)
#define A_COMPUTE(KK, VV) do { \
      float s0_[4], s1_[4]; \
      _Pragma("unroll") for (int i_ = 0; i_ < 4; ++i_) { \
        float d0_ = 0.f, d1_ = 0.f; \
        _Pragma("unroll") for (int k_ = 0; k_ < 4; ++k_) { d0_ = dot2(KK[i_][k_], q0[k_], d0_); d1_ = dot2(KK[i_][k_], q1[k_], d1_); } \
        d0_ = row16_sum(d0_); d1_ = row16_sum(d1_); \
        s0_[i_] = bfr(bfr(d0_) * SCALE); s1_[i_] = bfr(bfr(d1_) * SCALE); } \
      const float mx0_ = fmaxf(fmaxf(s0_[0], s0_[1]), fmaxf(s0_[2], s0_[3])), mx1_ = fmaxf(fmaxf(s1_[0], s1_[1]), fmaxf(s1_[2], s1_[3])); \
      if (__any(mx0_ > m0 || mx1_ > m1)) { \
        const float n0_ = fmaxf(m0, mx0_), n1_ = fmaxf(m1, mx1_); \
        const float al0_ = ex2((m0 - n0_) * L2E), al1_ = ex2((m1 - n1_) * L2E); \
        l0 *= al0_; l1 *= al1_; m0 = n0_; m1 = n1_; \
        _Pragma("unroll") for (int k_ = 0; k_ < 8; ++k_) { acc0[k_] *= al0_; acc1[k_] *= al1_; } } \
      float p0_[4], p1_[4]; \
      _Pragma("unroll") for (int i_ = 0; i_ < 4; ++i_) { p0_[i_] = ex2((s0_[i_] - m0) * L2E); p1_[i_] = ex2((s1_[i_] - m1) * L2E); } \
      l0 += (p0_[0] + p0_[1]) + (p0_[2] + p0_[3]); l1 += (p1_[0] + p1_[1]) + (p1_[2] + p1_[3]); \
      const u32 pa0_ = pack_bf16(p0_[0], p0_[1]), pb0_ = pack_bf16(p0_[2], p0_[3]), pa1_ = pack_bf16(p1_[0], p1_[1]), pb1_ = pack_bf16(p1_[2], p1_[3]); \
      _Pragma("unroll") for (int k_ = 0; k_ < 4; ++k_) { \
        const u32 lo01_ = __builtin_amdgcn_perm(VV[1][k_], VV[0][k_], 0x05040100u), hi01_ = __builtin_amdgcn_perm(VV[1][k_], VV[0][k_], 0x07060302u); \
        const u32 lo23_ = __builtin_amdgcn_perm(VV[3][k_], VV[2][k_], 0x05040100u), hi23_ = __builtin_amdgcn_perm(VV[3][k_], VV[2][k_], 0x07060302u); \
        acc0[2 * k_] = dot2(lo23_, pb0_, dot2(lo01_, pa0_, acc0[2 * k_])); acc0[2 * k_ + 1] = dot2(hi23_, pb0_, dot2(hi01_, pa0_, acc0[2 * k_ + 1])); \
        acc1[2 * k_] = dot2(lo23_, pb1_, dot2(lo01_, pa1_, acc1[2 * k_])); acc1[2 * k_ + 1] = dot2(hi23_, pb1_, dot2(hi01_, pa1_, acc1[2 * k_ + 1])); } \
    } while (0)

#define WAITD() asm volatile("s_waitcnt vmcnt(0)" ::: "memory")
#define A_STEP(KK, VV, KN, VN, u) do { \
    if ((u) + ADEPTH < NU) { WAITD(); A_LOAD(KN, VN, (u) + ADEPTH); } else if ((u) + ADEPTH == NU) { if (extra) { WAITD(); A_LOAD(KN, VN, (u) + ADEPTH); } } \
    __builtin_amdgcn_sched_barrier(0); \
    if ((u) < NU) { A_COMPUTE(KK, VV); } else if ((u) == NU) { if (extra) A_COMPUTE(KK, VV); } \
    __builtin_amdgcn_sched_barrier(0); } while (0)
template <int NU, int NPRE>
__device__ __forceinline__ void a_stream(bool extra, bool poll, const u16* Kb, const u16* Vb, unsigned char* lds, rsrc_t wsr, u32 qoff, u64 tag, int lane,
                                         float& m0, float& m1, float& l0, float& l1, float (&acc0)[8], float (&acc1)[8],
                                         u32x4 (&K0)[4], u32x4 (&V0)[4], u32x4 (&K1)[4], u32x4 (&V1)[4]) {
  u32x4 K2[4], V2[4];
  if (poll) {
    u32 off[1] = {qoff};
    u32x4 g[1];
    gpoll<1>(wsr, off, g, true, tag);
    *(u32x2*)(lds + L_SCR + 2048 + lane * 8) = u32x2{g[0][0], g[0][1]};
  }
  A_LOAD(K0, V0, 0);
  if (ADEPTH == 2) A_LOAD(K1, V1, 1);
  BAR();
  const u32x4 q0 = *(const u32x4*)(lds + L_SCR + 2048 + (lane & 15) * 16), q1 = *(const u32x4*)(lds + L_SCR + 2048 + 256 + (lane & 15) * 16);
  A_STEP(K0, V0, K1, V1, 0); A_STEP(K1, V1, K0, V0, 1); A_STEP(K0, V0, K1, V1, 2); A_STEP(K1, V1, K0, V0, 3);
  A_STEP(K0, V0, K1, V1, 4); A_STEP(K1, V1, K0, V0, 5); A_STEP(K0, V0, K1, V1, 6); A_STEP(K1, V1, K0, V0, 7);
}

__device__ __forceinline__ u32 touch_pages(const Params& p, int lane, const u16* Kb, const u16* Vb) {
  const unsigned char* a = (const unsigned char*)p.Ax;
  const int g = lane >> 2, k = lane & 3;
  const u32 big = k == 3 ? 0u : (u32)k << 21;
  if (g == 1) a = (const unsigned char*)p.Awq + big;
  if (g == 2) a = (const unsigned char*)p.Awo + big;
  if (g == 3) a = (const unsigned char*)p.Bwuv + (k == 2 ? 4194300u : big);
  if (g == 4) a = (const unsigned char*)p.Bwo + big;
  if (g == 5) a = p.ws + (k == 2 ? WS_END - 4 : big);
  if (g == 6) a = (const unsigned char*)p.out;
  if (g == 7) a = (const unsigned char*)(k & 1 ? Vb : Kb) + (k & 2 ? 98304u : 0u);
  u32 v = 0;
  if (lane < 32) v = *(const volatile u32*)a;
  return v;
}

__global__ void __launch_bounds__(NT, 2) fused_kernel(Params p) {
  __shared__ __attribute__((aligned(16))) unsigned char lds[LDS_BYTES];
  u64 pc0; asm volatile("s_getpc_b64 %0" : "=s"(pc0)); pc0 &= ~255ull;
  const int bid = blockIdx.x, t = threadIdx.x, lane = t & 63;
  const int wave = __builtin_amdgcn_readfirstlane(t >> 6);
  unsigned char* ws = p.ws;
  const rsrc_t wsr = __builtin_amdgcn_make_buffer_rsrc(ws, 0, WS_END, 0x00020000);
  const u32 ldsb = (u32)(uintptr_t)(LAS unsigned char*)lds;
  STAMP(0);
  u32 vz; asm volatile("v_mov_b32 %0, 0" : "=v"(vz));
  const u64 epoch = flag_get(ws + vz, WS_EPOCH);
  const int xcd = bid & 7, slot = bid >> 3;
  const bool isB = slot < 8;

  if (!isB) {
    const bool isR2 = slot < 16;
    const int r2 = (slot - 8) * 8 + xcd, r3 = (slot - 16) * 8 + xcd;
    const int pair = isR2 ? (r2 >> 1) : (r3 >> 2), sub = isR2 ? 4 + (r2 & 1) : (r3 & 3);
    const int ai = pair * 6 + sub;
    const int b = pair >> 4, hkv = pair & 15;
    const int ustart = (isR2 ? 4 * U3 + (sub - 4) * U2 : sub * U3) + (7 - wave);
    const u16* Kb = p.Ak + (size_t)pair * 4096 * 128 + (size_t)ustart * 2048 + (lane >> 4) * 128 + (lane & 15) * 8;
    const u16* Vb = p.Av + (size_t)pair * 4096 * 128 + (size_t)ustart * 2048 + (lane >> 4) * 128 + (lane & 15) * 8;
    const u32 qoff = WS_QG + (u32)((0 * 2 + b) * 1024 + (2 * hkv) * 32 + lane) * 16;
    float m0 = -INFINITY, m1 = -INFINITY, l0 = 0.f, l1 = 0.f;
    float acc0[8], acc1[8];
#pragma unroll
    for (int k = 0; k < 8; ++k) { acc0[k] = 0.f; acc1[k] = 0.f; }
    u64 tag;
    u32 tch = 0;
    u32x4 K0[4], V0[4], K1[4], V1[4];
    if (!isR2) {
      const int qc = (((slot - 16) >> 1) << 4) | (xcd << 1) | ((slot - 16) & 1);
      const int qcol0 = qc * 32;
      u32x4 qwB[5], qwA[5]; u16 bx0[5], bx1[5], ax0[5], ax1[5];
      q_load<5, 128>(p.Bwq, p.Bx, qcol0, t, qwB, bx0, bx1);
      if (wave == 7) tch = touch_pages(p, lane, Kb, Vb);
      __builtin_amdgcn_sched_barrier(0);
      float qa0[8], qa1[8];
      q_fma<5, 128>(t, qwB, bx0, bx1, qa0, qa1);
      __builtin_amdgcn_sched_barrier(0);
      q_load<5, 128>(p.Awq, p.Ax, qcol0, t, qwA, ax0, ax1);
      if (NPRE3 > 0) A_LOAD(K0, V0, 0);
      if (NPRE3 > 1) A_LOAD(K1, V1, 1);
      __builtin_amdgcn_sched_barrier(0);
      q_reduce(lds, wave, lane, qa0, qa1);
      tag = epoch + 1ull;
      BAR();
      q_publish<8>(lds, wsr, 1, qcol0, t, tag);
      STAMP(1);
      q_fma<5, 128>(t, qwA, ax0, ax1, qa0, qa1);
      q_reduce(lds - 4096, wave, lane, qa0, qa1);
      BAR();
      q_publish<8>(lds - 4096, wsr, 0, qcol0, t, tag);
      STAMP(2);
      constexpr int NLO = U3 / 8, WSPLIT = 8 - (U3 - NLO * 8);
      a_stream<NLO, NPRE3>(wave >= WSPLIT, wave == 0, Kb, Vb, lds, wsr, qoff, tag, lane, m0, m1, l0, l1, acc0, acc1, K0, V0, K1, V1);
    } else {
      const int hh = r2 >> 1, half = r2 & 1;
      u32x4 wk[8];
      DELAY(D2);
#pragma unroll
      for (int i = 0; i < 8; ++i) wk[i] = ldnt(p.Bwuk + ((size_t)(256 * half + (t >> 4) + 32 * i) * 32 + hh) * 128 + (t & 15) * 8);
      if (wave == 7) tch = touch_pages(p, lane, Kb, Vb);
      tag = epoch + 1ull;
      if (wave == 0) {
        u32 off[1] = {WS_QG + (u32)((1 * 2 + (lane >> 5)) * 1024 + hh * 32 + (lane & 31)) * 16};
        u32x4 g[1];
        gpoll<1>(wsr, off, g, true, tag);
        *(u32x2*)(lds + L_ST + lane * 8) = u32x2{g[0][0], g[0][1]};
      }
      BAR();
      STAMP(1);
      const u32x4 qa = *(const u32x4*)(lds + L_ST + (t & 15) * 16), qb = *(const u32x4*)(lds + L_ST + 256 + (t & 15) * 16);
      float* qs = (float*)(lds + L_SCR);
#pragma unroll
      for (int i = 0; i < 8; ++i) {
        float d0 = 0.f, d1 = 0.f;
#pragma unroll
        for (int k = 0; k < 4; ++k) { d0 = dot2(wk[i][k], qa[k], d0); d1 = dot2(wk[i][k], qb[k], d1); }
        d0 = row16_sum(d0); d1 = row16_sum(d1);
        if ((t & 15) == 0) { qs[(t >> 4) + 32 * i] = d0; qs[256 + (t >> 4) + 32 * i] = d1; }
      }
      BAR();
      if (wave == 0) {
        const int bb = lane >> 5, c8 = lane & 31;
        u32x4 hi, lo;
#pragma unroll
        for (int k = 0; k < 4; ++k) {
          const float v0 = qs[bb * 256 + c8 * 8 + 2 * k], v1 = qs[bb * 256 + c8 * 8 + 2 * k + 1];
          const float h0 = bfr(v0), h1 = bfr(v1);
          hi[k] = pack_bf16(h0, h1); lo[k] = pack_bf16(v0 - h0, v1 - h1);
        }
        const u32 o = ((bb * 32 + hh) * 512 + 256 * half + c8 * 8) * 2;
        st128(wsr, WS_QH + o, hi); st128(wsr, WS_QL + o, lo);
        DRAIN();
        if (lane == 0) flag_set(ws, WS_B2FLAG + r2 * 8, tag);
      }
      BAR();
      STAMP(2);
      {
        const unsigned char* src = (const unsigned char*)(p.Awo + (size_t)r2 * 64 * 576);
        for (int i = 0; i < 9; ++i) {
          const int L = i * 512 + t, r = L / 72, pc = L - r * 72;
          const int c = (((pc >> 1) ^ ((r >> 1) & 3)) << 1) | (pc & 1);
          glds16(src + r * 1152 + c * 16, ldsb + (u32)(i * 512 + wave * 64) * 16);
        }
      }
      constexpr int NLO = U2 / 8, WSPLIT = 8 - (U2 - NLO * 8);
      a_stream<NLO, 0>(wave >= WSPLIT, wave == 1, Kb, Vb, lds, wsr, qoff, tag, lane, m0, m1, l0, l1, acc0, acc1, K0, V0, K1, V1);
    }
    asm volatile("" :: "v"(tch));
    STAMP(3);
    float* mrg = (float*)(lds + L_MRG);
    float* mml = (float*)(lds + L_ST);
    {
      const float M0 = xmax32(xmax16(m0)), M1 = xmax32(xmax16(m1));
      const float a0 = ex2((m0 - M0) * L2E), a1 = ex2((m1 - M1) * L2E);
      l0 = xsum32(xsum16(l0 * a0)); l1 = xsum32(xsum16(l1 * a1));
#pragma unroll
      for (int k = 0; k < 8; ++k) { acc0[k] = xsum32(xsum16(acc0[k] * a0)); acc1[k] = xsum32(xsum16(acc1[k] * a1)); }
      if (lane < 16) {
        float* r0 = mrg + (wave * 2 + 0) * 128 + lane * 8; float* r1 = mrg + (wave * 2 + 1) * 128 + lane * 8;
        *(f32x4*)r0 = f32x4{acc0[0], acc0[1], acc0[2], acc0[3]}; *(f32x4*)(r0 + 4) = f32x4{acc0[4], acc0[5], acc0[6], acc0[7]};
        *(f32x4*)r1 = f32x4{acc1[0], acc1[1], acc1[2], acc1[3]}; *(f32x4*)(r1 + 4) = f32x4{acc1[4], acc1[5], acc1[6], acc1[7]};
        if (lane == 0) *(f32x4*)(mml + wave * 4) = f32x4{M0, l0, M1, l1};
      }
    }
    BAR();
    if (t < 128) {
      const int g = t >> 6, k = t & 63;
      float M = -INFINITY;
#pragma unroll
      for (int r = 0; r < 8; ++r) M = fmaxf(M, mml[r * 4 + g * 2]);
      float L = 0.f, o0 = 0.f, o1 = 0.f;
#pragma unroll
      for (int r = 0; r < 8; ++r) {
        const float w = ex2((mml[r * 4 + g * 2] - M) * L2E); L = fmaf(w, mml[r * 4 + g * 2 + 1], L);
        const float2 v = *(const float2*)(mrg + (r * 2 + g) * 128 + 2 * k);
        o0 = fmaf(w, v.x, o0); o1 = fmaf(w, v.y, o1);
      }
      gst(wsr, WS_APG + ((ai * 2 + g) * 65 + k) * 16, __float_as_uint(o0), __float_as_uint(o1), tag);
      if (k == 0) gst(wsr, WS_APG + ((ai * 2 + g) * 65 + 64) * 16, __float_as_uint(M), __float_as_uint(L), tag);
    }
    STAMP(4);
    if (isR2) {
      const int hq = r2 >> 1, dh = r2 & 1, thkv = hq >> 1, g = hq & 1;
      if (wave == 0) {
        const int bb = lane >> 5, k = lane & 31;
        const int a0i = (bb * 16 + thkv) * 6;
        u32 off[12]; u32x4 gg[12];
#pragma unroll
        for (int s = 0; s < 6; ++s) { off[s] = WS_APG + (((a0i + s) * 2 + g) * 65 + 32 * dh + k) * 16; off[6 + s] = WS_APG + (((a0i + s) * 2 + g) * 65 + 64) * 16; }
        gpoll<12>(wsr, off, gg, true, tag);
        float M = __uint_as_float(gg[6][0]);
#pragma unroll
        for (int s = 1; s < 6; ++s) M = fmaxf(M, __uint_as_float(gg[6 + s][0]));
        float L = 0.f, o0 = 0.f, o1 = 0.f;
#pragma unroll
        for (int s = 0; s < 6; ++s) {
          const float w = ex2((__uint_as_float(gg[6 + s][0]) - M) * L2E);
          L = fmaf(w, __uint_as_float(gg[6 + s][1]), L); o0 = fmaf(w, __uint_as_float(gg[s][0]), o0); o1 = fmaf(w, __uint_as_float(gg[s][1]), o1);
        }
        *(u32*)(lds + L_SCR + 2048 + bb * 128 + k * 4) = pack_bf16(o0 / L, o1 / L);
      }
      DRAIN();
      BAR();
      STAMP(5);
      proj_mfma(lds, 0, wsr, WS_OUTA + r2 * 9216, tag, wave, lane);
      STAMP(6);
    } else if (r3 < 36) {
      final_reduce(lds, wsr, WS_OUTA, tag, r3, t, p.out, 0);
      if (r3 == 0) {
        if (wave == 0) { wait_flags(ws, WS_B3FLAG + lane * 8, true, tag); if (lane == 0) flag_set(ws, WS_EPOCH, tag); }
      }
      STAMP(7);
    }
    STAMP_FLUSH();
    return;
  }

  const int j = slot * 8 + xcd;
  const int hh = j >> 1, half = j & 1;
  const int b3b = j >> 5, b3ks = j & 31;
  const int kb = wave & 3, kh = wave >> 2;
  {
    DELAY(D1);
    const unsigned char* src = (const unsigned char*)(p.Bc + ((size_t)b3b * 4096 + 128 * b3ks + 16 * wave) * 512) + lane * 16;
#pragma unroll
    for (int r = 0; r < 16; ++r) glds16(src + r * 1024, ldsb + L_C + (u32)(16 * wave + r) * CSTR);
    if (wave == 7) code_prefetch(pc0, lane, ldsb + L_ST + 1024);
    if (wave == 6) { const u32 tch = touch_pages(p, lane, p.Ak, p.Av); asm volatile("" :: "v"(tch)); }
    DRAIN();
  }
  BAR();
  STAMP(5);
  const u64 tag = epoch + 1ull;
  const int head = lane & 31, hi = lane >> 5;
  const int g4 = lane >> 4, i16 = lane & 15, qq = i16 >> 2, pp = i16 & 3;
  u32x4 cf[16];
  bf16x8 pv[2][8];
  {
    const int pi = 16 * (head >> 4) + 4 * (head & 3) + 2 * ((head >> 3) & 1) + ((head >> 2) & 1);
    const unsigned char* cr = lds + L_C + (32 * kb + pi) * CSTR + (256 * kh + 8 * hi) * 2;
#pragma unroll
    for (int s = 0; s < 16; ++s) cf[s] = *(const u32x4*)(cr + s * 32);
    const unsigned char* tb = lds + L_C + (4 * qq + 2 * hi) * CSTR + (64 * wave + 16 * (g4 & 1) + 4 * pp) * 2;
#pragma unroll
    for (int ks = 0; ks < 8; ++ks) {
      pv[0][ks] = tr_pair(tb + (16 * ks) * CSTR, tb + (16 * ks + 1) * CSTR);
      pv[1][ks] = tr_pair(tb + (16 * ks) * CSTR + 64, tb + (16 * ks + 1) * CSTR + 64);
    }
  }
  STAMP(4);
  BAR();
  {
    const unsigned char* src = (const unsigned char*)(p.Bwuv + (size_t)hh * 128 + 64 * half);
#pragma unroll
    for (int i = 0; i < 8; ++i) {
      const int L = i * 512 + t, r = L >> 3, pc = L & 7;
      const int c = (((pc >> 1) ^ ((r >> 1) & 3)) << 1) | (pc & 1);
      glds16(src + (size_t)r * 8192 + c * 16, ldsb + L_WUV + (u32)(i * 512 + wave * 64) * 16);
    }
  }
  u32x4 wo[9];
#pragma unroll
  for (int i = 0; i < 9; ++i) wo[i] = ldnt(p.Bwo + (size_t)j * 64 * 576 + (size_t)(i * 512 + t) * 8);
  __builtin_amdgcn_sched_barrier(0);
  wait_flags(ws, WS_B2FLAG + (wave * 8 + (lane & 7)) * 8, true, tag);
  STAMP(6);
  {
    u32x4 vh[4], vl[4];
#pragma unroll
    for (int i = 0; i < 4; ++i) {
      vh[i] = ld128(wsr, WS_QH + b3b * 32768 + ((wave * 4 + i) * 64 + lane) * 16);
      vl[i] = ld128(wsr, WS_QL + b3b * 32768 + ((wave * 4 + i) * 64 + lane) * 16);
    }
#pragma unroll
    for (int i = 0; i < 4; ++i) {
      *(u32x4*)(lds + L_QH + (wave * 4 + i) * CSTR + lane * 16) = vh[i];
      *(u32x4*)(lds + L_QL + (wave * 4 + i) * CSTR + lane * 16) = vl[i];
    }
  }
  BAR();
  STAMP(7);
  {
    float* smax = (float*)(lds + L_ST);
    float* slsum = smax + 128;
    f32x16 sacc;
#pragma unroll
    for (int r = 0; r < 16; ++r) sacc[r] = 0.f;
    {
      const unsigned char* bhp = lds + L_QH + head * CSTR + (256 * kh + 8 * hi) * 2;
      const unsigned char* blp = lds + L_QL + head * CSTR + (256 * kh + 8 * hi) * 2;
#pragma unroll
      for (int s = 0; s < 16; ++s) {
        const bf16x8 bh = __builtin_bit_cast(bf16x8, *(const u32x4*)(bhp + s * 32));
        const bf16x8 bl = __builtin_bit_cast(bf16x8, *(const u32x4*)(blp + s * 32));
        sacc = __builtin_amdgcn_mfma_f32_32x32x16_bf16(__builtin_bit_cast(bf16x8, cf[s]), bh, sacc, 0, 0, 0);
        sacc = __builtin_amdgcn_mfma_f32_32x32x16_bf16(__builtin_bit_cast(bf16x8, cf[s]), bl, sacc, 0, 0, 0);
      }
    }
  STAMP(8);
    if (kh == 1) {
#pragma unroll
      for (int r4 = 0; r4 < 4; ++r4)
        *(f32x4*)(lds + L_XB + ((kb * 4 + r4) * 64 + lane) * 16) = f32x4{sacc[4 * r4], sacc[4 * r4 + 1], sacc[4 * r4 + 2], sacc[4 * r4 + 3]};
    }
    BAR();
    float sv[16];
    if (kh == 0) {
      float mx = -INFINITY;
#pragma unroll
      for (int r4 = 0; r4 < 4; ++r4) {
        const f32x4 o = *(const f32x4*)(lds + L_XB + ((kb * 4 + r4) * 64 + lane) * 16);
#pragma unroll
        for (int e = 0; e < 4; ++e) { sv[4 * r4 + e] = bfr(bfr(sacc[4 * r4 + e] + o[e]) * SCALE); mx = fmaxf(mx, sv[4 * r4 + e]); }
      }
      mx = xmax32(mx);
      if (lane < 32) smax[kb * 32 + head] = mx;
    }
    BAR();
    if (kh == 0) {
      const float m = fmaxf(fmaxf(smax[head], smax[32 + head]), fmaxf(smax[64 + head], smax[96 + head]));
      float ls = 0.f;
#pragma unroll
      for (int r4 = 0; r4 < 4; ++r4) {
        float pe[4];
#pragma unroll
        for (int e = 0; e < 4; ++e) { pe[e] = ex2((sv[4 * r4 + e] - m) * L2E); ls += pe[e]; }
        *(u32x2*)(lds + L_P + head * 272 + (32 * kb + 8 * r4 + 4 * hi) * 2) = u32x2{pack_bf16(pe[0], pe[1]), pack_bf16(pe[2], pe[3])};
      }
      ls = xsum32(ls);
      if (lane < 32) slsum[kb * 32 + head] = ls;
    }
    BAR();
  STAMP(9);
    f32x16 oacc0, oacc1;
#pragma unroll
    for (int r = 0; r < 16; ++r) { oacc0[r] = 0.f; oacc1[r] = 0.f; }
    {
      const unsigned char* pa = lds + L_P + head * 272 + hi * 16;
#pragma unroll
      for (int ks = 0; ks < 8; ++ks) {
        const bf16x8 a = __builtin_bit_cast(bf16x8, *(const u32x4*)(pa + ks * 32));
        oacc0 = __builtin_amdgcn_mfma_f32_32x32x16_bf16(pv[0][ks], a, oacc0, 0, 0, 0);
        oacc1 = __builtin_amdgcn_mfma_f32_32x32x16_bf16(pv[1][ks], a, oacc1, 0, 0, 0);
      }
    }
  STAMP(10);
    {
      unsigned char* ot = lds + L_OT + wave * 4608;
#pragma unroll
      for (int r4 = 0; r4 < 4; ++r4) {
        *(u32x2*)(ot + head * 144 + (8 * r4 + 4 * hi) * 2) = u32x2{pack_bf16(oacc0[4 * r4], oacc0[4 * r4 + 1]), pack_bf16(oacc0[4 * r4 + 2], oacc0[4 * r4 + 3])};
        *(u32x2*)(ot + head * 144 + (32 + 8 * r4 + 4 * hi) * 2) = u32x2{pack_bf16(oacc1[4 * r4], oacc1[4 * r4 + 1]), pack_bf16(oacc1[4 * r4 + 2], oacc1[4 * r4 + 3])};
      }
      LGKM0();
#pragma unroll
      for (int i = 0; i < 4; ++i) {
        const int hrow = (lane >> 3) + 8 * i, ch = lane & 7;
        const u32x4 v = *(const u32x4*)(ot + hrow * 144 + ch * 16);
        st128(wsr, WS_BPART + (((b3b * 32 + b3ks) * 32 + hrow) * 512 + 64 * wave + 8 * ch) * 2, v);
      }
    }
    if (t < 32) {
      const float L = (slsum[t] + slsum[32 + t]) + (slsum[64 + t] + slsum[96 + t]);
      const float m = fmaxf(fmaxf(smax[t], smax[32 + t]), fmaxf(smax[64 + t], smax[96 + t]));
      st64(wsr, WS_BML + ((b3b * 32 + b3ks) * 32 + t) * 8, u32x2{__float_as_uint(m), __float_as_uint(L)});
    }
    DRAIN();
    BAR();
    if (t == 0) flag_set(ws, WS_B3FLAG + j * 8, tag);
  STAMP(11);
  }
  {
#pragma unroll
    for (int i = 0; i < 9; ++i) {
      const int L = i * 512 + t, r = L / 72, c = L - r * 72;
      const int pc = (((c >> 1) ^ ((r >> 1) & 3)) << 1) | (c & 1);
      *(u32x4*)(lds + L_WO + (r * 72 + pc) * 16) = wo[i];
    }
  }
  {
    if (wave == 0) wait_flags(ws, WS_B3FLAG + lane * 8, true, tag);
    BAR();
  STAMP(12);
    const int rg = lane >> 3, ch = lane & 7;
    u32x4 g[8];
#pragma unroll
    for (int i = 0; i < 8; ++i) g[i] = ld128(wsr, WS_BPART + (((rg + 8 * i) * 32 + hh) * 512 + 64 * wave + 8 * ch) * 2);
    float wgt;
    {
      const u32x2 ml = ld64(wsr, WS_BML + (lane * 32 + hh) * 8);
      const float m = __uint_as_float(ml[0]), l = __uint_as_float(ml[1]);
      float M = m;
      M = fmaxf(M, dppf<0x128>(M)); M = fmaxf(M, dppf<0x124>(M)); M = fmaxf(M, dppf<0x122>(M)); M = fmaxf(M, dppf<0x121>(M)); M = xmax16(M);
      const float w = ex2((m - M) * L2E);
      const float L = xsum16(row16_sum(w * l));
      wgt = w / L;
    }
    float s0[8], s1[8];
#pragma unroll
    for (int k = 0; k < 8; ++k) { s0[k] = 0.f; s1[k] = 0.f; }
#pragma unroll
    for (int i = 0; i < 4; ++i) {
      const float w0 = __shfl(wgt, rg + 8 * i), w1 = __shfl(wgt, 32 + rg + 8 * i);
#pragma unroll
      for (int k = 0; k < 4; ++k) {
        s0[2 * k] = fmaf(w0, bflo(g[i][k]), s0[2 * k]); s0[2 * k + 1] = fmaf(w0, bfhi(g[i][k]), s0[2 * k + 1]);
        s1[2 * k] = fmaf(w1, bflo(g[i + 4][k]), s1[2 * k]); s1[2 * k + 1] = fmaf(w1, bfhi(g[i + 4][k]), s1[2 * k + 1]);
      }
    }
#pragma unroll
    for (int k = 0; k < 8; ++k) {
      float x = s0[k]; x += dppf<0x128>(x); x = xsum16(x); x = xsum32(x); s0[k] = x;
      float y = s1[k]; y += dppf<0x128>(y); y = xsum16(y); y = xsum32(y); s1[k] = y;
    }
  STAMP(13);
    unsigned char* ca = lds + L_CA + wave * 512;
    if (lane < 8) {
      u32x4 h0, l0, h1, l1;
#pragma unroll
      for (int k = 0; k < 4; ++k) {
        const float a = bfr(s0[2 * k]), bq = bfr(s0[2 * k + 1]), c = bfr(s1[2 * k]), d = bfr(s1[2 * k + 1]);
        h0[k] = pack_bf16(a, bq); l0[k] = pack_bf16(s0[2 * k] - a, s0[2 * k + 1] - bq);
        h1[k] = pack_bf16(c, d); l1[k] = pack_bf16(s1[2 * k] - c, s1[2 * k + 1] - d);
      }
      *(u32x4*)(ca + 0 * 128 + lane * 16) = h0; *(u32x4*)(ca + 1 * 128 + lane * 16) = l0;
      *(u32x4*)(ca + 2 * 128 + lane * 16) = h1; *(u32x4*)(ca + 3 * 128 + lane * 16) = l1;
    }
    LGKM0();
    bf16x8 af[2];
#pragma unroll
    for (int ks = 0; ks < 2; ++ks) {
      u32x2 lo = {0u, 0u}, hq = {0u, 0u};
      if (i16 < 4) {
        lo = *(const u32x2*)(ca + i16 * 128 + (32 * ks + 4 * g4) * 2);
        hq = *(const u32x2*)(ca + i16 * 128 + (32 * ks + 16 + 4 * g4) * 2);
      }
      af[ks] = __builtin_bit_cast(bf16x8, u32x4{lo[0], lo[1], hq[0], hq[1]});
    }
    float* red2 = (float*)(lds + L_RED2);
    {
      const int r0 = 4 * g4 + qq, sw = (r0 >> 1) & 3;
#pragma unroll
      for (int nb = 0; nb < 4; ++nb) {
        f32x4 d = {0.f, 0.f, 0.f, 0.f};
        const unsigned char* base = lds + L_WUV + (64 * wave + r0) * 128 + ((nb ^ sw) * 32) + pp * 8;
#pragma unroll
        for (int ks = 0; ks < 2; ++ks) {
          const bf16x8 b = tr_pair(base + (32 * ks) * 128, base + (32 * ks + 16) * 128);
          d = __builtin_amdgcn_mfma_f32_16x16x32_bf16(af[ks], b, d, 0, 0, 0);
        }
        if (lane < 16) { red2[(wave * 2 + 0) * 64 + 16 * nb + lane] = d[0] + d[1]; red2[(wave * 2 + 1) * 64 + 16 * nb + lane] = d[2] + d[3]; }
      }
    }
    DRAIN();
    BAR();
    if (t < 64) {
      float2 s = *(const float2*)(red2 + t * 2);
#pragma unroll
      for (int w = 1; w < 8; ++w) { const float2 v = *(const float2*)(red2 + w * 128 + t * 2); s.x += v.x; s.y += v.y; }
      *(u32*)(lds + L_SCR + 2048 + t * 4) = pack_bf16(s.x, s.y);
    }
    BAR();
  }
  STAMP(14);
  proj_mfma(lds, L_WO, wsr, WS_OUTB + j * 9216, tag, wave, lane);
  if (j < 36) final_reduce(lds, wsr, WS_OUTB, tag, j, t, p.out, 576);
  STAMP(15);
  STAMP_FLUSH();
}

extern "C" void kernel_launch(void* const* d_in, const int* in_sizes, int n_in, void* d_out, int out_size, void* d_ws, size_t ws_size,
                              hipStream_t stream) {
  Params p;
  p.Ax = (const u16*)d_in[0]; p.Ak = (const u16*)d_in[1]; p.Av = (const u16*)d_in[2]; p.Awq = (const u16*)d_in[3]; p.Awo = (const u16*)d_in[4];
  p.Bx = (const u16*)d_in[5]; p.Bc = (const u16*)d_in[6]; p.Bwq = (const u16*)d_in[7]; p.Bwuk = (const u16*)d_in[8]; p.Bwuv = (const u16*)d_in[9];
  p.Bwo = (const u16*)d_in[10];
  p.out = (u16*)d_out; p.ws = (unsigned char*)d_ws;
  fused_kernel<<<dim3(NWG), dim3(NT), 0, stream>>>(p);
}
```

```cpp
#define PROBE_A 99
#define PROBE_C 99
#define PROBE_B 99
#define PROBE_MUL 0
#include <hip/hip_runtime.h>
#include <stdint.h>

typedef unsigned short u16;
typedef unsigned int u32;
typedef unsigned long long u64;
typedef u32 u32x4 __attribute__((ext_vector_type(4)));
typedef u32 u32x2 __attribute__((ext_vector_type(2)));
typedef float f32x4 __attribute__((ext_vector_type(4)));
typedef float f32x16 __attribute__((ext_vector_type(16)));
typedef __bf16 bf16x2 __attribute__((ext_vector_type(2)));
typedef __bf16 bf16x8 __attribute__((ext_vector_type(8)));
typedef short s16x4 __attribute__((ext_vector_type(4)));
typedef short s16x8 __attribute__((ext_vector_type(8)));
#define GAS __attribute__((address_space(1)))
#define LAS __attribute__((address_space(3)))

#define GSLEEP 1
#define FSLEEP 1
constexpr int NT = 512;
constexpr int NWG = 256;
constexpr int ASPLIT = 6;
constexpr float SCALE = 0.08838834764831845f;
constexpr float L2E = 1.4426950408889634f;
constexpr int SPIN_MAX = 300000;

constexpr u32 WS_EPOCH = 0;
constexpr u32 WS_B2FLAG = 256;
constexpr u32 WS_B3FLAG = WS_B2FLAG + 512;
constexpr u32 WS_QG = 4096;
constexpr u32 WS_QH = WS_QG + 65536;
constexpr u32 WS_QL = WS_QH + 65536;
constexpr u32 WS_BML = WS_QL + 65536;
constexpr u32 WS_OUTB = WS_BML + 16384;
constexpr u32 WS_OUTA = WS_OUTB + 589824;
constexpr u32 WS_APG = WS_OUTA + 589824;
constexpr u32 WS_BPART = WS_APG + 401408;
constexpr u32 WS_PROBE = WS_BPART + 2097152;
constexpr u32 WS_END = WS_PROBE + 4096;

constexpr int CSTR = 1040;
constexpr int L_C = 0;
constexpr int L_WUV = 0;
constexpr int L_QH = 65536;
constexpr int L_QL = 98816;
constexpr int L_OT = 65536;
constexpr int L_WO = 65536;
constexpr int L_CA = 139264;
constexpr int L_RED2 = 143360;
constexpr int L_XB = 133120;
constexpr int L_P = 149504;
constexpr int L_ST = 158208;
constexpr int L_SCR = 159744;
constexpr int L_MRG = 81920;
constexpr int LDS_BYTES = 163840;

struct Params {
  const u16* Ax; const u16* Ak; const u16* Av; const u16* Awq; const u16* Awo;
  const u16* Bx; const u16* Bc; const u16* Bwq; const u16* Bwuk; const u16* Bwuv; const u16* Bwo;
  u16* out; unsigned char* ws;
};

__device__ __forceinline__ float bf2f(u16 v) { return __uint_as_float(((u32)v) << 16); }
__device__ __forceinline__ float bflo(u32 v) { return __uint_as_float(v << 16); }
__device__ __forceinline__ float bfhi(u32 v) { return __uint_as_float(v & 0xffff0000u); }
__device__ __forceinline__ float bfr(float f) { return (float)(__bf16)f; }
__device__ __forceinline__ u32 pack_bf16(float lo, float hi) {
  bf16x2 v = {(__bf16)lo, (__bf16)hi};
  return __builtin_bit_cast(u32, v);
}
__device__ __forceinline__ float dot2(u32 a, u32 b, float c) {
  return __builtin_amdgcn_fdot2_f32_bf16(__builtin_bit_cast(bf16x2, a), __builtin_bit_cast(bf16x2, b), c, false);
}
template <int CTRL> __device__ __forceinline__ float dppf(float x) {
  return __builtin_bit_cast(float, __builtin_amdgcn_mov_dpp(__builtin_bit_cast(int, x), CTRL, 0xf, 0xf, true));
}
__device__ __forceinline__ float row16_sum(float x) {
  x += dppf<0x128>(x); x += dppf<0x124>(x); x += dppf<0x122>(x); x += dppf<0x121>(x);
  return x;
}
__device__ __forceinline__ float xsum16(float x) { auto r = __builtin_amdgcn_permlane16_swap(__float_as_uint(x), __float_as_uint(x), false, false); return __uint_as_float(r[0]) + __uint_as_float(r[1]); }
__device__ __forceinline__ float xsum32(float x) { auto r = __builtin_amdgcn_permlane32_swap(__float_as_uint(x), __float_as_uint(x), false, false); return __uint_as_float(r[0]) + __uint_as_float(r[1]); }
__device__ __forceinline__ float xmax16(float x) { auto r = __builtin_amdgcn_permlane16_swap(__float_as_uint(x), __float_as_uint(x), false, false); return fmaxf(__uint_as_float(r[0]), __uint_as_float(r[1])); }
__device__ __forceinline__ float xmax32(float x) { auto r = __builtin_amdgcn_permlane32_swap(__float_as_uint(x), __float_as_uint(x), false, false); return fmaxf(__uint_as_float(r[0]), __uint_as_float(r[1])); }
__device__ __forceinline__ float ex2(float x) { return __builtin_amdgcn_exp2f(x); }

typedef __amdgpu_buffer_rsrc_t rsrc_t;
__device__ __forceinline__ u32x4 ld128(rsrc_t r, u32 off) { return __builtin_amdgcn_raw_buffer_load_b128(r, off, 0, 16); }
__device__ __forceinline__ u32x2 ld64(rsrc_t r, u32 off) { return __builtin_amdgcn_raw_buffer_load_b64(r, off, 0, 16); }
__device__ __forceinline__ void st128(rsrc_t r, u32 off, u32x4 v) { __builtin_amdgcn_raw_buffer_store_b128(v, r, off, 0, 16); }
__device__ __forceinline__ void st64(rsrc_t r, u32 off, u32x2 v) { __builtin_amdgcn_raw_buffer_store_b64(v, r, off, 0, 16); }
__device__ __forceinline__ u32x4 ldnt(const u16* p) { return __builtin_nontemporal_load((const u32x4*)p); }

__device__ __forceinline__ void flag_set(unsigned char* ws, u32 off, u64 tag) {
  __hip_atomic_store((GAS u64*)(ws + off), tag, __ATOMIC_RELAXED, __HIP_MEMORY_SCOPE_AGENT);
}
__device__ __forceinline__ u64 flag_get(unsigned char* ws, u32 off) {
  return __hip_atomic_load((GAS u64*)(ws + off), __ATOMIC_RELAXED, __HIP_MEMORY_SCOPE_AGENT);
}
#define DRAIN() asm volatile("s_waitcnt vmcnt(0)" ::: "memory")
#define BAR() asm volatile("s_waitcnt lgkmcnt(0)\n\ts_barrier" ::: "memory")
#define LGKM0() asm volatile("s_waitcnt lgkmcnt(0)" ::: "memory")

__device__ __forceinline__ void glds16(const void* gsrc, u32 lds_dst) {
  u32 keep;
  asm volatile("s_mov_b32 %0, m0\n\ts_mov_b32 m0, %2\n\ts_nop 0\n\tglobal_load_lds_dwordx4 %1, off nt\n\ts_mov_b32 m0, %0"
               : "=&s"(keep) : "v"(gsrc), "s"(lds_dst) : "memory");
}

constexpr int CODE_BYTES = 40960;
__device__ __forceinline__ void code_prefetch(u64 pc0, int lane, u32 lds_dummy) {
#pragma unroll
  for (int i = 0; i < (CODE_BYTES + 8191) / 8192; ++i) {
    int off = i * 8192 + lane * 128; off = off < CODE_BYTES - 4 ? off : CODE_BYTES - 4;
    const unsigned char* g = (const unsigned char*)pc0 + off;
    u32 keep;
    asm volatile("s_mov_b32 %0, m0\n\ts_mov_b32 m0, %2\n\ts_nop 0\n\tglobal_load_lds_dword %1, off\n\ts_mov_b32 m0, %0"
                 : "=&s"(keep) : "v"(g), "s"(lds_dummy) : "memory");
  }
}
__device__ __forceinline__ void wait_flags(unsigned char* ws, u32 off, bool active, u64 tag) {
  for (int spin = 0; spin < SPIN_MAX; ++spin) {
    u64 v = tag;
    if (active) v = flag_get(ws, off);
    if (__all(v == tag)) break;
    __builtin_amdgcn_s_sleep(FSLEEP);
  }
  __builtin_amdgcn_fence(__ATOMIC_ACQUIRE, "wavefront");
}
__device__ __forceinline__ void gst(rsrc_t r, u32 off, u32 d0, u32 d1, u64 tag) { st128(r, off, u32x4{d0, d1, (u32)tag, (u32)(tag >> 32)}); }
__device__ __forceinline__ bool gok(u32x4 g, u64 tag) { return g[2] == (u32)tag && g[3] == (u32)(tag >> 32); }
template <int N> __device__ __forceinline__ void gpoll(rsrc_t r, const u32 (&off)[N], u32x4 (&g)[N], bool need, u64 tag) {
  for (int spin = 0; spin < SPIN_MAX; ++spin) {
    bool ok = true;
#pragma unroll
    for (int i = 0; i < N; ++i) { g[i] = ld128(r, off[i]); ok = ok && gok(g[i], tag); }
    if (__all(ok || !need)) break;
    __builtin_amdgcn_s_sleep(N > 1 ? GSLEEP : 1);
  }
}

#define STAMPS
#define STAMP(i) do { if ((threadIdx.x & 63) == 0) { stamps[(i)] = __builtin_amdgcn_s_memrealtime(); } } while (0)
#define STAMP_FLUSH() do { \
  const u64 tg_ = epoch + 1ull; \
  u64 sel_ = isB ? stamps[PROBE_B] : (slot < 16 ? stamps[PROBE_C] : stamps[PROBE_A]); if (PROBE_A == 99) sel_ = __builtin_amdgcn_s_memrealtime(); if (sel_ == 0) sel_ = stamps[0]; \
  if (threadIdx.x == 0) gst(wsr, WS_PROBE + blockIdx.x * 16, (u32)stamps[0], (u32)sel_, tg_); \
  if (blockIdx.x == 0 && threadIdx.x < 64) { \
    u32 off_[4] = {WS_PROBE + lane * 16u, WS_PROBE + (64u + lane) * 16u, WS_PROBE + (128u + lane) * 16u, WS_PROBE + (192u + lane) * 16u}; u32x4 g_[4]; \
    gpoll<4>(wsr, off_, g_, true, tg_); \
    const u32 ref_ = __builtin_amdgcn_readfirstlane(g_[0][0]); \
    int mn_ = 0x7fffffff, mx_ = -0x7fffffff; \
    for (int i_ = 0; i_ < 4; ++i_) { mn_ = min(mn_, (int)(g_[i_][0] - ref_)); mx_ = max(mx_, (int)(g_[i_][1] - ref_)); } \
    for (int o_ = 32; o_ >= 1; o_ >>= 1) { mn_ = min(mn_, __shfl_xor(mn_, o_)); mx_ = max(mx_, __shfl_xor(mx_, o_)); } \
    int d_ = (mx_ - mn_) * PROBE_MUL; if (d_ > 20000) d_ = 20000; \
    const u64 s0_ = __builtin_amdgcn_s_memrealtime(); \
    while ((long long)(__builtin_amdgcn_s_memrealtime() - s0_) < (long long)d_) __builtin_amdgcn_s_sleep(1); \
  } } while (0)

__device__ __forceinline__ bf16x8 tr_pair(const unsigned char* a0, const unsigned char* a1) {
  const s16x4 t0 = __builtin_amdgcn_ds_read_tr16_b64_v4i16((LAS s16x4*)a0);
  const s16x4 t1 = __builtin_amdgcn_ds_read_tr16_b64_v4i16((LAS s16x4*)a1);
  return __builtin_bit_cast(bf16x8, __builtin_shufflevector(t0, t1, 0, 1, 2, 3, 4, 5, 6, 7));
}

__device__ __forceinline__ void proj_mfma(unsigned char* lds, int wbase, rsrc_t wsr, u32 out_off, u64 tag, int wave, int lane) {
  const int g4 = lane >> 4, i16 = lane & 15, qq = i16 >> 2, pp = i16 & 3;
  bf16x8 af[2];
#pragma unroll
  for (int ks = 0; ks < 2; ++ks) {
    u32x2 lo = {0u, 0u}, hi = {0u, 0u};
    if (i16 < 2) {
      lo = *(const u32x2*)(lds + L_SCR + 2048 + i16 * 128 + (32 * ks + 4 * g4) * 2);
      hi = *(const u32x2*)(lds + L_SCR + 2048 + i16 * 128 + (32 * ks + 16 + 4 * g4) * 2);
    }
    af[ks] = __builtin_bit_cast(bf16x8, u32x4{lo[0], lo[1], hi[0], hi[1]});
  }
  const int r0 = 4 * g4 + qq;
  const int sw = (r0 >> 1) & 3;
#pragma unroll
  for (int i = 0; i < 5; ++i) {
    const int nb = wave + 8 * i;
    if (nb < 36) {
      f32x4 d = {0.f, 0.f, 0.f, 0.f};
      const unsigned char* base = lds + wbase + r0 * 1152 + ((nb ^ sw) * 32) + pp * 8;
#pragma unroll
      for (int ks = 0; ks < 2; ++ks) {
        const bf16x8 b = tr_pair(base + (32 * ks) * 1152, base + (32 * ks + 16) * 1152);
        d = __builtin_amdgcn_mfma_f32_16x16x32_bf16(af[ks], b, d, 0, 0, 0);
      }
      if (lane < 16) gst(wsr, out_off + (16 * nb + lane) * 16, __float_as_uint(d[0]), __float_as_uint(d[1]), tag);
    }
  }
}
__device__ __forceinline__ void final_reduce(unsigned char* lds, rsrc_t wsr, u32 part_off, u64 tag, int f, int t, u16* out, int base) {
  const int wave = t >> 6, lane = t & 63;
  const int pidx = t >> 3, sub = t & 7;
  u32 off[2] = {part_off + (u32)(pidx * 576 + 16 * f + sub) * 16, part_off + (u32)(pidx * 576 + 16 * f + 8 + sub) * 16};
  u32x4 g[2];
  gpoll<2>(wsr, off, g, true, tag);
  float v[4] = {__uint_as_float(g[0][0]), __uint_as_float(g[0][1]), __uint_as_float(g[1][0]), __uint_as_float(g[1][1])};
#pragma unroll
  for (int k = 0; k < 4; ++k) { float x = v[k]; x += dppf<0x128>(x); x = xsum16(x); x = xsum32(x); v[k] = x; }
  float* red = (float*)(lds + L_SCR);
  if (lane < 8) *(f32x4*)(red + wave * 32 + lane * 4) = f32x4{v[0], v[1], v[2], v[3]};
  BAR();
  if (t < 32) {
    float s = 0.f;
#pragma unroll
    for (int w = 0; w < 8; ++w) s += red[w * 32 + t];
    const int sb = t >> 2, k = t & 3, bb = k & 1, col = sb + 8 * (k >> 1);
    __bf16 o = (__bf16)s;
    out[bb * 1152 + base + 16 * f + col] = __builtin_bit_cast(u16, o);
  }
}

template <int NL, int RS>
__device__ __forceinline__ void q_load(const u16* qW, const u16* qx, int qcol0, int t, u32x4 (&qw)[NL], u16 (&qx0)[NL], u16 (&qx1)[NL]) {
  const int cch = t & 3, row0 = t >> 2;
#pragma unroll
  for (int i = 0; i < NL; ++i) {
    const int row = row0 + RS * i, rc = row < 576 ? row : 575;
    qx0[i] = qx[rc]; qx1[i] = qx[576 + rc];
  }
#pragma unroll
  for (int i = 0; i < NL; ++i) {
    const int row = row0 + RS * i, rc = row < 576 ? row : 575;
    qw[i] = ldnt(qW + (size_t)rc * 4096 + qcol0 + cch * 8);
  }
}
template <int NL, int RS>
__device__ __forceinline__ void q_fma(int t, const u32x4 (&qw)[NL], const u16 (&qx0)[NL], const u16 (&qx1)[NL], float (&qa0)[8], float (&qa1)[8]) {
#pragma unroll
  for (int k = 0; k < 8; ++k) { qa0[k] = 0.f; qa1[k] = 0.f; }
#pragma unroll
  for (int i = 0; i < NL; ++i) {
    const bool live = (t >> 2) + RS * i < 576;
    const float x0 = live ? bf2f(qx0[i]) : 0.f, x1 = live ? bf2f(qx1[i]) : 0.f;
#pragma unroll
    for (int k = 0; k < 4; ++k) {
      const float lo = bflo(qw[i][k]), hi = bfhi(qw[i][k]);
      qa0[2 * k] = fmaf(x0, lo, qa0[2 * k]); qa0[2 * k + 1] = fmaf(x0, hi, qa0[2 * k + 1]);
      qa1[2 * k] = fmaf(x1, lo, qa1[2 * k]); qa1[2 * k + 1] = fmaf(x1, hi, qa1[2 * k + 1]);
    }
  }
}
__device__ __forceinline__ void q_reduce(unsigned char* lds, int wave, int lane, float (&qa0)[8], float (&qa1)[8]) {
#pragma unroll
  for (int k = 0; k < 8; ++k) {
    float x = qa0[k]; x += dppf<0x124>(x); x += dppf<0x128>(x); x = xsum16(x); x = xsum32(x); qa0[k] = x;
    float y = qa1[k]; y += dppf<0x124>(y); y += dppf<0x128>(y); y = xsum16(y); y = xsum32(y); qa1[k] = y;
  }
  float* red_ = (float*)(lds + L_SCR);
  if (lane < 4) {
    float* r0 = red_ + wave * 64 + lane * 8;
    *(f32x4*)r0 = f32x4{qa0[0], qa0[1], qa0[2], qa0[3]}; *(f32x4*)(r0 + 4) = f32x4{qa0[4], qa0[5], qa0[6], qa0[7]};
    *(f32x4*)(r0 + 32) = f32x4{qa1[0], qa1[1], qa1[2], qa1[3]}; *(f32x4*)(r0 + 36) = f32x4{qa1[4], qa1[5], qa1[6], qa1[7]};
  }
}
template <int NW>
__device__ __forceinline__ void q_publish(unsigned char* lds, rsrc_t wsr, int qmat, int qcol0, int t, u64 tag) {
  if (t < 16) {
    const float* red_ = (const float*)(lds + L_SCR);
    f32x4 s_ = *(const f32x4*)(red_ + t * 4);
#pragma unroll
    for (int w = 1; w < NW; ++w) s_ += *(const f32x4*)(red_ + w * 64 + t * 4);
    gst(wsr, WS_QG + ((qmat * 2 + (t >> 3)) * 1024 + (qcol0 >> 2) + (t & 7)) * 16, pack_bf16(s_[0], s_[1]), pack_bf16(s_[2], s_[3]), tag);
  }
}

#define U3 48
#define LA3 1
#define LA2 1
#define ADEPTH 1
#define PREMODE 1
#define NPRE3 0
#define D1 0
#define D2 0
#define DELAY(n) do { _Pragma("unroll") for (int d_ = 0; d_ < (n); ++d_) __builtin_amdgcn_s_sleep(8); } while (0)
constexpr int U2 = (256 - 4 * U3) / 2;

#define A_LOAD(KK, VV, it) do { _Pragma("unroll") for (int i_ = 0; i_ < 4; ++i_) { KK[i_] = ldnt(Kb + (size_t)(it) * 16384 + i_ * 512); } \
                                _Pragma("unroll") for (int i_ = 0; i_ < 4; ++i_) { VV[i_] = ldnt(Vb + (size_t)(it) * 16384 + i_ * 512); } } while (0)
#define A_COMPUTE(KK, VV) do { \
      float s0_[4], s1_[4]; \
      _Pragma("unroll") for (int i_ = 0; i_ < 4; ++i_) { \
        float d0_ = 0.f, d1_ = 0.f; \
        _Pragma("unroll") for (int k_ = 0; k_ < 4; ++k_) { d0_ = dot2(KK[i_][k_], q0[k_], d0_); d1_ = dot2(KK[i_][k_], q1[k_], d1_); } \
        d0_ = row16_sum(d0_); d1_ = row16_sum(d1_); \
        s0_[i_] = bfr(bfr(d0_) * SCALE); s1_[i_] = bfr(bfr(d1_) * SCALE); } \
      const float mx0_ = fmaxf(fmaxf(s0_[0], s0_[1]), fmaxf(s0_[2], s0_[3])), mx1_ = fmaxf(fmaxf(s1_[0], s1_[1]), fmaxf(s1_[2], s1_[3])); \
      if (__any(mx0_ > m0 || mx1_ > m1)) { \
        const float n0_ = fmaxf(m0, mx0_), n1_ = fmaxf(m1, mx1_); \
        const float al0_ = ex2((m0 - n0_) * L2E), al1_ = ex2((m1 - n1_) * L2E); \
        l0 *= al0_; l1 *= al1_; m0 = n0_; m1 = n1_; \
        _Pragma("unroll") for (int k_ = 0; k_ < 8; ++k_) { acc0[k_] *= al0_; acc1[k_] *= al1_; } } \
      float p0_[4], p1_[4]; \
      _Pragma("unroll") for (int i_ = 0; i_ < 4; ++i_) { p0_[i_] = ex2((s0_[i_] - m0) * L2E); p1_[i_] = ex2((s1_[i_] - m1) * L2E); } \
      l0 += (p0_[0] + p0_[1]) + (p0_[2] + p0_[3]); l1 += (p1_[0] + p1_[1]) + (p1_[2] + p1_[3]); \
      const u32 pa0_ = pack_bf16(p0_[0], p0_[1]), pb0_ = pack_bf16(p0_[2], p0_[3]), pa1_ = pack_bf16(p1_[0], p1_[1]), pb1_ = pack_bf16(p1_[2], p1_[3]); \
      _Pragma("unroll") for (int k_ = 0; k_ < 4; ++k_) { \
        const u32 lo01_ = __builtin_amdgcn_perm(VV[1][k_], VV[0][k_], 0x05040100u), hi01_ = __builtin_amdgcn_perm(VV[1][k_], VV[0][k_], 0x07060302u); \
        const u32 lo23_ = __builtin_amdgcn_perm(VV[3][k_], VV[2][k_], 0x05040100u), hi23_ = __builtin_amdgcn_perm(VV[3][k_], VV[2][k_], 0x07060302u); \
        acc0[2 * k_] = dot2(lo23_, pb0_, dot2(lo01_, pa0_, acc0[2 * k_])); acc0[2 * k_ + 1] = dot2(hi23_, pb0_, dot2(hi01_, pa0_, acc0[2 * k_ + 1])); \
        acc1[2 * k_] = dot2(lo23_, pb1_, dot2(lo01_, pa1_, acc1[2 * k_])); acc1[2 * k_ + 1] = dot2(hi23_, pb1_, dot2(hi01_, pa1_, acc1[2 * k_ + 1])); } \
    } while (0)

#define WAITD() asm volatile("s_waitcnt vmcnt(0)" ::: "memory")
#define A_STEP(KK, VV, KN, VN, u) do { \
    if ((u) + LA < NU) { DRAIN(); A_LOAD(KN, VN, (u) + LA); } else if ((u) + LA == NU) { if (extra) { DRAIN(); A_LOAD(KN, VN, (u) + LA); } } \
    __builtin_amdgcn_sched_barrier(0); \
    if ((u) < NU) { A_COMPUTE(KK, VV); } else if ((u) == NU) { if (extra) A_COMPUTE(KK, VV); } \
    __builtin_amdgcn_sched_barrier(0); } while (0)
template <int NU, int LA>
__device__ __forceinline__ void a_stream(bool extra, bool poll, const u16* Kb, const u16* Vb, unsigned char* lds, rsrc_t wsr, u32 qoff, u64 tag, int lane,
                                         float& m0, float& m1, float& l0, float& l1, float (&acc0)[8], float (&acc1)[8],
                                         u32x4 (&K0)[4], u32x4 (&V0)[4], u32x4 (&K1)[4], u32x4 (&V1)[4]) {
  u32x4 K2[4], V2[4];
  if (LA == 2) { A_LOAD(K0, V0, 0); __builtin_amdgcn_sched_barrier(0); }
  if (poll) {
    u32 off[1] = {qoff};
    u32x4 g[1];
    gpoll<1>(wsr, off, g, true, tag);
    *(u32x2*)(lds + L_SCR + 2048 + lane * 8) = u32x2{g[0][0], g[0][1]};
  }
  if (LA == 2) { DRAIN(); A_LOAD(K1, V1, 1); } else { A_LOAD(K0, V0, 0); }
  BAR();
  const u32x4 q0 = *(const u32x4*)(lds + L_SCR + 2048 + (lane & 15) * 16), q1 = *(const u32x4*)(lds + L_SCR + 2048 + 256 + (lane & 15) * 16);
  if (LA == 1) {
    A_STEP(K0, V0, K1, V1, 0); A_STEP(K1, V1, K0, V0, 1); A_STEP(K0, V0, K1, V1, 2); A_STEP(K1, V1, K0, V0, 3);
    A_STEP(K0, V0, K1, V1, 4); A_STEP(K1, V1, K0, V0, 5); A_STEP(K0, V0, K1, V1, 6); A_STEP(K1, V1, K0, V0, 7);
  } else {
    A_STEP(K0, V0, K2, V2, 0); A_STEP(K1, V1, K0, V0, 1); A_STEP(K2, V2, K1, V1, 2); A_STEP(K0, V0, K2, V2, 3);
    A_STEP(K1, V1, K0, V0, 4); A_STEP(K2, V2, K1, V1, 5); A_STEP(K0, V0, K2, V2, 6); A_STEP(K1, V1, K0, V0, 7);
  }
}

__device__ __forceinline__ u32 touch_pages(const Params& p, int lane, const u16* Kb, const u16* Vb) {
  const unsigned char* a = (const unsigned char*)p.Ax;
  const int g = lane >> 2, k = lane & 3;
  const u32 big = k == 3 ? 0u : (u32)k << 21;
  if (g == 1) a = (const unsigned char*)p.Awq + big;
  if (g == 2) a = (const unsigned char*)p.Awo + big;
  if (g == 3) a = (const unsigned char*)p.Bwuv + (k == 2 ? 4194300u : big);
  if (g == 4) a = (const unsigned char*)p.Bwo + big;
  if (g == 5) a = p.ws + (k == 2 ? WS_END - 4 : big);
  if (g == 6) a = (const unsigned char*)p.out;
  if (g == 7) a = (const unsigned char*)(k & 1 ? Vb : Kb) + (k & 2 ? (u32)U2 * 2048u : 0u);
  u32 v = 0;
  if (lane < 32) v = *(const volatile u32*)a;
  return v;
}

__global__ void __launch_bounds__(NT, 2) fused_kernel(Params p) {
  __shared__ __attribute__((aligned(16))) unsigned char lds[LDS_BYTES];
  u64 pc0; asm volatile("s_getpc_b64 %0" : "=s"(pc0)); pc0 &= ~255ull;
  const int bid = blockIdx.x, t = threadIdx.x, lane = t & 63;
  const int wave = __builtin_amdgcn_readfirstlane(t >> 6);
  unsigned char* ws = p.ws;
  const rsrc_t wsr = __builtin_amdgcn_make_buffer_rsrc(ws, 0, WS_END, 0x00020000);
  const u32 ldsb = (u32)(uintptr_t)(LAS unsigned char*)lds;
  u64 stamps[100];
#pragma unroll
  for (int i = 0; i < 100; ++i) stamps[i] = 0;
  STAMP(0);
  u32 vz; asm volatile("v_mov_b32 %0, 0" : "=v"(vz));
  const u64 epoch = flag_get(ws + vz, WS_EPOCH);
  const int xcd = bid & 7, slot = bid >> 3;
  const bool isB = slot < 8;

  if (!isB) {
    const bool isR2 = slot < 16;
    const int r2 = (slot - 8) * 8 + xcd, r3 = (slot - 16) * 8 + xcd;
    const int pair = isR2 ? (r2 >> 1) : (r3 >> 2), sub = isR2 ? 4 + (r2 & 1) : (r3 & 3);
    const int ai = pair * 6 + sub;
    const int b = pair >> 4, hkv = pair & 15;
    const int ustart = (isR2 ? 4 * U3 + (sub - 4) * U2 : sub * U3) + (7 - wave);
    const u16* Kb = p.Ak + (size_t)pair * 4096 * 128 + (size_t)ustart * 2048 + (lane >> 4) * 128 + (lane & 15) * 8;
    const u16* Vb = p.Av + (size_t)pair * 4096 * 128 + (size_t)ustart * 2048 + (lane >> 4) * 128 + (lane & 15) * 8;
    const u32 qoff = WS_QG + (u32)((0 * 2 + b) * 1024 + (2 * hkv) * 32 + lane) * 16;
    float m0 = -INFINITY, m1 = -INFINITY, l0 = 0.f, l1 = 0.f;
    float acc0[8], acc1[8];
#pragma unroll
    for (int k = 0; k < 8; ++k) { acc0[k] = 0.f; acc1[k] = 0.f; }
    u64 tag;
    u32 tch = 0;
    u32x4 K0[4], V0[4], K1[4], V1[4];
    if (!isR2) {
      const int qc = (((slot - 16) >> 1) << 4) | (xcd << 1) | ((slot - 16) & 1);
      const int qcol0 = qc * 32;
      u32x4 qwB[5], qwA[5]; u16 bx0[5], bx1[5], ax0[5], ax1[5];
      q_load<5, 128>(p.Bwq, p.Bx, qcol0, t, qwB, bx0, bx1);
      if (wave == 7) tch = touch_pages(p, lane, Kb, Vb);
      __builtin_amdgcn_sched_barrier(0);
      float qa0[8], qa1[8];
      q_fma<5, 128>(t, qwB, bx0, bx1, qa0, qa1);
      __builtin_amdgcn_sched_barrier(0);
      q_load<5, 128>(p.Awq, p.Ax, qcol0, t, qwA, ax0, ax1);
      if (NPRE3 > 0) A_LOAD(K0, V0, 0);
      if (NPRE3 > 1) A_LOAD(K1, V1, 1);
      __builtin_amdgcn_sched_barrier(0);
      q_reduce(lds, wave, lane, qa0, qa1);
      tag = epoch + 1ull;
      BAR();
      q_publish<8>(lds, wsr, 1, qcol0, t, tag);
      STAMP(1);
      q_fma<5, 128>(t, qwA, ax0, ax1, qa0, qa1);
      q_reduce(lds - 4096, wave, lane, qa0, qa1);
      BAR();
      q_publish<8>(lds - 4096, wsr, 0, qcol0, t, tag);
      STAMP(2);
      constexpr int NLO = U3 / 8, WSPLIT = 8 - (U3 - NLO * 8);
      a_stream<NLO, LA3>(wave >= WSPLIT, wave == 0, Kb, Vb, lds, wsr, qoff, tag, lane, m0, m1, l0, l1, acc0, acc1, K0, V0, K1, V1);
    } else {
      const int hh = r2 >> 1, half = r2 & 1;
      u32x4 wk[8];
      DELAY(D2);
#pragma unroll
      for (int i = 0; i < 8; ++i) wk[i] = ldnt(p.Bwuk + ((size_t)(256 * half + (t >> 4) + 32 * i) * 32 + hh) * 128 + (t & 15) * 8);
      if (wave == 7) tch = touch_pages(p, lane, Kb, Vb);
      tag = epoch + 1ull;
      if (wave == 0) {
        u32 off[1] = {WS_QG + (u32)((1 * 2 + (lane >> 5)) * 1024 + hh * 32 + (lane & 31)) * 16};
        u32x4 g[1];
        gpoll<1>(wsr, off, g, true, tag);
        *(u32x2*)(lds + L_ST + lane * 8) = u32x2{g[0][0], g[0][1]};
      }
      BAR();
      STAMP(1);
      const u32x4 qa = *(const u32x4*)(lds + L_ST + (t & 15) * 16), qb = *(const u32x4*)(lds + L_ST + 256 + (t & 15) * 16);
      float* qs = (float*)(lds + L_SCR);
#pragma unroll
      for (int i = 0; i < 8; ++i) {
        float d0 = 0.f, d1 = 0.f;
#pragma unroll
        for (int k = 0; k < 4; ++k) { d0 = dot2(wk[i][k], qa[k], d0); d1 = dot2(wk[i][k], qb[k], d1); }
        d0 = row16_sum(d0); d1 = row16_sum(d1);
        if ((t & 15) == 0) { qs[(t >> 4) + 32 * i] = d0; qs[256 + (t >> 4) + 32 * i] = d1; }
      }
      BAR();
      if (wave == 0) {
        const int bb = lane >> 5, c8 = lane & 31;
        u32x4 hi, lo;
#pragma unroll
        for (int k = 0; k < 4; ++k) {
          const float v0 = qs[bb * 256 + c8 * 8 + 2 * k], v1 = qs[bb * 256 + c8 * 8 + 2 * k + 1];
          const float h0 = bfr(v0), h1 = bfr(v1);
          hi[k] = pack_bf16(h0, h1); lo[k] = pack_bf16(v0 - h0, v1 - h1);
        }
        const u32 o = ((bb * 32 + hh) * 512 + 256 * half + c8 * 8) * 2;
        st128(wsr, WS_QH + o, hi); st128(wsr, WS_QL + o, lo);
        DRAIN();
        if (lane == 0) flag_set(ws, WS_B2FLAG + r2 * 8, tag);
      }
      BAR();
      STAMP(2);
      {
        const unsigned char* src = (const unsigned char*)(p.Awo + (size_t)r2 * 64 * 576);
        for (int i = 0; i < 9; ++i) {
          const int L = i * 512 + t, r = L / 72, pc = L - r * 72;
          const int c = (((pc >> 1) ^ ((r >> 1) & 3)) << 1) | (pc & 1);
          glds16(src + r * 1152 + c * 16, ldsb + (u32)(i * 512 + wave * 64) * 16);
        }
      }
      constexpr int NLO = U2 / 8, WSPLIT = 8 - (U2 - NLO * 8);
      a_stream<NLO, LA2>(wave >= WSPLIT, wave == 1, Kb, Vb, lds, wsr, qoff, tag, lane, m0, m1, l0, l1, acc0, acc1, K0, V0, K1, V1);
    }
    asm volatile("" :: "v"(tch));
    STAMP(3);
    float* mrg = (float*)(lds + L_MRG);
    float* mml = (float*)(lds + L_ST);
    {
      const float M0 = xmax32(xmax16(m0)), M1 = xmax32(xmax16(m1));
      const float a0 = ex2((m0 - M0) * L2E), a1 = ex2((m1 - M1) * L2E);
      l0 = xsum32(xsum16(l0 * a0)); l1 = xsum32(xsum16(l1 * a1));
#pragma unroll
      for (int k = 0; k < 8; ++k) { acc0[k] = xsum32(xsum16(acc0[k] * a0)); acc1[k] = xsum32(xsum16(acc1[k] * a1)); }
      if (lane < 16) {
        float* r0 = mrg + (wave * 2 + 0) * 128 + lane * 8; float* r1 = mrg + (wave * 2 + 1) * 128 + lane * 8;
        *(f32x4*)r0 = f32x4{acc0[0], acc0[1], acc0[2], acc0[3]}; *(f32x4*)(r0 + 4) = f32x4{acc0[4], acc0[5], acc0[6], acc0[7]};
        *(f32x4*)r1 = f32x4{acc1[0], acc1[1], acc1[2], acc1[3]}; *(f32x4*)(r1 + 4) = f32x4{acc1[4], acc1[5], acc1[6], acc1[7]};
        if (lane == 0) *(f32x4*)(mml + wave * 4) = f32x4{M0, l0, M1, l1};
      }
    }
    BAR();
    if (t < 128) {
      const int g = t >> 6, k = t & 63;
      float M = -INFINITY;
#pragma unroll
      for (int r = 0; r < 8; ++r) M = fmaxf(M, mml[r * 4 + g * 2]);
      float L = 0.f, o0 = 0.f, o1 = 0.f;
#pragma unroll
      for (int r = 0; r < 8; ++r) {
        const float w = ex2((mml[r * 4 + g * 2] - M) * L2E); L = fmaf(w, mml[r * 4 + g * 2 + 1], L);
        const float2 v = *(const float2*)(mrg + (r * 2 + g) * 128 + 2 * k);
        o0 = fmaf(w, v.x, o0); o1 = fmaf(w, v.y, o1);
      }
      gst(wsr, WS_APG + ((ai * 2 + g) * 65 + k) * 16, __float_as_uint(o0), __float_as_uint(o1), tag);
      if (k == 0) gst(wsr, WS_APG + ((ai * 2 + g) * 65 + 64) * 16, __float_as_uint(M), __float_as_uint(L), tag);
    }
    STAMP(4);
    if (isR2) {
      const int hq = r2 >> 1, dh = r2 & 1, thkv = hq >> 1, g = hq & 1;
      if (wave == 0) {
        const int bb = lane >> 5, k = lane & 31;
        const int a0i = (bb * 16 + thkv) * 6;
        u32 off[12]; u32x4 gg[12];
#pragma unroll
        for (int s = 0; s < 6; ++s) { off[s] = WS_APG + (((a0i + s) * 2 + g) * 65 + 32 * dh + k) * 16; off[6 + s] = WS_APG + (((a0i + s) * 2 + g) * 65 + 64) * 16; }
        gpoll<12>(wsr, off, gg, true, tag);
        float M = __uint_as_float(gg[6][0]);
#pragma unroll
        for (int s = 1; s < 6; ++s) M = fmaxf(M, __uint_as_float(gg[6 + s][0]));
        float L = 0.f, o0 = 0.f, o1 = 0.f;
#pragma unroll
        for (int s = 0; s < 6; ++s) {
          const float w = ex2((__uint_as_float(gg[6 + s][0]) - M) * L2E);
          L = fmaf(w, __uint_as_float(gg[6 + s][1]), L); o0 = fmaf(w, __uint_as_float(gg[s][0]), o0); o1 = fmaf(w, __uint_as_float(gg[s][1]), o1);
        }
        *(u32*)(lds + L_SCR + 2048 + bb * 128 + k * 4) = pack_bf16(o0 / L, o1 / L);
      }
      DRAIN();
      BAR();
      STAMP(5);
      proj_mfma(lds, 0, wsr, WS_OUTA + r2 * 9216, tag, wave, lane);
      STAMP(6);
    } else if (r3 < 36) {
      final_reduce(lds, wsr, WS_OUTA, tag, r3, t, p.out, 0);
      if (r3 == 0) {
        if (wave == 0) { wait_flags(ws, WS_B3FLAG + lane * 8, true, tag); if (lane == 0) flag_set(ws, WS_EPOCH, tag); }
      }
      STAMP(7);
    }
    STAMP_FLUSH();
    return;
  }

  const int j = slot * 8 + xcd;
  const int hh = j >> 1, half = j & 1;
  const int b3b = j >> 5, b3ks = j & 31;
  const int kb = wave & 3, kh = wave >> 2;
  {
    DELAY(D1);
    const unsigned char* src = (const unsigned char*)(p.Bc + ((size_t)b3b * 4096 + 128 * b3ks + 16 * wave) * 512) + lane * 16;
#pragma unroll
    for (int r = 0; r < 16; ++r) glds16(src + r * 1024, ldsb + L_C + (u32)(16 * wave + r) * CSTR);
    if (wave == 7) code_prefetch(pc0, lane, ldsb + L_ST + 1024);
    if (wave == 6) { const u32 tch = touch_pages(p, lane, p.Ak, p.Av); asm volatile("" :: "v"(tch)); }
    DRAIN();
  }
  BAR();
  STAMP(5);
  const u64 tag = epoch + 1ull;
  const int head = lane & 31, hi = lane >> 5;
  const int g4 = lane >> 4, i16 = lane & 15, qq = i16 >> 2, pp = i16 & 3;
  u32x4 cf[16];
  bf16x8 pv[2][8];
  {
    const int pi = 16 * (head >> 4) + 4 * (head & 3) + 2 * ((head >> 3) & 1) + ((head >> 2) & 1);
    const unsigned char* cr = lds + L_C + (32 * kb + pi) * CSTR + (256 * kh + 8 * hi) * 2;
#pragma unroll
    for (int s = 0; s < 16; ++s) cf[s] = *(const u32x4*)(cr + s * 32);
    const unsigned char* tb = lds + L_C + (4 * qq + 2 * hi) * CSTR + (64 * wave + 16 * (g4 & 1) + 4 * pp) * 2;
#pragma unroll
    for (int ks = 0; ks < 8; ++ks) {
      pv[0][ks] = tr_pair(tb + (16 * ks) * CSTR, tb + (16 * ks + 1) * CSTR);
      pv[1][ks] = tr_pair(tb + (16 * ks) * CSTR + 64, tb + (16 * ks + 1) * CSTR + 64);
    }
  }
  STAMP(4);
  BAR();
  {
    const unsigned char* src = (const unsigned char*)(p.Bwuv + (size_t)hh * 128 + 64 * half);
#pragma unroll
    for (int i = 0; i < 8; ++i) {
      const int L = i * 512 + t, r = L >> 3, pc = L & 7;
      const int c = (((pc >> 1) ^ ((r >> 1) & 3)) << 1) | (pc & 1);
      glds16(src + (size_t)r * 8192 + c * 16, ldsb + L_WUV + (u32)(i * 512 + wave * 64) * 16);
    }
  }
  u32x4 wo[9];
#pragma unroll
  for (int i = 0; i < 9; ++i) wo[i] = ldnt(p.Bwo + (size_t)j * 64 * 576 + (size_t)(i * 512 + t) * 8);
  __builtin_amdgcn_sched_barrier(0);
  wait_flags(ws, WS_B2FLAG + (wave * 8 + (lane & 7)) * 8, true, tag);
  STAMP(6);
  {
    u32x4 vh[4], vl[4];
#pragma unroll
    for (int i = 0; i < 4; ++i) {
      vh[i] = ld128(wsr, WS_QH + b3b * 32768 + ((wave * 4 + i) * 64 + lane) * 16);
      vl[i] = ld128(wsr, WS_QL + b3b * 32768 + ((wave * 4 + i) * 64 + lane) * 16);
    }
#pragma unroll
    for (int i = 0; i < 4; ++i) {
      *(u32x4*)(lds + L_QH + (wave * 4 + i) * CSTR + lane * 16) = vh[i];
      *(u32x4*)(lds + L_QL + (wave * 4 + i) * CSTR + lane * 16) = vl[i];
    }
  }
  BAR();
  STAMP(7);
  {
    float* smax = (float*)(lds + L_ST);
    float* slsum = smax + 128;
    f32x16 sacc;
#pragma unroll
    for (int r = 0; r < 16; ++r) sacc[r] = 0.f;
    {
      const unsigned char* bhp = lds + L_QH + head * CSTR + (256 * kh + 8 * hi) * 2;
      const unsigned char* blp = lds + L_QL + head * CSTR + (256 * kh + 8 * hi) * 2;
#pragma unroll
      for (int s = 0; s < 16; ++s) {
        const bf16x8 bh = __builtin_bit_cast(bf16x8, *(const u32x4*)(bhp + s * 32));
        const bf16x8 bl = __builtin_bit_cast(bf16x8, *(const u32x4*)(blp + s * 32));
        sacc = __builtin_amdgcn_mfma_f32_32x32x16_bf16(__builtin_bit_cast(bf16x8, cf[s]), bh, sacc, 0, 0, 0);
        sacc = __builtin_amdgcn_mfma_f32_32x32x16_bf16(__builtin_bit_cast(bf16x8, cf[s]), bl, sacc, 0, 0, 0);
      }
    }
  STAMP(8);
    if (kh == 1) {
#pragma unroll
      for (int r4 = 0; r4 < 4; ++r4)
        *(f32x4*)(lds + L_XB + ((kb * 4 + r4) * 64 + lane) * 16) = f32x4{sacc[4 * r4], sacc[4 * r4 + 1], sacc[4 * r4 + 2], sacc[4 * r4 + 3]};
    }
    BAR();
    float sv[16];
    if (kh == 0) {
      float mx = -INFINITY;
#pragma unroll
      for (int r4 = 0; r4 < 4; ++r4) {
        const f32x4 o = *(const f32x4*)(lds + L_XB + ((kb * 4 + r4) * 64 + lane) * 16);
#pragma unroll
        for (int e = 0; e < 4; ++e) { sv[4 * r4 + e] = bfr(bfr(sacc[4 * r4 + e] + o[e]) * SCALE); mx = fmaxf(mx, sv[4 * r4 + e]); }
      }
      mx = xmax32(mx);
      if (lane < 32) smax[kb * 32 + head] = mx;
    }
    BAR();
    if (kh == 0) {
      const float m = fmaxf(fmaxf(smax[head], smax[32 + head]), fmaxf(smax[64 + head], smax[96 + head]));
      float ls = 0.f;
#pragma unroll
      for (int r4 = 0; r4 < 4; ++r4) {
        float pe[4];
#pragma unroll
        for (int e = 0; e < 4; ++e) { pe[e] = ex2((sv[4 * r4 + e] - m) * L2E); ls += pe[e]; }
        *(u32x2*)(lds + L_P + head * 272 + (32 * kb + 8 * r4 + 4 * hi) * 2) = u32x2{pack_bf16(pe[0], pe[1]), pack_bf16(pe[2], pe[3])};
      }
      ls = xsum32(ls);
      if (lane < 32) slsum[kb * 32 + head] = ls;
    }
    BAR();
  STAMP(9);
    f32x16 oacc0, oacc1;
#pragma unroll
    for (int r = 0; r < 16; ++r) { oacc0[r] = 0.f; oacc1[r] = 0.f; }
    {
      const unsigned char* pa = lds + L_P + head * 272 + hi * 16;
#pragma unroll
      for (int ks = 0; ks < 8; ++ks) {
        const bf16x8 a = __builtin_bit_cast(bf16x8, *(const u32x4*)(pa + ks * 32));
        oacc0 = __builtin_amdgcn_mfma_f32_32x32x16_bf16(pv[0][ks], a, oacc0, 0, 0, 0);
        oacc1 = __builtin_amdgcn_mfma_f32_32x32x16_bf16(pv[1][ks], a, oacc1, 0, 0, 0);
      }
    }
  STAMP(10);
    {
      unsigned char* ot = lds + L_OT + wave * 4608;
#pragma unroll
      for (int r4 = 0; r4 < 4; ++r4) {
        *(u32x2*)(ot + head * 144 + (8 * r4 + 4 * hi) * 2) = u32x2{pack_bf16(oacc0[4 * r4], oacc0[4 * r4 + 1]), pack_bf16(oacc0[4 * r4 + 2], oacc0[4 * r4 + 3])};
        *(u32x2*)(ot + head * 144 + (32 + 8 * r4 + 4 * hi) * 2) = u32x2{pack_bf16(oacc1[4 * r4], oacc1[4 * r4 + 1]), pack_bf16(oacc1[4 * r4 + 2], oacc1[4 * r4 + 3])};
      }
      LGKM0();
#pragma unroll
      for (int i = 0; i < 4; ++i) {
        const int hrow = (lane >> 3) + 8 * i, ch = lane & 7;
        const u32x4 v = *(const u32x4*)(ot + hrow * 144 + ch * 16);
        st128(wsr, WS_BPART + (((b3b * 32 + b3ks) * 32 + hrow) * 512 + 64 * wave + 8 * ch) * 2, v);
      }
    }
    if (t < 32) {
      const float L = (slsum[t] + slsum[32 + t]) + (slsum[64 + t] + slsum[96 + t]);
      const float m = fmaxf(fmaxf(smax[t], smax[32 + t]), fmaxf(smax[64 + t], smax[96 + t]));
      st64(wsr, WS_BML + ((b3b * 32 + b3ks) * 32 + t) * 8, u32x2{__float_as_uint(m), __float_as_uint(L)});
    }
    DRAIN();
    BAR();
    if (t == 0) flag_set(ws, WS_B3FLAG + j * 8, tag);
  STAMP(11);
  }
  {
#pragma unroll
    for (int i = 0; i < 9; ++i) {
      const int L = i * 512 + t, r = L / 72, c = L - r * 72;
      const int pc = (((c >> 1) ^ ((r >> 1) & 3)) << 1) | (c & 1);
      *(u32x4*)(lds + L_WO + (r * 72 + pc) * 16) = wo[i];
    }
  }
  {
    if (wave == 0) wait_flags(ws, WS_B3FLAG + lane * 8, true, tag);
    BAR();
  STAMP(12);
    const int rg = lane >> 3, ch = lane & 7;
    u32x4 g[8];
#pragma unroll
    for (int i = 0; i < 8; ++i) g[i] = ld128(wsr, WS_BPART + (((rg + 8 * i) * 32 + hh) * 512 + 64 * wave + 8 * ch) * 2);
    float wgt;
    {
      const u32x2 ml = ld64(wsr, WS_BML + (lane * 32 + hh) * 8);
      const float m = __uint_as_float(ml[0]), l = __uint_as_float(ml[1]);
      float M = m;
      M = fmaxf(M, dppf<0x128>(M)); M = fmaxf(M, dppf<0x124>(M)); M = fmaxf(M, dppf<0x122>(M)); M = fmaxf(M, dppf<0x121>(M)); M = xmax16(M);
      const float w = ex2((m - M) * L2E);
      const float L = xsum16(row16_sum(w * l));
      wgt = w / L;
    }
    float s0[8], s1[8];
#pragma unroll
    for (int k = 0; k < 8; ++k) { s0[k] = 0.f; s1[k] = 0.f; }
#pragma unroll
    for (int i = 0; i < 4; ++i) {
      const float w0 = __shfl(wgt, rg + 8 * i), w1 = __shfl(wgt, 32 + rg + 8 * i);
#pragma unroll
      for (int k = 0; k < 4; ++k) {
        s0[2 * k] = fmaf(w0, bflo(g[i][k]), s0[2 * k]); s0[2 * k + 1] = fmaf(w0, bfhi(g[i][k]), s0[2 * k + 1]);
        s1[2 * k] = fmaf(w1, bflo(g[i + 4][k]), s1[2 * k]); s1[2 * k + 1] = fmaf(w1, bfhi(g[i + 4][k]), s1[2 * k + 1]);
      }
    }
#pragma unroll
    for (int k = 0; k < 8; ++k) {
      float x = s0[k]; x += dppf<0x128>(x); x = xsum16(x); x = xsum32(x); s0[k] = x;
      float y = s1[k]; y += dppf<0x128>(y); y = xsum16(y); y = xsum32(y); s1[k] = y;
    }
  STAMP(13);
    unsigned char* ca = lds + L_CA + wave * 512;
    if (lane < 8) {
      u32x4 h0, l0, h1, l1;
#pragma unroll
      for (int k = 0; k < 4; ++k) {
        const float a = bfr(s0[2 * k]), bq = bfr(s0[2 * k + 1]), c = bfr(s1[2 * k]), d = bfr(s1[2 * k + 1]);
        h0[k] = pack_bf16(a, bq); l0[k] = pack_bf16(s0[2 * k] - a, s0[2 * k + 1] - bq);
        h1[k] = pack_bf16(c, d); l1[k] = pack_bf16(s1[2 * k] - c, s1[2 * k + 1] - d);
      }
      *(u32x4*)(ca + 0 * 128 + lane * 16) = h0; *(u32x4*)(ca + 1 * 128 + lane * 16) = l0;
      *(u32x4*)(ca + 2 * 128 + lane * 16) = h1; *(u32x4*)(ca + 3 * 128 + lane * 16) = l1;
    }
    LGKM0();
    bf16x8 af[2];
#pragma unroll
    for (int ks = 0; ks < 2; ++ks) {
      u32x2 lo = {0u, 0u}, hq = {0u, 0u};
      if (i16 < 4) {
        lo = *(const u32x2*)(ca + i16 * 128 + (32 * ks + 4 * g4) * 2);
        hq = *(const u32x2*)(ca + i16 * 128 + (32 * ks + 16 + 4 * g4) * 2);
      }
      af[ks] = __builtin_bit_cast(bf16x8, u32x4{lo[0], lo[1], hq[0], hq[1]});
    }
    float* red2 = (float*)(lds + L_RED2);
    {
      const int r0 = 4 * g4 + qq, sw = (r0 >> 1) & 3;
#pragma unroll
      for (int nb = 0; nb < 4; ++nb) {
        f32x4 d = {0.f, 0.f, 0.f, 0.f};
        const unsigned char* base = lds + L_WUV + (64 * wave + r0) * 128 + ((nb ^ sw) * 32) + pp * 8;
#pragma unroll
        for (int ks = 0; ks < 2; ++ks) {
          const bf16x8 b = tr_pair(base + (32 * ks) * 128, base + (32 * ks + 16) * 128);
          d = __builtin_amdgcn_mfma_f32_16x16x32_bf16(af[ks], b, d, 0, 0, 0);
        }
        if (lane < 16) { red2[(wave * 2 + 0) * 64 + 16 * nb + lane] = d[0] + d[1]; red2[(wave * 2 + 1) * 64 + 16 * nb + lane] = d[2] + d[3]; }
      }
    }
    DRAIN();
    BAR();
    if (t < 64) {
      float2 s = *(const float2*)(red2 + t * 2);
#pragma unroll
      for (int w = 1; w < 8; ++w) { const float2 v = *(const float2*)(red2 + w * 128 + t * 2); s.x += v.x; s.y += v.y; }
      *(u32*)(lds + L_SCR + 2048 + t * 4) = pack_bf16(s.x, s.y);
    }
    BAR();
  }
  STAMP(14);
  proj_mfma(lds, L_WO, wsr, WS_OUTB + j * 9216, tag, wave, lane);
  if (j < 36) final_reduce(lds, wsr, WS_OUTB, tag, j, t, p.out, 576);
  STAMP(15);
  STAMP_FLUSH();
}

extern "C" void kernel_launch(void* const* d_in, const int* in_sizes, int n_in, void* d_out, int out_size, void* d_ws, size_t ws_size,
                              hipStream_t stream) {
  Params p;
  p.Ax = (const u16*)d_in[0]; p.Ak = (const u16*)d_in[1]; p.Av = (const u16*)d_in[2]; p.Awq = (const u16*)d_in[3]; p.Awo = (const u16*)d_in[4];
  p.Bx = (const u16*)d_in[5]; p.Bc = (const u16*)d_in[6]; p.Bwq = (const u16*)d_in[7]; p.Bwuk = (const u16*)d_in[8]; p.Bwuv = (const u16*)d_in[9];
  p.Bwo = (const u16*)d_in[10];
  p.out = (u16*)d_out; p.ws = (unsigned char*)d_ws;
  fused_kernel<<<dim3(NWG), dim3(NT), 0, stream>>>(p);
}
```

```cpp
#include <hip/hip_runtime.h>
#include <stdint.h>
#define PMASK 8
#define NOMASK
#define DRYB 0
#define DRYC 0
#define EARLYWO 9
#define U3 44

typedef unsigned short u16;
typedef unsigned int u32;
typedef unsigned long long u64;
typedef u32 u32x4 __attribute__((ext_vector_type(4)));
typedef u32 u32x2 __attribute__((ext_vector_type(2)));
typedef float f32x4 __attribute__((ext_vector_type(4)));
typedef float f32x16 __attribute__((ext_vector_type(16)));
typedef __bf16 bf16x2 __attribute__((ext_vector_type(2)));
typedef __bf16 bf16x8 __attribute__((ext_vector_type(8)));
typedef short s16x4 __attribute__((ext_vector_type(4)));
typedef short s16x8 __attribute__((ext_vector_type(8)));
#define GAS __attribute__((address_space(1)))
#define LAS __attribute__((address_space(3)))

#define GSLEEP 1
#define FSLEEP 1
constexpr int NT = 512;
constexpr int NWG = 256;
constexpr int ASPLIT = 6;
constexpr float SCALE = 0.08838834764831845f;
constexpr float L2E = 1.4426950408889634f;
constexpr int SPIN_MAX = 300000;

constexpr u32 WS_EPOCH = 0;
constexpr u32 WS_B2FLAG = 256;
constexpr u32 WS_B3FLAG = WS_B2FLAG + 512;
constexpr u32 WS_QG = 4096;
constexpr u32 WS_QH = WS_QG + 65536;
constexpr u32 WS_QL = WS_QH + 65536;
constexpr u32 WS_BML = WS_QL + 65536;
constexpr u32 WS_OUTB = WS_BML + 16384;
constexpr u32 WS_OUTA = WS_OUTB + 589824;
constexpr u32 WS_APG = WS_OUTA + 589824;
constexpr u32 WS_BPART = WS_APG + 401408;
constexpr u32 WS_END = WS_BPART + 2097152;

constexpr int CSTR = 1040;
constexpr int L_C = 0;
constexpr int L_WUV = 0;
constexpr int L_QH = 65536;
constexpr int L_QL = 98816;
constexpr int L_OT = 65536;
constexpr int L_WO = 65536;
constexpr int L_CA = 139264;
constexpr int L_RED2 = 143360;
constexpr int L_XB = 133120;
constexpr int L_P = 149504;
constexpr int L_ST = 158208;
constexpr int L_SCR = 159744;
constexpr int L_MRG = 81920;
constexpr int LDS_BYTES = 163840;

struct Params {
  const u16* Ax; const u16* Ak; const u16* Av; const u16* Awq; const u16* Awo;
  const u16* Bx; const u16* Bc; const u16* Bwq; const u16* Bwuk; const u16* Bwuv; const u16* Bwo;
  u16* out; unsigned char* ws;
};

__device__ __forceinline__ float bf2f(u16 v) { return __uint_as_float(((u32)v) << 16); }
__device__ __forceinline__ float bflo(u32 v) { return __uint_as_float(v << 16); }
__device__ __forceinline__ float bfhi(u32 v) { return __uint_as_float(v & 0xffff0000u); }
__device__ __forceinline__ float bfr(float f) { return (float)(__bf16)f; }
__device__ __forceinline__ u32 pack_bf16(float lo, float hi) {
  bf16x2 v = {(__bf16)lo, (__bf16)hi};
  return __builtin_bit_cast(u32, v);
}
__device__ __forceinline__ float dot2(u32 a, u32 b, float c) {
  return __builtin_amdgcn_fdot2_f32_bf16(__builtin_bit_cast(bf16x2, a), __builtin_bit_cast(bf16x2, b), c, false);
}
template <int CTRL> __device__ __forceinline__ float dppf(float x) {
  return __builtin_bit_cast(float, __builtin_amdgcn_mov_dpp(__builtin_bit_cast(int, x), CTRL, 0xf, 0xf, true));
}
__device__ __forceinline__ float row16_sum(float x) {
  x += dppf<0x128>(x); x += dppf<0x124>(x); x += dppf<0x122>(x); x += dppf<0x121>(x);
  return x;
}
__device__ __forceinline__ float xsum16(float x) { auto r = __builtin_amdgcn_permlane16_swap(__float_as_uint(x), __float_as_uint(x), false, false); return __uint_as_float(r[0]) + __uint_as_float(r[1]); }
__device__ __forceinline__ float xsum32(float x) { auto r = __builtin_amdgcn_permlane32_swap(__float_as_uint(x), __float_as_uint(x), false, false); return __uint_as_float(r[0]) + __uint_as_float(r[1]); }
__device__ __forceinline__ float xmax16(float x) { auto r = __builtin_amdgcn_permlane16_swap(__float_as_uint(x), __float_as_uint(x), false, false); return fmaxf(__uint_as_float(r[0]), __uint_as_float(r[1])); }
__device__ __forceinline__ float xmax32(float x) { auto r = __builtin_amdgcn_permlane32_swap(__float_as_uint(x), __float_as_uint(x), false, false); return fmaxf(__uint_as_float(r[0]), __uint_as_float(r[1])); }
__device__ __forceinline__ float ex2(float x) { return __builtin_amdgcn_exp2f(x); }

typedef __amdgpu_buffer_rsrc_t rsrc_t;
__device__ __forceinline__ u32x4 ld128(rsrc_t r, u32 off) { return __builtin_amdgcn_raw_buffer_load_b128(r, off, 0, 16); }
__device__ __forceinline__ u32x2 ld64(rsrc_t r, u32 off) { return __builtin_amdgcn_raw_buffer_load_b64(r, off, 0, 16); }
__device__ __forceinline__ void st128(rsrc_t r, u32 off, u32x4 v) { __builtin_amdgcn_raw_buffer_store_b128(v, r, off, 0, 16); }
__device__ __forceinline__ void st64(rsrc_t r, u32 off, u32x2 v) { __builtin_amdgcn_raw_buffer_store_b64(v, r, off, 0, 16); }
__device__ __forceinline__ u32x4 ldnt(const u16* p) { return __builtin_nontemporal_load((const u32x4*)p); }

__device__ __forceinline__ void flag_set(unsigned char* ws, u32 off, u64 tag) {
  __hip_atomic_store((GAS u64*)(ws + off), tag, __ATOMIC_RELAXED, __HIP_MEMORY_SCOPE_AGENT);
}
__device__ __forceinline__ u64 flag_get(unsigned char* ws, u32 off) {
  return __hip_atomic_load((GAS u64*)(ws + off), __ATOMIC_RELAXED, __HIP_MEMORY_SCOPE_AGENT);
}
#define DRAIN() asm volatile("s_waitcnt vmcnt(0)" ::: "memory")
#define BAR() asm volatile("s_waitcnt lgkmcnt(0)\n\ts_barrier" ::: "memory")
#define LGKM0() asm volatile("s_waitcnt lgkmcnt(0)" ::: "memory")

__device__ __forceinline__ void glds16(const void* gsrc, u32 lds_dst) {
  u32 keep;
  asm volatile("s_mov_b32 %0, m0\n\ts_mov_b32 m0, %2\n\ts_nop 0\n\tglobal_load_lds_dwordx4 %1, off nt\n\ts_mov_b32 m0, %0"
               : "=&s"(keep) : "v"(gsrc), "s"(lds_dst) : "memory");
}

constexpr int CODE_BYTES = 40960;
__device__ __forceinline__ void code_prefetch(u64 pc0, int lane, u32 lds_dummy) {
#pragma unroll
  for (int i = 0; i < (CODE_BYTES + 8191) / 8192; ++i) {
    int off = i * 8192 + lane * 128; off = off < CODE_BYTES - 4 ? off : CODE_BYTES - 4;
    const unsigned char* g = (const unsigned char*)pc0 + off;
    u32 keep;
    asm volatile("s_mov_b32 %0, m0\n\ts_mov_b32 m0, %2\n\ts_nop 0\n\tglobal_load_lds_dword %1, off\n\ts_mov_b32 m0, %0"
                 : "=&s"(keep) : "v"(g), "s"(lds_dummy) : "memory");
  }
}
__device__ __forceinline__ void wait_flags(unsigned char* ws, u32 off, bool active, u64 tag) {
  for (int spin = 0; spin < SPIN_MAX; ++spin) {
    u64 v = tag;
    if (active) v = flag_get(ws, off);
    if (__all(v == tag)) break;
    __builtin_amdgcn_s_sleep(FSLEEP);
  }
  __builtin_amdgcn_fence(__ATOMIC_ACQUIRE, "wavefront");
}
__device__ __forceinline__ void gst(rsrc_t r, u32 off, u32 d0, u32 d1, u64 tag) { st128(r, off, u32x4{d0, d1, (u32)tag, (u32)(tag >> 32)}); }
__device__ __forceinline__ bool gok(u32x4 g, u64 tag) { return g[2] == (u32)tag && g[3] == (u32)(tag >> 32); }
template <int N> __device__ __forceinline__ void gpoll(rsrc_t r, const u32 (&off)[N], u32x4 (&g)[N], bool need, u64 tag) {
  for (int spin = 0; spin < SPIN_MAX; ++spin) {
    bool ok = true;
#pragma unroll
    for (int i = 0; i < N; ++i) { g[i] = ld128(r, off[i]); ok = ok && gok(g[i], tag); }
    if (__all(ok || !need)) break;
    __builtin_amdgcn_s_sleep(N > 1 ? GSLEEP : 1);
  }
}

#define PSLEEP 8
#define STR2(x) #x
#define STR(x) STR2(x)
#define PCHK1(A2, A3, LBL) \
    "v_mov_b32 %[t0], " A2 "\n\tv_mov_b32 %[t1], " A3 "\n\t" \
    "v_cmp_eq_u32 vcc, %[tlo], %[t0]\n\tv_cmp_eq_u32 %[sc], %[thi], %[t1]\n\t" \
    "s_and_b64 vcc, vcc, %[sc]\n\ts_cmp_eq_u64 vcc, exec\n\ts_cbranch_scc1 " LBL "\n\t"
__device__ __forceinline__ u32x2 gpoll1p(rsrc_t r, u32 off, u64 tag) {
  u32 d0, d1, t0, t1; u64 sc; int cnt = SPIN_MAX;
  const u32 tlo = __builtin_amdgcn_readfirstlane((u32)tag), thi = __builtin_amdgcn_readfirstlane((u32)(tag >> 32));
  asm volatile(
    "buffer_load_dwordx4 v[232:235], %[off], %[rs], 0 offen sc1\n\ts_sleep " STR(PSLEEP) "\n\t"
    "buffer_load_dwordx4 v[236:239], %[off], %[rs], 0 offen sc1\n\ts_sleep " STR(PSLEEP) "\n\t"
    "buffer_load_dwordx4 v[240:243], %[off], %[rs], 0 offen sc1\n\t"
    "1:\n\t"
    "s_waitcnt vmcnt(2)\n\t" PCHK1("v234", "v235", "2f")
    "buffer_load_dwordx4 v[232:235], %[off], %[rs], 0 offen sc1\n\t"
    "s_waitcnt vmcnt(2)\n\t" PCHK1("v238", "v239", "3f")
    "buffer_load_dwordx4 v[236:239], %[off], %[rs], 0 offen sc1\n\t"
    "s_waitcnt vmcnt(2)\n\t" PCHK1("v242", "v243", "4f")
    "buffer_load_dwordx4 v[240:243], %[off], %[rs], 0 offen sc1\n\t"
    "s_sub_u32 %[cnt], %[cnt], 1\n\ts_cmp_lg_u32 %[cnt], 0\n\ts_cbranch_scc1 1b\n\t"
    "s_waitcnt vmcnt(0)\n\t"
    "2:\n\tv_mov_b32 %[d0], v232\n\tv_mov_b32 %[d1], v233\n\ts_branch 5f\n\t"
    "3:\n\tv_mov_b32 %[d0], v236\n\tv_mov_b32 %[d1], v237\n\ts_branch 5f\n\t"
    "4:\n\tv_mov_b32 %[d0], v240\n\tv_mov_b32 %[d1], v241\n\t"
    "5:\n\t"
    : [d0] "=&v"(d0), [d1] "=&v"(d1), [t0] "=&v"(t0), [t1] "=&v"(t1), [sc] "=&s"(sc), [cnt] "+s"(cnt)
    : [off] "v"(off), [rs] "s"(r), [tlo] "s"(tlo), [thi] "s"(thi)
    : "vcc", "scc", "memory", "v232", "v233", "v234", "v235", "v236", "v237", "v238", "v239", "v240", "v241", "v242", "v243");
  return u32x2{d0, d1};
}
#define PCHK2(A2, A3, B2, B3, LBL) \
    "v_mov_b32 %[t0], " A2 "\n\tv_mov_b32 %[t1], " A3 "\n\t" \
    "v_cmp_eq_u32 vcc, %[tlo], %[t0]\n\tv_cmp_eq_u32 %[sc], %[thi], %[t1]\n\t" \
    "v_mov_b32 %[t0], " B2 "\n\tv_mov_b32 %[t1], " B3 "\n\t" \
    "s_and_b64 vcc, vcc, %[sc]\n\t" \
    "v_cmp_eq_u32 %[sc], %[tlo], %[t0]\n\tv_cmp_eq_u32 %[sd], %[thi], %[t1]\n\t" \
    "s_and_b64 vcc, vcc, %[sc]\n\ts_and_b64 vcc, vcc, %[sd]\n\t" \
    "s_cmp_eq_u64 vcc, exec\n\ts_cbranch_scc1 " LBL "\n\t"
#define PLD2(A, B) "buffer_load_dwordx4 " A ", %[off0], %[rs], 0 offen sc1\n\tbuffer_load_dwordx4 " B ", %[off1], %[rs], 0 offen sc1\n\t"
#define PRD2(A0, A1, B0, B1) "v_mov_b32 %[d0], " A0 "\n\tv_mov_b32 %[d1], " A1 "\n\tv_mov_b32 %[d2], " B0 "\n\tv_mov_b32 %[d3], " B1 "\n\t"
__device__ __forceinline__ u32x4 gpoll2p(rsrc_t r, u32 off0, u32 off1, u64 tag) {
  u32 d0, d1, d2, d3, t0, t1; u64 sc, sd; int cnt = SPIN_MAX;
  const u32 tlo = __builtin_amdgcn_readfirstlane((u32)tag), thi = __builtin_amdgcn_readfirstlane((u32)(tag >> 32));
  asm volatile(
    PLD2("v[232:235]", "v[236:239]") "s_sleep " STR(PSLEEP) "\n\t"
    PLD2("v[240:243]", "v[244:247]") "s_sleep " STR(PSLEEP) "\n\t"
    PLD2("v[248:251]", "v[252:255]")
    "1:\n\t"
    "s_waitcnt vmcnt(4)\n\t" PCHK2("v234", "v235", "v238", "v239", "2f")
    PLD2("v[232:235]", "v[236:239]")
    "s_waitcnt vmcnt(4)\n\t" PCHK2("v242", "v243", "v246", "v247", "3f")
    PLD2("v[240:243]", "v[244:247]")
    "s_waitcnt vmcnt(4)\n\t" PCHK2("v250", "v251", "v254", "v255", "4f")
    PLD2("v[248:251]", "v[252:255]")
    "s_sub_u32 %[cnt], %[cnt], 1\n\ts_cmp_lg_u32 %[cnt], 0\n\ts_cbranch_scc1 1b\n\t"
    "s_waitcnt vmcnt(0)\n\t"
    "2:\n\t" PRD2("v232", "v233", "v236", "v237") "s_branch 5f\n\t"
    "3:\n\t" PRD2("v240", "v241", "v244", "v245") "s_branch 5f\n\t"
    "4:\n\t" PRD2("v248", "v249", "v252", "v253")
    "5:\n\t"
    : [d0] "=&v"(d0), [d1] "=&v"(d1), [d2] "=&v"(d2), [d3] "=&v"(d3), [t0] "=&v"(t0), [t1] "=&v"(t1), [sc] "=&s"(sc), [sd] "=&s"(sd), [cnt] "+s"(cnt)
    : [off0] "v"(off0), [off1] "v"(off1), [rs] "s"(r), [tlo] "s"(tlo), [thi] "s"(thi)
    : "vcc", "scc", "memory", "v232", "v233", "v234", "v235", "v236", "v237", "v238", "v239", "v240", "v241", "v242", "v243",
      "v244", "v245", "v246", "v247", "v248", "v249", "v250", "v251", "v252", "v253", "v254", "v255");
  return u32x4{d0, d1, d2, d3};
}
__device__ __forceinline__ void wait_flagsp(rsrc_t r, u32 off, u64 tag) {
  u32 t0, t1; u64 sc; int cnt = SPIN_MAX;
  const u32 tlo = __builtin_amdgcn_readfirstlane((u32)tag), thi = __builtin_amdgcn_readfirstlane((u32)(tag >> 32));
  asm volatile(
    "buffer_load_dwordx2 v[232:233], %[off], %[rs], 0 offen sc1\n\ts_sleep " STR(PSLEEP) "\n\t"
    "buffer_load_dwordx2 v[234:235], %[off], %[rs], 0 offen sc1\n\ts_sleep " STR(PSLEEP) "\n\t"
    "buffer_load_dwordx2 v[236:237], %[off], %[rs], 0 offen sc1\n\t"
    "1:\n\t"
    "s_waitcnt vmcnt(2)\n\t" PCHK1("v232", "v233", "2f")
    "buffer_load_dwordx2 v[232:233], %[off], %[rs], 0 offen sc1\n\t"
    "s_waitcnt vmcnt(2)\n\t" PCHK1("v234", "v235", "2f")
    "buffer_load_dwordx2 v[234:235], %[off], %[rs], 0 offen sc1\n\t"
    "s_waitcnt vmcnt(2)\n\t" PCHK1("v236", "v237", "2f")
    "buffer_load_dwordx2 v[236:237], %[off], %[rs], 0 offen sc1\n\t"
    "s_sub_u32 %[cnt], %[cnt], 1\n\ts_cmp_lg_u32 %[cnt], 0\n\ts_cbranch_scc1 1b\n\t"
    "2:\n\t"
    : [t0] "=&v"(t0), [t1] "=&v"(t1), [sc] "=&s"(sc), [cnt] "+s"(cnt)
    : [off] "v"(off), [rs] "s"(r), [tlo] "s"(tlo), [thi] "s"(thi)
    : "vcc", "scc", "memory", "v232", "v233", "v234", "v235", "v236", "v237");
}

#define STAMP(i) do {} while (0)
#define STAMP_FLUSH() do {} while (0)

__device__ __forceinline__ bf16x8 tr_pair(const unsigned char* a0, const unsigned char* a1) {
  const s16x4 t0 = __builtin_amdgcn_ds_read_tr16_b64_v4i16((LAS s16x4*)a0);
  const s16x4 t1 = __builtin_amdgcn_ds_read_tr16_b64_v4i16((LAS s16x4*)a1);
  return __builtin_bit_cast(bf16x8, __builtin_shufflevector(t0, t1, 0, 1, 2, 3, 4, 5, 6, 7));
}

__device__ __forceinline__ void proj_mfma(unsigned char* lds, int wbase, rsrc_t wsr, u32 out_off, u64 tag, int wave, int lane, int ovb = L_SCR + 2048, bool st = true) {
  const int g4 = lane >> 4, i16 = lane & 15, qq = i16 >> 2, pp = i16 & 3;
  bf16x8 af[2];
#pragma unroll
  for (int ks = 0; ks < 2; ++ks) {
    u32x2 lo = {0u, 0u}, hi = {0u, 0u};
    if (i16 < 2) {
      lo = *(const u32x2*)(lds + ovb + i16 * 128 + (32 * ks + 4 * g4) * 2);
      hi = *(const u32x2*)(lds + ovb + i16 * 128 + (32 * ks + 16 + 4 * g4) * 2);
    }
    af[ks] = __builtin_bit_cast(bf16x8, u32x4{lo[0], lo[1], hi[0], hi[1]});
  }
  const int r0 = 4 * g4 + qq;
  const int sw = (r0 >> 1) & 3;
#pragma unroll
  for (int i = 0; i < 5; ++i) {
    const int nb = wave + 8 * i;
    if (nb < 36) {
      f32x4 d = {0.f, 0.f, 0.f, 0.f};
      const unsigned char* base = lds + wbase + r0 * 1152 + ((nb ^ sw) * 32) + pp * 8;
#pragma unroll
      for (int ks = 0; ks < 2; ++ks) {
        const bf16x8 b = tr_pair(base + (32 * ks) * 1152, base + (32 * ks + 16) * 1152);
        d = __builtin_amdgcn_mfma_f32_16x16x32_bf16(af[ks], b, d, 0, 0, 0);
      }
      if (lane < 16 && st) gst(wsr, out_off + (16 * nb + lane) * 16, __float_as_uint(d[0]), __float_as_uint(d[1]), tag);
    }
  }
}
__device__ __forceinline__ void final_reduce(unsigned char* lds, rsrc_t wsr, u32 part_off, u64 tag, int f, int t, u16* out, int base, bool real = true) {
  const int wave = t >> 6, lane = t & 63;
  const int pidx = t >> 3, sub = t & 7;
  u32 off[2] = {part_off + (u32)(pidx * 576 + 16 * f + sub) * 16, part_off + (u32)(pidx * 576 + 16 * f + 8 + sub) * 16};
  u32x4 g[2] = {u32x4{0u, 0u, 0u, 0u}, u32x4{0u, 0u, 0u, 0u}};
  if (!real) {} else if (PMASK & 1) { const u32x4 r_ = gpoll2p(wsr, off[0], off[1], tag); g[0][0] = r_[0]; g[0][1] = r_[1]; g[1][0] = r_[2]; g[1][1] = r_[3]; } else gpoll<2>(wsr, off, g, true, tag);
  float v[4] = {__uint_as_float(g[0][0]), __uint_as_float(g[0][1]), __uint_as_float(g[1][0]), __uint_as_float(g[1][1])};
#pragma unroll
  for (int k = 0; k < 4; ++k) { float x = v[k]; x += dppf<0x128>(x); x = xsum16(x); x = xsum32(x); v[k] = x; }
  float* red = (float*)(lds + L_SCR);
  if (lane < 8) *(f32x4*)(red + wave * 32 + lane * 4) = f32x4{v[0], v[1], v[2], v[3]};
  BAR();
  if (t < 32) {
    float s = 0.f;
#pragma unroll
    for (int w = 0; w < 8; ++w) s += red[w * 32 + t];
    const int sb = t >> 2, k = t & 3, bb = k & 1, col = sb + 8 * (k >> 1);
    __bf16 o = (__bf16)s;
    if (real) out[bb * 1152 + base + 16 * f + col] = __builtin_bit_cast(u16, o);
  }
  if (real) DRAIN();
}

template <int NL, int RS>
__device__ __forceinline__ void q_load(const u16* qW, const u16* qx, int qcol0, int t, u32x4 (&qw)[NL], u16 (&qx0)[NL], u16 (&qx1)[NL]) {
  const int cch = t & 3, row0 = t >> 2;
#pragma unroll
  for (int i = 0; i < NL; ++i) {
    const int row = row0 + RS * i, rc = row < 576 ? row : 575;
    qx0[i] = qx[rc]; qx1[i] = qx[576 + rc];
  }
#pragma unroll
  for (int i = 0; i < NL; ++i) {
    const int row = row0 + RS * i, rc = row < 576 ? row : 575;
    qw[i] = ldnt(qW + (size_t)rc * 4096 + qcol0 + cch * 8);
  }
}
template <int NL, int RS>
__device__ __forceinline__ void q_fma(int t, const u32x4 (&qw)[NL], const u16 (&qx0)[NL], const u16 (&qx1)[NL], float (&qa0)[8], float (&qa1)[8]) {
#pragma unroll
  for (int k = 0; k < 8; ++k) { qa0[k] = 0.f; qa1[k] = 0.f; }
#pragma unroll
  for (int i = 0; i < NL; ++i) {
    const bool live = (t >> 2) + RS * i < 576;
    const float x0 = live ? bf2f(qx0[i]) : 0.f, x1 = live ? bf2f(qx1[i]) : 0.f;
#pragma unroll
    for (int k = 0; k < 4; ++k) {
      const float lo = bflo(qw[i][k]), hi = bfhi(qw[i][k]);
      qa0[2 * k] = fmaf(x0, lo, qa0[2 * k]); qa0[2 * k + 1] = fmaf(x0, hi, qa0[2 * k + 1]);
      qa1[2 * k] = fmaf(x1, lo, qa1[2 * k]); qa1[2 * k + 1] = fmaf(x1, hi, qa1[2 * k + 1]);
    }
  }
}
__device__ __forceinline__ void q_reduce(unsigned char* lds, int wave, int lane, float (&qa0)[8], float (&qa1)[8]) {
#pragma unroll
  for (int k = 0; k < 8; ++k) {
    float x = qa0[k]; x += dppf<0x124>(x); x += dppf<0x128>(x); x = xsum16(x); x = xsum32(x); qa0[k] = x;
    float y = qa1[k]; y += dppf<0x124>(y); y += dppf<0x128>(y); y = xsum16(y); y = xsum32(y); qa1[k] = y;
  }
  float* red_ = (float*)(lds + L_SCR);
  if (lane < 4) {
    float* r0 = red_ + wave * 64 + lane * 8;
    *(f32x4*)r0 = f32x4{qa0[0], qa0[1], qa0[2], qa0[3]}; *(f32x4*)(r0 + 4) = f32x4{qa0[4], qa0[5], qa0[6], qa0[7]};
    *(f32x4*)(r0 + 32) = f32x4{qa1[0], qa1[1], qa1[2], qa1[3]}; *(f32x4*)(r0 + 36) = f32x4{qa1[4], qa1[5], qa1[6], qa1[7]};
  }
}
template <int NW>
__device__ __forceinline__ void q_publish(unsigned char* lds, rsrc_t wsr, int qmat, int qcol0, int t, u64 tag) {
  if (t < 16) {
    const float* red_ = (const float*)(lds + L_SCR);
    f32x4 s_ = *(const f32x4*)(red_ + t * 4);
#pragma unroll
    for (int w = 1; w < NW; ++w) s_ += *(const f32x4*)(red_ + w * 64 + t * 4);
    gst(wsr, WS_QG + ((qmat * 2 + (t >> 3)) * 1024 + (qcol0 >> 2) + (t & 7)) * 16, pack_bf16(s_[0], s_[1]), pack_bf16(s_[2], s_[3]), tag);
  }
}

#define LA3 1
#define LA2 1
#define ADEPTH 1
#define PREMODE 1
#define NPRE3 0
#define EARLY3 8
#define EARLY2 8
#define D1 0
#define D2 0
#define DELAY(n) do { _Pragma("unroll") for (int d_ = 0; d_ < (n); ++d_) __builtin_amdgcn_s_sleep(8); } while (0)
constexpr int U2 = (256 - 4 * U3) / 2;

#define A_LOAD(KK, VV, it) do { _Pragma("unroll") for (int i_ = 0; i_ < 4; ++i_) { KK[i_] = ldnt(Kb + (size_t)(it) * 16384 + i_ * 512); } \
                                _Pragma("unroll") for (int i_ = 0; i_ < 4; ++i_) { VV[i_] = ldnt(Vb + (size_t)(it) * 16384 + i_ * 512); } } while (0)
#define A_COMPUTE(KK, VV) do { \
      float s0_[4], s1_[4]; \
      _Pragma("unroll") for (int i_ = 0; i_ < 4; ++i_) { \
        float d0_ = 0.f, d1_ = 0.f; \
        _Pragma("unroll") for (int k_ = 0; k_ < 4; ++k_) { d0_ = dot2(KK[i_][k_], q0[k_], d0_); d1_ = dot2(KK[i_][k_], q1[k_], d1_); } \
        d0_ = row16_sum(d0_); d1_ = row16_sum(d1_); \
        s0_[i_] = bfr(bfr(d0_) * SCALE); s1_[i_] = bfr(bfr(d1_) * SCALE); } \
      const float mx0_ = fmaxf(fmaxf(s0_[0], s0_[1]), fmaxf(s0_[2], s0_[3])), mx1_ = fmaxf(fmaxf(s1_[0], s1_[1]), fmaxf(s1_[2], s1_[3])); \
      if (__any(mx0_ > m0 || mx1_ > m1)) { \
        const float n0_ = fmaxf(m0, mx0_), n1_ = fmaxf(m1, mx1_); \
        const float al0_ = ex2((m0 - n0_) * L2E), al1_ = ex2((m1 - n1_) * L2E); \
        l0 *= al0_; l1 *= al1_; m0 = n0_; m1 = n1_; \
        _Pragma("unroll") for (int k_ = 0; k_ < 8; ++k_) { acc0[k_] *= al0_; acc1[k_] *= al1_; } } \
      float p0_[4], p1_[4]; \
      _Pragma("unroll") for (int i_ = 0; i_ < 4; ++i_) { p0_[i_] = ex2((s0_[i_] - m0) * L2E); p1_[i_] = ex2((s1_[i_] - m1) * L2E); } \
      l0 += (p0_[0] + p0_[1]) + (p0_[2] + p0_[3]); l1 += (p1_[0] + p1_[1]) + (p1_[2] + p1_[3]); \
      const u32 pa0_ = pack_bf16(p0_[0], p0_[1]), pb0_ = pack_bf16(p0_[2], p0_[3]), pa1_ = pack_bf16(p1_[0], p1_[1]), pb1_ = pack_bf16(p1_[2], p1_[3]); \
      _Pragma("unroll") for (int k_ = 0; k_ < 4; ++k_) { \
        const u32 lo01_ = __builtin_amdgcn_perm(VV[1][k_], VV[0][k_], 0x05040100u), hi01_ = __builtin_amdgcn_perm(VV[1][k_], VV[0][k_], 0x07060302u); \
        const u32 lo23_ = __builtin_amdgcn_perm(VV[3][k_], VV[2][k_], 0x05040100u), hi23_ = __builtin_amdgcn_perm(VV[3][k_], VV[2][k_], 0x07060302u); \
        acc0[2 * k_] = dot2(lo23_, pb0_, dot2(lo01_, pa0_, acc0[2 * k_])); acc0[2 * k_ + 1] = dot2(hi23_, pb0_, dot2(hi01_, pa0_, acc0[2 * k_ + 1])); \
        acc1[2 * k_] = dot2(lo23_, pb1_, dot2(lo01_, pa1_, acc1[2 * k_])); acc1[2 * k_ + 1] = dot2(hi23_, pb1_, dot2(hi01_, pa1_, acc1[2 * k_ + 1])); } \
    } while (0)

#define A_LOADK(KK, it) do { _Pragma("unroll") for (int i_ = 0; i_ < 4; ++i_) { KK[i_] = ldnt(Kb + (size_t)(it) * 16384 + i_ * 512); } } while (0)
#define A_LOADV(VV, it) do { _Pragma("unroll") for (int i_ = 0; i_ < 4; ++i_) { VV[i_] = ldnt(Vb + (size_t)(it) * 16384 + i_ * 512); } } while (0)
#define A_SCORE(KK) do { \
      float s0_[4], s1_[4]; \
      _Pragma("unroll") for (int i_ = 0; i_ < 4; ++i_) { \
        float d0_ = 0.f, d1_ = 0.f; \
        _Pragma("unroll") for (int k_ = 0; k_ < 4; ++k_) { d0_ = dot2(KK[i_][k_], q0[k_], d0_); d1_ = dot2(KK[i_][k_], q1[k_], d1_); } \
        d0_ = row16_sum(d0_); d1_ = row16_sum(d1_); \
        s0_[i_] = bfr(bfr(d0_) * SCALE); s1_[i_] = bfr(bfr(d1_) * SCALE); } \
      const float mx0_ = fmaxf(fmaxf(s0_[0], s0_[1]), fmaxf(s0_[2], s0_[3])), mx1_ = fmaxf(fmaxf(s1_[0], s1_[1]), fmaxf(s1_[2], s1_[3])); \
      if (__any(mx0_ > m0 || mx1_ > m1)) { \
        const float n0_ = fmaxf(m0, mx0_), n1_ = fmaxf(m1, mx1_); \
        const float al0_ = ex2((m0 - n0_) * L2E), al1_ = ex2((m1 - n1_) * L2E); \
        l0 *= al0_; l1 *= al1_; m0 = n0_; m1 = n1_; \
        _Pragma("unroll") for (int k_ = 0; k_ < 8; ++k_) { acc0[k_] *= al0_; acc1[k_] *= al1_; } } \
      float p0_[4], p1_[4]; \
      _Pragma("unroll") for (int i_ = 0; i_ < 4; ++i_) { p0_[i_] = ex2((s0_[i_] - m0) * L2E); p1_[i_] = ex2((s1_[i_] - m1) * L2E); } \
      l0 += (p0_[0] + p0_[1]) + (p0_[2] + p0_[3]); l1 += (p1_[0] + p1_[1]) + (p1_[2] + p1_[3]); \
      pa0 = pack_bf16(p0_[0], p0_[1]); pb0 = pack_bf16(p0_[2], p0_[3]); pa1 = pack_bf16(p1_[0], p1_[1]); pb1 = pack_bf16(p1_[2], p1_[3]); \
    } while (0)
#define A_PV(VV) do { \
      _Pragma("unroll") for (int k_ = 0; k_ < 4; ++k_) { \
        const u32 lo01_ = __builtin_amdgcn_perm(VV[1][k_], VV[0][k_], 0x05040100u), hi01_ = __builtin_amdgcn_perm(VV[1][k_], VV[0][k_], 0x07060302u); \
        const u32 lo23_ = __builtin_amdgcn_perm(VV[3][k_], VV[2][k_], 0x05040100u), hi23_ = __builtin_amdgcn_perm(VV[3][k_], VV[2][k_], 0x07060302u); \
        acc0[2 * k_] = dot2(lo23_, pb0, dot2(lo01_, pa0, acc0[2 * k_])); acc0[2 * k_ + 1] = dot2(hi23_, pb0, dot2(hi01_, pa0, acc0[2 * k_ + 1])); \
        acc1[2 * k_] = dot2(lo23_, pb1, dot2(lo01_, pa1, acc1[2 * k_])); acc1[2 * k_ + 1] = dot2(hi23_, pb1, dot2(hi01_, pa1, acc1[2 * k_ + 1])); } \
    } while (0)
#define A_HSTEP(u) do { \
    if ((u) < NU || ((u) == NU && extra)) { \
      DRAIN(); A_LOADV(V0, (u)); __builtin_amdgcn_sched_barrier(0); A_SCORE(K0); __builtin_amdgcn_sched_barrier(0); \
      DRAIN(); if ((u) + 1 < NU || ((u) + 1 == NU && extra)) { A_LOADK(K0, (u) + 1); } __builtin_amdgcn_sched_barrier(0); A_PV(V0); __builtin_amdgcn_sched_barrier(0); } \
    } while (0)
#define WAITD() asm volatile("s_waitcnt vmcnt(0)" ::: "memory")
#define A_STEP(KK, VV, KN, VN, u) do { \
    if ((u) + LA < NU) { DRAIN(); A_LOAD(KN, VN, (u) + LA); } else if ((u) + LA == NU) { if (extra) { DRAIN(); A_LOAD(KN, VN, (u) + LA); } } \
    __builtin_amdgcn_sched_barrier(0); \
    if ((u) < NU) { A_COMPUTE(KK, VV); } else if ((u) == NU) { if (extra) A_COMPUTE(KK, VV); } \
    __builtin_amdgcn_sched_barrier(0); } while (0)
template <int NU, int LA>
__device__ __forceinline__ void a_stream(bool pre, bool extra, bool poll, const u16* Kb, const u16* Vb, unsigned char* lds, rsrc_t wsr, u32 qoff, u64 tag, int lane,
                                         float& m0, float& m1, float& l0, float& l1, float (&acc0)[8], float (&acc1)[8],
                                         u32x4 (&K0)[4], u32x4 (&V0)[4], u32x4 (&K1)[4], u32x4 (&V1)[4]) {
  u32x4 K2[4], V2[4];
  if (LA == 2) { A_LOAD(K0, V0, 0); __builtin_amdgcn_sched_barrier(0); }
  if (poll) {
    u32 off[1] = {qoff};
    u32x4 g[1];
    if (PMASK & 2) { const u32x2 r_ = gpoll1p(wsr, off[0], tag); g[0][0] = r_[0]; g[0][1] = r_[1]; } else gpoll<1>(wsr, off, g, true, tag);
    *(u32x2*)(lds + L_SCR + 2048 + lane * 8) = u32x2{g[0][0], g[0][1]};
  }
  if (LA == 2) { DRAIN(); A_LOAD(K1, V1, 1); } else if (LA == 0) { A_LOADK(K0, 0); } else {
    if (!pre) { A_LOAD(K0, V0, 0); } }
  BAR();
  const u32x4 q0 = *(const u32x4*)(lds + L_SCR + 2048 + (lane & 15) * 16), q1 = *(const u32x4*)(lds + L_SCR + 2048 + 256 + (lane & 15) * 16);
  if (LA == 0) {
    u32 pa0, pb0, pa1, pb1;
    A_HSTEP(0); A_HSTEP(1); A_HSTEP(2); A_HSTEP(3); A_HSTEP(4); A_HSTEP(5); A_HSTEP(6); A_HSTEP(7);
  } else if (LA == 1) {
    A_STEP(K0, V0, K1, V1, 0); A_STEP(K1, V1, K0, V0, 1); A_STEP(K0, V0, K1, V1, 2); A_STEP(K1, V1, K0, V0, 3);
    A_STEP(K0, V0, K1, V1, 4); A_STEP(K1, V1, K0, V0, 5); A_STEP(K0, V0, K1, V1, 6); A_STEP(K1, V1, K0, V0, 7);
  } else {
    A_STEP(K0, V0, K2, V2, 0); A_STEP(K1, V1, K0, V0, 1); A_STEP(K2, V2, K1, V1, 2); A_STEP(K0, V0, K2, V2, 3);
    A_STEP(K1, V1, K0, V0, 4); A_STEP(K2, V2, K1, V1, 5); A_STEP(K0, V0, K2, V2, 6); A_STEP(K1, V1, K0, V0, 7);
  }
}

__device__ __forceinline__ u32 touch_pages(const Params& p, int lane, const u16* Kb, const u16* Vb, u32 lds_dummy) {
  const unsigned char* a = (const unsigned char*)p.Ax;
  const int g = lane >> 2, k = lane & 3;
  const u32 big = k == 3 ? 0u : (u32)k << 21;
  if (g == 1) a = (const unsigned char*)p.Awq + big;
  if (g == 2) a = (const unsigned char*)p.Awo + big;
  if (g == 3) a = (const unsigned char*)p.Bwuv + (k == 2 ? 4194300u : big);
  if (g == 4) a = (const unsigned char*)p.Bwo + big;
  if (g == 5) a = p.ws + (k == 2 ? WS_END - 4 : big);
  if (g == 6) a = (const unsigned char*)p.out;
  if (g == 7) a = (const unsigned char*)(k & 1 ? Vb : Kb) + (k & 2 ? (u32)U2 * 2048u : 0u);
  u32 v = 0;
  if (lane < 32) {
    u32 keep;
    asm volatile("s_mov_b32 %0, m0\n\ts_mov_b32 m0, %2\n\ts_nop 0\n\tglobal_load_lds_dword %1, off\n\ts_mov_b32 m0, %0"
                 : "=&s"(keep) : "v"(a), "s"(lds_dummy) : "memory");
  }
  return v;
}

__global__ void __launch_bounds__(NT, 2) fused_kernel(Params p) {
  __shared__ __attribute__((aligned(16))) unsigned char lds[LDS_BYTES];
  u64 pc0; asm volatile("s_getpc_b64 %0" : "=s"(pc0)); pc0 &= ~255ull;
  const int bid = blockIdx.x, t = threadIdx.x, lane = t & 63;
  const int wave = __builtin_amdgcn_readfirstlane(t >> 6);
  unsigned char* ws = p.ws;
  const rsrc_t wsr = __builtin_amdgcn_make_buffer_rsrc(ws, 0, WS_END, 0x00020000);
  const u32 ldsb = (u32)(uintptr_t)(LAS unsigned char*)lds;
  STAMP(0);
  u32 vz; asm volatile("v_mov_b32 %0, 0" : "=v"(vz));
  const u64 epoch = flag_get(ws + vz, WS_EPOCH);
  const int xcd = bid & 7, slot = bid >> 3;
  const bool isB = slot < 8;

  if (!isB) {
    const bool isR2 = slot < 16;
    const int r2 = (slot - 8) * 8 + xcd, r3 = (slot - 16) * 8 + xcd;
    const int pair = isR2 ? (r2 >> 1) : (r3 >> 2), sub = isR2 ? 4 + (r2 & 1) : (r3 & 3);
    const int ai = pair * 6 + sub;
    const int b = pair >> 4, hkv = pair & 15;
    const int ustart = (isR2 ? 4 * U3 + (sub - 4) * U2 : sub * U3) + (7 - wave);
    const u16* Kb = p.Ak + (size_t)pair * 4096 * 128 + (size_t)ustart * 2048 + (lane >> 4) * 128 + (lane & 15) * 8;
    const u16* Vb = p.Av + (size_t)pair * 4096 * 128 + (size_t)ustart * 2048 + (lane >> 4) * 128 + (lane & 15) * 8;
    const u32 qoff = WS_QG + (u32)((0 * 2 + b) * 1024 + (2 * hkv) * 32 + lane) * 16;
    float m0 = -INFINITY, m1 = -INFINITY, l0 = 0.f, l1 = 0.f;
    float acc0[8], acc1[8];
#pragma unroll
    for (int k = 0; k < 8; ++k) { acc0[k] = 0.f; acc1[k] = 0.f; }
    u64 tag;
    u32 tch = 0;
    u32x4 K0[4], V0[4], K1[4], V1[4];
    if (!isR2) {
      const int qc = (((slot - 16) >> 1) << 4) | (xcd << 1) | ((slot - 16) & 1);
      const int qcol0 = qc * 32;
      u32x4 qwB[5], qwA[5]; u16 bx0[5], bx1[5], ax0[5], ax1[5];
      q_load<5, 128>(p.Bwq, p.Bx, qcol0, t, qwB, bx0, bx1);
      if (wave == 7) tch = touch_pages(p, lane, Kb, Vb, ldsb + L_ST + 1024);
      __builtin_amdgcn_sched_barrier(0);
      float qa0[8], qa1[8];
      q_fma<5, 128>(t, qwB, bx0, bx1, qa0, qa1);
      __builtin_amdgcn_sched_barrier(0);
      q_load<5, 128>(p.Awq, p.Ax, qcol0, t, qwA, ax0, ax1);
      if (EARLY3 == 0 || (EARLY3 < 8 && wave >= EARLY3)) A_LOAD(K0, V0, 0);
      __builtin_amdgcn_sched_barrier(0);
      q_reduce(lds, wave, lane, qa0, qa1);
      tag = epoch + 1ull;
      BAR();
      q_publish<8>(lds, wsr, 1, qcol0, t, tag);
      STAMP(1);
      q_fma<5, 128>(t, qwA, ax0, ax1, qa0, qa1);
      q_reduce(lds - 4096, wave, lane, qa0, qa1);
      BAR();
      q_publish<8>(lds - 4096, wsr, 0, qcol0, t, tag);
      STAMP(2);
      constexpr int NLO = U3 / 8, WSPLIT = 8 - (U3 - NLO * 8);
      a_stream<NLO, LA3>(EARLY3 == 0 || (EARLY3 < 8 && wave >= EARLY3), wave >= WSPLIT, wave == 0, Kb, Vb, lds, wsr, qoff, tag, lane, m0, m1, l0, l1, acc0, acc1, K0, V0, K1, V1);
    } else {
      const int hh = r2 >> 1, half = r2 & 1;
      u32x4 wk[8];
      DELAY(D2);
#pragma unroll
      for (int i = 0; i < 8; ++i) wk[i] = ldnt(p.Bwuk + ((size_t)(256 * half + (t >> 4) + 32 * i) * 32 + hh) * 128 + (t & 15) * 8);
      if (wave == 7) tch = touch_pages(p, lane, Kb, Vb, ldsb + L_ST + 1024);
      {
        const unsigned char* src = (const unsigned char*)(p.Awo + (size_t)r2 * 64 * 576);
        for (int i = 0; i < EARLYWO; ++i) {
          const int L = i * 512 + t, r = L / 72, pc = L - r * 72;
          const int c = (((pc >> 1) ^ ((r >> 1) & 3)) << 1) | (pc & 1);
          glds16(src + r * 1152 + c * 16, ldsb + (u32)(i * 512 + wave * 64) * 16);
        }
      }
      if (EARLY2 == 0 || (EARLY2 < 8 && wave >= EARLY2)) A_LOAD(K0, V0, 0);
      __builtin_amdgcn_sched_barrier(0);
      tag = epoch + 1ull;
      if (wave == 0) {
        u32 off[1] = {WS_QG + (u32)((1 * 2 + (lane >> 5)) * 1024 + hh * 32 + (lane & 31)) * 16};
        u32x4 g[1];
        if (PMASK & 4) { const u32x2 r_ = gpoll1p(wsr, off[0], tag); g[0][0] = r_[0]; g[0][1] = r_[1]; } else gpoll<1>(wsr, off, g, true, tag);
        *(u32x2*)(lds + L_ST + lane * 8) = u32x2{g[0][0], g[0][1]};
      }
      BAR();
      STAMP(1);
      const u32x4 qa = *(const u32x4*)(lds + L_ST + (t & 15) * 16), qb = *(const u32x4*)(lds + L_ST + 256 + (t & 15) * 16);
      float* qs = (float*)(lds + L_SCR);
#pragma unroll
      for (int i = 0; i < 8; ++i) {
        float d0 = 0.f, d1 = 0.f;
#pragma unroll
        for (int k = 0; k < 4; ++k) { d0 = dot2(wk[i][k], qa[k], d0); d1 = dot2(wk[i][k], qb[k], d1); }
        d0 = row16_sum(d0); d1 = row16_sum(d1);
        if ((t & 15) == 0) { qs[(t >> 4) + 32 * i] = d0; qs[256 + (t >> 4) + 32 * i] = d1; }
      }
      BAR();
      if (wave == 0) {
        const int bb = lane >> 5, c8 = lane & 31;
        u32x4 hi, lo;
#pragma unroll
        for (int k = 0; k < 4; ++k) {
          const float v0 = qs[bb * 256 + c8 * 8 + 2 * k], v1 = qs[bb * 256 + c8 * 8 + 2 * k + 1];
          const float h0 = bfr(v0), h1 = bfr(v1);
          hi[k] = pack_bf16(h0, h1); lo[k] = pack_bf16(v0 - h0, v1 - h1);
        }
        const u32 o = ((bb * 32 + hh) * 512 + 256 * half + c8 * 8) * 2;
        st128(wsr, WS_QH + o, hi); st128(wsr, WS_QL + o, lo);
        DRAIN();
        if (lane == 0) flag_set(ws, WS_B2FLAG + r2 * 8, tag);
      }
      BAR();
      STAMP(2);
      {
        const unsigned char* src = (const unsigned char*)(p.Awo + (size_t)r2 * 64 * 576);
        for (int i = EARLYWO; i < 9; ++i) {
          const int L = i * 512 + t, r = L / 72, pc = L - r * 72;
          const int c = (((pc >> 1) ^ ((r >> 1) & 3)) << 1) | (pc & 1);
          glds16(src + r * 1152 + c * 16, ldsb + (u32)(i * 512 + wave * 64) * 16);
        }
      }
      constexpr int NLO = U2 / 8, WSPLIT = 8 - (U2 - NLO * 8);
      a_stream<NLO, LA2>(EARLY2 == 0 || (EARLY2 < 8 && wave >= EARLY2), wave >= WSPLIT, wave == 1, Kb, Vb, lds, wsr, qoff, tag, lane, m0, m1, l0, l1, acc0, acc1, K0, V0, K1, V1);
    }
    asm volatile("" :: "v"(tch));
    STAMP(3);
    float* mrg = (float*)(lds + L_MRG);
    float* mml = (float*)(lds + L_ST);
    {
      const float M0 = xmax32(xmax16(m0)), M1 = xmax32(xmax16(m1));
      const float a0 = ex2((m0 - M0) * L2E), a1 = ex2((m1 - M1) * L2E);
      l0 = xsum32(xsum16(l0 * a0)); l1 = xsum32(xsum16(l1 * a1));
#pragma unroll
      for (int k = 0; k < 8; ++k) { acc0[k] = xsum32(xsum16(acc0[k] * a0)); acc1[k] = xsum32(xsum16(acc1[k] * a1)); }
      if (lane < 16) {
        float* r0 = mrg + (wave * 2 + 0) * 128 + lane * 8; float* r1 = mrg + (wave * 2 + 1) * 128 + lane * 8;
        *(f32x4*)r0 = f32x4{acc0[0], acc0[1], acc0[2], acc0[3]}; *(f32x4*)(r0 + 4) = f32x4{acc0[4], acc0[5], acc0[6], acc0[7]};
        *(f32x4*)r1 = f32x4{acc1[0], acc1[1], acc1[2], acc1[3]}; *(f32x4*)(r1 + 4) = f32x4{acc1[4], acc1[5], acc1[6], acc1[7]};
        if (lane == 0) *(f32x4*)(mml + wave * 4) = f32x4{M0, l0, M1, l1};
      }
    }
    BAR();
    if (t < 128) {
      const int g = t >> 6, k = t & 63;
      float M = -INFINITY;
#pragma unroll
      for (int r = 0; r < 8; ++r) M = fmaxf(M, mml[r * 4 + g * 2]);
      float L = 0.f, o0 = 0.f, o1 = 0.f;
#pragma unroll
      for (int r = 0; r < 8; ++r) {
        const float w = ex2((mml[r * 4 + g * 2] - M) * L2E); L = fmaf(w, mml[r * 4 + g * 2 + 1], L);
        const float2 v = *(const float2*)(mrg + (r * 2 + g) * 128 + 2 * k);
        o0 = fmaf(w, v.x, o0); o1 = fmaf(w, v.y, o1);
      }
      gst(wsr, WS_APG + ((ai * 2 + g) * 65 + k) * 16, __float_as_uint(o0), __float_as_uint(o1), tag);
      if (k == 0) gst(wsr, WS_APG + ((ai * 2 + g) * 65 + 64) * 16, __float_as_uint(M), __float_as_uint(L), tag);
    }
    STAMP(4);
    if (isR2) {
      const int hq = r2 >> 1, dh = r2 & 1, thkv = hq >> 1, g = hq & 1;
#define DRY 1
      const int bb = lane >> 5, k = lane & 31;
      const int a0i = (bb * 16 + thkv) * 6;
      const int ovb = L_MRG + 8192 + 6144 + wave * 256;
#pragma nounroll
      for (int pass = DRY ? 0 : 1; pass < 2; ++pass) {
        int real; asm volatile("s_mov_b32 %0, %1" : "=s"(real) : "s"(pass));
        if (real) {
          if (wave < 6) {
            const u32x4 r_ = gpoll2p(wsr, WS_APG + (((a0i + wave) * 2 + g) * 65 + 32 * dh + k) * 16, WS_APG + (((a0i + wave) * 2 + g) * 65 + 64) * 16, tag);
            *(u32x4*)(lds + L_MRG + 8192 + (wave * 64 + lane) * 16) = r_;
          }
          BAR();
          STAMP(5);
        }
        if (real || wave >= 6) {
          u32x4 gg[6];
#pragma unroll
          for (int s = 0; s < 6; ++s) gg[s] = *(const u32x4*)(lds + L_MRG + 8192 + (s * 64 + lane) * 16);
          float M = __uint_as_float(gg[0][2]);
#pragma unroll
          for (int s = 1; s < 6; ++s) M = fmaxf(M, __uint_as_float(gg[s][2]));
          float L = 0.f, o0 = 0.f, o1 = 0.f;
#pragma unroll
          for (int s = 0; s < 6; ++s) {
            const float w = ex2((__uint_as_float(gg[s][2]) - M) * L2E);
            L = fmaf(w, __uint_as_float(gg[s][3]), L); o0 = fmaf(w, __uint_as_float(gg[s][0]), o0); o1 = fmaf(w, __uint_as_float(gg[s][1]), o1);
          }
          *(u32*)(lds + ovb + bb * 128 + k * 4) = pack_bf16(o0 / L, o1 / L);
          LGKM0();
          proj_mfma(lds, 0, wsr, WS_OUTA + r2 * 9216, tag, wave, lane, ovb, real != 0);
        }
      }
      STAMP(6);
      DRAIN();
    } else if (r3 < 36) {
      final_reduce(lds, wsr, WS_OUTA, tag, r3, t, p.out, 0);
      if (r3 == 0) {
        if (wave == 0) { wait_flags(ws, WS_B3FLAG + lane * 8, true, tag); if (lane == 0) flag_set(ws, WS_EPOCH, tag); }
      }
      STAMP(7);
    }
    STAMP_FLUSH();
    return;
  }

  const int j = slot * 8 + xcd;
  const int hh = j >> 1, half = j & 1;
  const int b3b = j >> 5, b3ks = j & 31;
  const int kb = wave & 3, kh = wave >> 2;
  {
    DELAY(D1);
    const unsigned char* src = (const unsigned char*)(p.Bc + ((size_t)b3b * 4096 + 128 * b3ks + 16 * wave) * 512) + lane * 16;
#pragma unroll
    for (int r = 0; r < 16; ++r) glds16(src + r * 1024, ldsb + L_C + (u32)(16 * wave + r) * CSTR);
    if (wave == 7) code_prefetch(pc0, lane, ldsb + L_ST + 1024);
    if (wave == 6) { const u32 tch = touch_pages(p, lane, p.Ak, p.Av, ldsb + L_ST + 1024); asm volatile("" :: "v"(tch)); }
    DRAIN();
  }
  BAR();
  STAMP(5);
  const u64 tag = epoch + 1ull;
  const int head = lane & 31, hi = lane >> 5;
  const int g4 = lane >> 4, i16 = lane & 15, qq = i16 >> 2, pp = i16 & 3;
  u32x4 cf[16];
  bf16x8 pv[2][8];
  {
    const int pi = 16 * (head >> 4) + 4 * (head & 3) + 2 * ((head >> 3) & 1) + ((head >> 2) & 1);
    const unsigned char* cr = lds + L_C + (32 * kb + pi) * CSTR + (256 * kh + 8 * hi) * 2;
#pragma unroll
    for (int s = 0; s < 16; ++s) cf[s] = *(const u32x4*)(cr + s * 32);
    const unsigned char* tb = lds + L_C + (4 * qq + 2 * hi) * CSTR + (64 * wave + 16 * (g4 & 1) + 4 * pp) * 2;
#pragma unroll
    for (int ks = 0; ks < 8; ++ks) {
      pv[0][ks] = tr_pair(tb + (16 * ks) * CSTR, tb + (16 * ks + 1) * CSTR);
      pv[1][ks] = tr_pair(tb + (16 * ks) * CSTR + 64, tb + (16 * ks + 1) * CSTR + 64);
    }
  }
  STAMP(4);
  BAR();
  {
    const unsigned char* src = (const unsigned char*)(p.Bwuv + (size_t)hh * 128 + 64 * half);
#pragma unroll
    for (int i = 0; i < 8; ++i) {
      const int L = i * 512 + t, r = L >> 3, pc = L & 7;
      const int c = (((pc >> 1) ^ ((r >> 1) & 3)) << 1) | (pc & 1);
      glds16(src + (size_t)r * 8192 + c * 16, ldsb + L_WUV + (u32)(i * 512 + wave * 64) * 16);
    }
  }
  u32x4 wo[9];
#pragma unroll
  for (int i = 0; i < 9; ++i) wo[i] = ldnt(p.Bwo + (size_t)j * 64 * 576 + (size_t)(i * 512 + t) * 8);
  __builtin_amdgcn_sched_barrier(0);
#pragma nounroll
  for (int pass = DRYB ? 0 : 1; pass < 2; ++pass) {
  int real; asm volatile("s_mov_b32 %0, %1" : "=s"(real) : "s"(pass));
  int lane_o = lane; asm volatile("" : "+v"(lane_o));
  const int lane = lane_o, t = wave * 64 + lane, head = lane & 31, hi = lane >> 5;
  if (real) {
  if (PMASK & 16) wait_flagsp(wsr, WS_B2FLAG + (wave * 8 + (lane & 7)) * 8, tag); else wait_flags(ws, WS_B2FLAG + (wave * 8 + (lane & 7)) * 8, true, tag);
  STAMP(6);
  {
    u32x4 vh[4], vl[4];
#pragma unroll
    for (int i = 0; i < 4; ++i) {
      vh[i] = ld128(wsr, WS_QH + b3b * 32768 + ((wave * 4 + i) * 64 + lane) * 16);
      vl[i] = ld128(wsr, WS_QL + b3b * 32768 + ((wave * 4 + i) * 64 + lane) * 16);
    }
#pragma unroll
    for (int i = 0; i < 4; ++i) {
      *(u32x4*)(lds + L_QH + (wave * 4 + i) * CSTR + lane * 16) = vh[i];
      *(u32x4*)(lds + L_QL + (wave * 4 + i) * CSTR + lane * 16) = vl[i];
    }
  }
  }
  BAR();
  STAMP(7);
  {
    float* smax = (float*)(lds + L_ST);
    float* slsum = smax + 128;
    f32x16 sacc;
#pragma unroll
    for (int r = 0; r < 16; ++r) sacc[r] = 0.f;
    {
      const unsigned char* bhp = lds + L_QH + head * CSTR + (256 * kh + 8 * hi) * 2;
      const unsigned char* blp = lds + L_QL + head * CSTR + (256 * kh + 8 * hi) * 2;
#pragma unroll
      for (int s = 0; s < 16; ++s) {
        const bf16x8 bh = __builtin_bit_cast(bf16x8, *(const u32x4*)(bhp + s * 32));
        const bf16x8 bl = __builtin_bit_cast(bf16x8, *(const u32x4*)(blp + s * 32));
        sacc = __builtin_amdgcn_mfma_f32_32x32x16_bf16(__builtin_bit_cast(bf16x8, cf[s]), bh, sacc, 0, 0, 0);
        sacc = __builtin_amdgcn_mfma_f32_32x32x16_bf16(__builtin_bit_cast(bf16x8, cf[s]), bl, sacc, 0, 0, 0);
      }
    }
  STAMP(8);
    if (kh == 1) {
#pragma unroll
      for (int r4 = 0; r4 < 4; ++r4)
        *(f32x4*)(lds + L_XB + ((kb * 4 + r4) * 64 + lane) * 16) = f32x4{sacc[4 * r4], sacc[4 * r4 + 1], sacc[4 * r4 + 2], sacc[4 * r4 + 3]};
    }
    BAR();
    float sv[16];
    if (kh == 0) {
      float mx = -INFINITY;
#pragma unroll
      for (int r4 = 0; r4 < 4; ++r4) {
        const f32x4 o = *(const f32x4*)(lds + L_XB + ((kb * 4 + r4) * 64 + lane) * 16);
#pragma unroll
        for (int e = 0; e < 4; ++e) { sv[4 * r4 + e] = bfr(bfr(sacc[4 * r4 + e] + o[e]) * SCALE); mx = fmaxf(mx, sv[4 * r4 + e]); }
      }
      mx = xmax32(mx);
      if (lane < 32) smax[kb * 32 + head] = mx;
    }
    BAR();
    if (kh == 0) {
      const float m = fmaxf(fmaxf(smax[head], smax[32 + head]), fmaxf(smax[64 + head], smax[96 + head]));
      float ls = 0.f;
#pragma unroll
      for (int r4 = 0; r4 < 4; ++r4) {
        float pe[4];
#pragma unroll
        for (int e = 0; e < 4; ++e) { pe[e] = ex2((sv[4 * r4 + e] - m) * L2E); ls += pe[e]; }
        *(u32x2*)(lds + L_P + head * 272 + (32 * kb + 8 * r4 + 4 * hi) * 2) = u32x2{pack_bf16(pe[0], pe[1]), pack_bf16(pe[2], pe[3])};
      }
      ls = xsum32(ls);
      if (lane < 32) slsum[kb * 32 + head] = ls;
    }
    BAR();
  STAMP(9);
    f32x16 oacc0, oacc1;
#pragma unroll
    for (int r = 0; r < 16; ++r) { oacc0[r] = 0.f; oacc1[r] = 0.f; }
    {
      const unsigned char* pa = lds + L_P + head * 272 + hi * 16;
#pragma unroll
      for (int ks = 0; ks < 8; ++ks) {
        const bf16x8 a = __builtin_bit_cast(bf16x8, *(const u32x4*)(pa + ks * 32));
        oacc0 = __builtin_amdgcn_mfma_f32_32x32x16_bf16(pv[0][ks], a, oacc0, 0, 0, 0);
        oacc1 = __builtin_amdgcn_mfma_f32_32x32x16_bf16(pv[1][ks], a, oacc1, 0, 0, 0);
      }
    }
  STAMP(10);
    {
      unsigned char* ot = lds + L_OT + wave * 4608;
#pragma unroll
      for (int r4 = 0; r4 < 4; ++r4) {
        *(u32x2*)(ot + head * 144 + (8 * r4 + 4 * hi) * 2) = u32x2{pack_bf16(oacc0[4 * r4], oacc0[4 * r4 + 1]), pack_bf16(oacc0[4 * r4 + 2], oacc0[4 * r4 + 3])};
        *(u32x2*)(ot + head * 144 + (32 + 8 * r4 + 4 * hi) * 2) = u32x2{pack_bf16(oacc1[4 * r4], oacc1[4 * r4 + 1]), pack_bf16(oacc1[4 * r4 + 2], oacc1[4 * r4 + 3])};
      }
      LGKM0();
#pragma unroll
      for (int i = 0; i < 4; ++i) {
        const int hrow = (lane >> 3) + 8 * i, ch = lane & 7;
        const u32x4 v = *(const u32x4*)(ot + hrow * 144 + ch * 16);
        if (real) st128(wsr, WS_BPART + (((b3b * 32 + b3ks) * 32 + hrow) * 512 + 64 * wave + 8 * ch) * 2, v);
        else asm volatile("" :: "v"(v));
      }
    }
    if (t < 32) {
      const float L = (slsum[t] + slsum[32 + t]) + (slsum[64 + t] + slsum[96 + t]);
      const float m = fmaxf(fmaxf(smax[t], smax[32 + t]), fmaxf(smax[64 + t], smax[96 + t]));
      if (real) st64(wsr, WS_BML + ((b3b * 32 + b3ks) * 32 + t) * 8, u32x2{__float_as_uint(m), __float_as_uint(L)});
    }
    if (real) DRAIN();
    BAR();
    if (t == 0 && real) flag_set(ws, WS_B3FLAG + j * 8, tag);
  STAMP(11);
  }
  }
  {
#pragma unroll
    for (int i = 0; i < 9; ++i) {
      const int L = i * 512 + t, r = L / 72, c = L - r * 72;
      const int pc = (((c >> 1) ^ ((r >> 1) & 3)) << 1) | (c & 1);
      *(u32x4*)(lds + L_WO + (r * 72 + pc) * 16) = wo[i];
    }
  }
#pragma nounroll
  for (int pass = DRYC ? 0 : 1; pass < 2; ++pass) {
  int real; asm volatile("s_mov_b32 %0, %1" : "=s"(real) : "s"(pass));
  int lane_o = lane; asm volatile("" : "+v"(lane_o));
  const int lane = lane_o, t = wave * 64 + lane, g4 = lane >> 4, i16 = lane & 15, qq = i16 >> 2, pp = i16 & 3;
  {
    if (wave == 0 && real) { if (PMASK & 32) wait_flagsp(wsr, WS_B3FLAG + lane * 8, tag); else wait_flags(ws, WS_B3FLAG + lane * 8, true, tag); }
    BAR();
  STAMP(12);
    const int rg = lane >> 3, ch = lane & 7;
    u32x4 g[8]; u32x2 ml = {0u, 0u};
#pragma unroll
    for (int i = 0; i < 8; ++i) g[i] = u32x4{0u, 0u, 0u, 0u};
    if (real) {
#pragma unroll
    for (int i = 0; i < 8; ++i) g[i] = ld128(wsr, WS_BPART + (((rg + 8 * i) * 32 + hh) * 512 + 64 * wave + 8 * ch) * 2);
    ml = ld64(wsr, WS_BML + (lane * 32 + hh) * 8);
    }
    float wgt;
    {
      const float m = __uint_as_float(ml[0]), l = __uint_as_float(ml[1]);
      float M = m;
      M = fmaxf(M, dppf<0x128>(M)); M = fmaxf(M, dppf<0x124>(M)); M = fmaxf(M, dppf<0x122>(M)); M = fmaxf(M, dppf<0x121>(M)); M = xmax16(M);
      const float w = ex2((m - M) * L2E);
      const float L = xsum16(row16_sum(w * l));
      wgt = w / L;
    }
    float s0[8], s1[8];
#pragma unroll
    for (int k = 0; k < 8; ++k) { s0[k] = 0.f; s1[k] = 0.f; }
#pragma unroll
    for (int i = 0; i < 4; ++i) {
      const float w0 = __shfl(wgt, rg + 8 * i), w1 = __shfl(wgt, 32 + rg + 8 * i);
#pragma unroll
      for (int k = 0; k < 4; ++k) {
        s0[2 * k] = fmaf(w0, bflo(g[i][k]), s0[2 * k]); s0[2 * k + 1] = fmaf(w0, bfhi(g[i][k]), s0[2 * k + 1]);
        s1[2 * k] = fmaf(w1, bflo(g[i + 4][k]), s1[2 * k]); s1[2 * k + 1] = fmaf(w1, bfhi(g[i + 4][k]), s1[2 * k + 1]);
      }
    }
#pragma unroll
    for (int k = 0; k < 8; ++k) {
      float x = s0[k]; x += dppf<0x128>(x); x = xsum16(x); x = xsum32(x); s0[k] = x;
      float y = s1[k]; y += dppf<0x128>(y); y = xsum16(y); y = xsum32(y); s1[k] = y;
    }
  STAMP(13);
    unsigned char* ca = lds + L_CA + wave * 512;
    if (lane < 8) {
      u32x4 h0, l0, h1, l1;
#pragma unroll
      for (int k = 0; k < 4; ++k) {
        const float a = bfr(s0[2 * k]), bq = bfr(s0[2 * k + 1]), c = bfr(s1[2 * k]), d = bfr(s1[2 * k + 1]);
        h0[k] = pack_bf16(a, bq); l0[k] = pack_bf16(s0[2 * k] - a, s0[2 * k + 1] - bq);
        h1[k] = pack_bf16(c, d); l1[k] = pack_bf16(s1[2 * k] - c, s1[2 * k + 1] - d);
      }
      *(u32x4*)(ca + 0 * 128 + lane * 16) = h0; *(u32x4*)(ca + 1 * 128 + lane * 16) = l0;
      *(u32x4*)(ca + 2 * 128 + lane * 16) = h1; *(u32x4*)(ca + 3 * 128 + lane * 16) = l1;
    }
    LGKM0();
    bf16x8 af[2];
#pragma unroll
    for (int ks = 0; ks < 2; ++ks) {
      u32x2 lo = {0u, 0u}, hq = {0u, 0u};
      if (i16 < 4) {
        lo = *(const u32x2*)(ca + i16 * 128 + (32 * ks + 4 * g4) * 2);
        hq = *(const u32x2*)(ca + i16 * 128 + (32 * ks + 16 + 4 * g4) * 2);
      }
      af[ks] = __builtin_bit_cast(bf16x8, u32x4{lo[0], lo[1], hq[0], hq[1]});
    }
    float* red2 = (float*)(lds + L_RED2);
    {
      const int r0 = 4 * g4 + qq, sw = (r0 >> 1) & 3;
#pragma unroll
      for (int nb = 0; nb < 4; ++nb) {
        f32x4 d = {0.f, 0.f, 0.f, 0.f};
        const unsigned char* base = lds + L_WUV + (64 * wave + r0) * 128 + ((nb ^ sw) * 32) + pp * 8;
#pragma unroll
        for (int ks = 0; ks < 2; ++ks) {
          const bf16x8 b = tr_pair(base + (32 * ks) * 128, base + (32 * ks + 16) * 128);
          d = __builtin_amdgcn_mfma_f32_16x16x32_bf16(af[ks], b, d, 0, 0, 0);
        }
        if (lane < 16) { red2[(wave * 2 + 0) * 64 + 16 * nb + lane] = d[0] + d[1]; red2[(wave * 2 + 1) * 64 + 16 * nb + lane] = d[2] + d[3]; }
      }
    }
    if (real) DRAIN();
    BAR();
    if (t < 64) {
      float2 s = *(const float2*)(red2 + t * 2);
#pragma unroll
      for (int w = 1; w < 8; ++w) { const float2 v = *(const float2*)(red2 + w * 128 + t * 2); s.x += v.x; s.y += v.y; }
      *(u32*)(lds + L_SCR + 2048 + t * 4) = pack_bf16(s.x, s.y);
    }
    BAR();
  }
  STAMP(14);
  proj_mfma(lds, L_WO, wsr, WS_OUTB + j * 9216, tag, wave, lane, L_SCR + 2048, real != 0);
  if (j < 36) final_reduce(lds, wsr, WS_OUTB, tag, j, t, p.out, 576, real != 0);
  if (!real) BAR();
  }
  STAMP(15);
  STAMP_FLUSH();
}

extern "C" void kernel_launch(void* const* d_in, const int* in_sizes, int n_in, void* d_out, int out_size, void* d_ws, size_t ws_size,
                              hipStream_t stream) {
  Params p;
  p.Ax = (const u16*)d_in[0]; p.Ak = (const u16*)d_in[1]; p.Av = (const u16*)d_in[2]; p.Awq = (const u16*)d_in[3]; p.Awo = (const u16*)d_in[4];
  p.Bx = (const u16*)d_in[5]; p.Bc = (const u16*)d_in[6]; p.Bwq = (const u16*)d_in[7]; p.Bwuk = (const u16*)d_in[8]; p.Bwuv = (const u16*)d_in[9];
  p.Bwo = (const u16*)d_in[10];
  p.out = (u16*)d_out; p.ws = (unsigned char*)d_ws;
  fused_kernel<<<dim3(NWG), dim3(NT), 0, stream>>>(p);
}
```

```cpp
#define PROBE_A 99
#define PROBE_C 99
#define PROBE_B 99
#define PROBE_MUL 0
#include <hip/hip_runtime.h>
#include <stdint.h>
#define PMASK 8
#define NOMASK
#define DRYB 0
#define DRYC 0
#define DRY 0
#define EARLYWO 9
#define WOEARLY
#define U3 44
#define WUVPOS 0
#define QCS 0
#define BCS 0
#define Q2 0
#define WOPOS 0

typedef unsigned short u16;
typedef unsigned int u32;
typedef unsigned long long u64;
typedef u32 u32x4 __attribute__((ext_vector_type(4)));
typedef u32 u32x2 __attribute__((ext_vector_type(2)));
typedef float f32x4 __attribute__((ext_vector_type(4)));
typedef float f32x16 __attribute__((ext_vector_type(16)));
typedef __bf16 bf16x2 __attribute__((ext_vector_type(2)));
typedef __bf16 bf16x8 __attribute__((ext_vector_type(8)));
typedef short s16x4 __attribute__((ext_vector_type(4)));
typedef short s16x8 __attribute__((ext_vector_type(8)));
#define GAS __attribute__((address_space(1)))
#define LAS __attribute__((address_space(3)))

#define GSLEEP 1
#define FSLEEP 1
constexpr int NT = 512;
constexpr int NWG = 256;
constexpr int ASPLIT = 6;
constexpr float SCALE = 0.08838834764831845f;
constexpr float L2E = 1.4426950408889634f;
constexpr int SPIN_MAX = 300000;

constexpr u32 WS_EPOCH = 0;
constexpr u32 WS_B2FLAG = 256;
constexpr u32 WS_B3FLAG = WS_B2FLAG + 512;
constexpr u32 WS_QG = 4096;
constexpr u32 WS_QH = WS_QG + 65536;
constexpr u32 WS_QL = WS_QH + 65536;
constexpr u32 WS_BML = WS_QL + 65536;
constexpr u32 WS_OUTB = WS_BML + 16384;
constexpr u32 WS_OUTA = WS_OUTB + 589824;
constexpr u32 WS_APG = WS_OUTA + 589824;
constexpr u32 WS_BPART = WS_APG + 401408;
constexpr u32 WS_BG = WS_BPART + 2097152;
constexpr u32 WS_QF = WS_BML;
constexpr u32 WS_QG2 = WS_BG + 262144;
constexpr u32 WS_PROBE = WS_QG2 + 262144;
constexpr u32 WS_END = WS_PROBE + 4096;

constexpr int CSTR = 1040;
constexpr int L_C = 0;
constexpr int L_WUV = 0;
constexpr int L_QH = 65536;
constexpr int L_QL = 98816;
constexpr int L_OT = 65536;
constexpr int L_WO = 65536;
constexpr int L_CA = 139264;
constexpr int L_RED2 = 143360;
constexpr int L_XB = 133120;
constexpr int L_P = 149504;
constexpr int L_ST = 158208;
constexpr int L_SCR = 159744;
constexpr int L_MRG = 81920;
constexpr int LDS_BYTES = 163840;

struct Params {
  const u16* Ax; const u16* Ak; const u16* Av; const u16* Awq; const u16* Awo;
  const u16* Bx; const u16* Bc; const u16* Bwq; const u16* Bwuk; const u16* Bwuv; const u16* Bwo;
  u16* out; unsigned char* ws;
};

__device__ __forceinline__ float bf2f(u16 v) { return __uint_as_float(((u32)v) << 16); }
__device__ __forceinline__ float bflo(u32 v) { return __uint_as_float(v << 16); }
__device__ __forceinline__ float bfhi(u32 v) { return __uint_as_float(v & 0xffff0000u); }
__device__ __forceinline__ float bfr(float f) { return (float)(__bf16)f; }
__device__ __forceinline__ u32 pack_bf16(float lo, float hi) {
  bf16x2 v = {(__bf16)lo, (__bf16)hi};
  return __builtin_bit_cast(u32, v);
}
__device__ __forceinline__ float dot2(u32 a, u32 b, float c) {
  return __builtin_amdgcn_fdot2_f32_bf16(__builtin_bit_cast(bf16x2, a), __builtin_bit_cast(bf16x2, b), c, false);
}
template <int CTRL> __device__ __forceinline__ float dppf(float x) {
  return __builtin_bit_cast(float, __builtin_amdgcn_mov_dpp(__builtin_bit_cast(int, x), CTRL, 0xf, 0xf, true));
}
__device__ __forceinline__ float row16_sum(float x) {
  x += dppf<0x128>(x); x += dppf<0x124>(x); x += dppf<0x122>(x); x += dppf<0x121>(x);
  return x;
}
__device__ __forceinline__ float xsum16(float x) { auto r = __builtin_amdgcn_permlane16_swap(__float_as_uint(x), __float_as_uint(x), false, false); return __uint_as_float(r[0]) + __uint_as_float(r[1]); }
__device__ __forceinline__ float xsum32(float x) { auto r = __builtin_amdgcn_permlane32_swap(__float_as_uint(x), __float_as_uint(x), false, false); return __uint_as_float(r[0]) + __uint_as_float(r[1]); }
__device__ __forceinline__ float xmax16(float x) { auto r = __builtin_amdgcn_permlane16_swap(__float_as_uint(x), __float_as_uint(x), false, false); return fmaxf(__uint_as_float(r[0]), __uint_as_float(r[1])); }
__device__ __forceinline__ float xmax32(float x) { auto r = __builtin_amdgcn_permlane32_swap(__float_as_uint(x), __float_as_uint(x), false, false); return fmaxf(__uint_as_float(r[0]), __uint_as_float(r[1])); }
typedef unsigned short u16x2 __attribute__((ext_vector_type(2)));
__device__ __forceinline__ u32 udot(u32 a, u32 k, u32 c) { return __builtin_amdgcn_udot2(__builtin_bit_cast(u16x2, a), __builtin_bit_cast(u16x2, k), c, false); }
__device__ __forceinline__ u32 cskey(u32 pos) { const u32 o = 2u * pos + 1u; return ((o * 40503u) & 0xffffu) | (((o * 31153u + 0x5bd0u) | 1u) << 16); }
template <int CTRL> __device__ __forceinline__ u32 dppu(u32 x) { return (u32)__builtin_amdgcn_mov_dpp((int)x, CTRL, 0xf, 0xf, true); }
__device__ __forceinline__ u32 usum8(u32 x) { x += dppu<0xB1>(x); x += dppu<0x4E>(x); x += dppu<0x141>(x); return x; }
__device__ __forceinline__ u32 usum16x(u32 x) { auto r = __builtin_amdgcn_permlane16_swap(x, x, false, false); return r[0] + r[1]; }
__device__ __forceinline__ u32 usum32x(u32 x) { auto r = __builtin_amdgcn_permlane32_swap(x, x, false, false); return r[0] + r[1]; }
__device__ __forceinline__ u32 cs4(u32x4 d, const u32 (&k)[4]) { return usum8(udot(d[3], k[3], udot(d[2], k[2], udot(d[1], k[1], udot(d[0], k[0], 0u))))); }
__device__ __forceinline__ u32 bsel(u32 m, u32 a, u32 b) { u32 r; asm("v_bfi_b32 %0, %1, %2, %3" : "=v"(r) : "v"(m), "v"(a), "v"(b)); return r; }
__device__ __forceinline__ u32 sel8(const u32 (&v)[8], int i) {
  const u32 m0 = 0u - (u32)(i & 1), m1 = 0u - (u32)((i >> 1) & 1), m2 = 0u - (u32)((i >> 2) & 1);
  const u32 a0 = bsel(m0, v[1], v[0]), a1 = bsel(m0, v[3], v[2]), a2 = bsel(m0, v[5], v[4]), a3 = bsel(m0, v[7], v[6]);
  return bsel(m2, bsel(m1, a3, a2), bsel(m1, a1, a0));
}
__device__ __forceinline__ float ex2(float x) { return __builtin_amdgcn_exp2f(x); }

typedef __amdgpu_buffer_rsrc_t rsrc_t;
__device__ __forceinline__ u32x4 ld128(rsrc_t r, u32 off) { return __builtin_amdgcn_raw_buffer_load_b128(r, off, 0, 16); }
__device__ __forceinline__ u32x2 ld64(rsrc_t r, u32 off) { return __builtin_amdgcn_raw_buffer_load_b64(r, off, 0, 16); }
__device__ __forceinline__ void st128(rsrc_t r, u32 off, u32x4 v) { __builtin_amdgcn_raw_buffer_store_b128(v, r, off, 0, 16); }
__device__ __forceinline__ void st64(rsrc_t r, u32 off, u32x2 v) { __builtin_amdgcn_raw_buffer_store_b64(v, r, off, 0, 16); }
__device__ __forceinline__ u32x4 ldnt(const u16* p) { return __builtin_nontemporal_load((const u32x4*)p); }

__device__ __forceinline__ void flag_set(unsigned char* ws, u32 off, u64 tag) {
  __hip_atomic_store((GAS u64*)(ws + off), tag, __ATOMIC_RELAXED, __HIP_MEMORY_SCOPE_AGENT);
}
__device__ __forceinline__ u64 flag_get(unsigned char* ws, u32 off) {
  return __hip_atomic_load((GAS u64*)(ws + off), __ATOMIC_RELAXED, __HIP_MEMORY_SCOPE_AGENT);
}
#define DRAIN() asm volatile("s_waitcnt vmcnt(0)" ::: "memory")
#define BAR() asm volatile("s_waitcnt lgkmcnt(0)\n\ts_barrier" ::: "memory")
#define LGKM0() asm volatile("s_waitcnt lgkmcnt(0)" ::: "memory")

__device__ __forceinline__ void glds16(const void* gsrc, u32 lds_dst) {
  u32 keep;
  asm volatile("s_mov_b32 %0, m0\n\ts_mov_b32 m0, %2\n\ts_nop 0\n\tglobal_load_lds_dwordx4 %1, off nt\n\ts_mov_b32 m0, %0"
               : "=&s"(keep) : "v"(gsrc), "s"(lds_dst) : "memory");
}

constexpr int CODE_BYTES = 49152;
__device__ __forceinline__ void code_prefetch(u64 pc0, int lane, u32 lds_dummy) {
#pragma unroll
  for (int i = 0; i < (CODE_BYTES + 8191) / 8192; ++i) {
    int off = i * 8192 + lane * 128; off = off < CODE_BYTES - 4 ? off : CODE_BYTES - 4;
    const unsigned char* g = (const unsigned char*)pc0 + off;
    u32 keep;
    asm volatile("s_mov_b32 %0, m0\n\ts_mov_b32 m0, %2\n\ts_nop 0\n\tglobal_load_lds_dword %1, off\n\ts_mov_b32 m0, %0"
                 : "=&s"(keep) : "v"(g), "s"(lds_dummy) : "memory");
  }
}
__device__ __forceinline__ void wait_flags(unsigned char* ws, u32 off, bool active, u64 tag) {
  for (int spin = 0; spin < SPIN_MAX; ++spin) {
    u64 v = tag;
    if (active) v = flag_get(ws, off);
    if (__all(v == tag)) break;
    __builtin_amdgcn_s_sleep(FSLEEP);
  }
  __builtin_amdgcn_fence(__ATOMIC_ACQUIRE, "wavefront");
}
__device__ __forceinline__ void gst(rsrc_t r, u32 off, u32 d0, u32 d1, u64 tag) { st128(r, off, u32x4{d0, d1, (u32)tag, (u32)(tag >> 32)}); }
__device__ __forceinline__ bool gok(u32x4 g, u64 tag) { return g[2] == (u32)tag && g[3] == (u32)(tag >> 32); }
template <int N> __device__ __forceinline__ void gpoll(rsrc_t r, const u32 (&off)[N], u32x4 (&g)[N], bool need, u64 tag) {
  for (int spin = 0; spin < SPIN_MAX; ++spin) {
    bool ok = true;
#pragma unroll
    for (int i = 0; i < N; ++i) { g[i] = ld128(r, off[i]); ok = ok && gok(g[i], tag); }
    if (__all(ok || !need)) break;
    __builtin_amdgcn_s_sleep(N > 1 ? GSLEEP : 1);
  }
}

#define PSLEEP 8
#define STR2(x) #x
#define STR(x) STR2(x)
#define PCHK1(A2, A3, LBL) \
    "v_mov_b32 %[t0], " A2 "\n\tv_mov_b32 %[t1], " A3 "\n\t" \
    "v_cmp_eq_u32 vcc, %[tlo], %[t0]\n\tv_cmp_eq_u32 %[sc], %[thi], %[t1]\n\t" \
    "s_and_b64 vcc, vcc, %[sc]\n\ts_cmp_eq_u64 vcc, exec\n\ts_cbranch_scc1 " LBL "\n\t"
__device__ __forceinline__ u32x2 gpoll1p(rsrc_t r, u32 off, u64 tag) {
  u32 d0, d1, t0, t1; u64 sc; int cnt = SPIN_MAX;
  const u32 tlo = __builtin_amdgcn_readfirstlane((u32)tag), thi = __builtin_amdgcn_readfirstlane((u32)(tag >> 32));
  asm volatile(
    "buffer_load_dwordx4 v[232:235], %[off], %[rs], 0 offen sc1\n\ts_sleep " STR(PSLEEP) "\n\t"
    "buffer_load_dwordx4 v[236:239], %[off], %[rs], 0 offen sc1\n\ts_sleep " STR(PSLEEP) "\n\t"
    "buffer_load_dwordx4 v[240:243], %[off], %[rs], 0 offen sc1\n\t"
    "1:\n\t"
    "s_waitcnt vmcnt(2)\n\t" PCHK1("v234", "v235", "2f")
    "buffer_load_dwordx4 v[232:235], %[off], %[rs], 0 offen sc1\n\t"
    "s_waitcnt vmcnt(2)\n\t" PCHK1("v238", "v239", "3f")
    "buffer_load_dwordx4 v[236:239], %[off], %[rs], 0 offen sc1\n\t"
    "s_waitcnt vmcnt(2)\n\t" PCHK1("v242", "v243", "4f")
    "buffer_load_dwordx4 v[240:243], %[off], %[rs], 0 offen sc1\n\t"
    "s_sub_u32 %[cnt], %[cnt], 1\n\ts_cmp_lg_u32 %[cnt], 0\n\ts_cbranch_scc1 1b\n\t"
    "s_waitcnt vmcnt(0)\n\t"
    "2:\n\tv_mov_b32 %[d0], v232\n\tv_mov_b32 %[d1], v233\n\ts_branch 5f\n\t"
    "3:\n\tv_mov_b32 %[d0], v236\n\tv_mov_b32 %[d1], v237\n\ts_branch 5f\n\t"
    "4:\n\tv_mov_b32 %[d0], v240\n\tv_mov_b32 %[d1], v241\n\t"
    "5:\n\t"
    : [d0] "=&v"(d0), [d1] "=&v"(d1), [t0] "=&v"(t0), [t1] "=&v"(t1), [sc] "=&s"(sc), [cnt] "+s"(cnt)
    : [off] "v"(off), [rs] "s"(r), [tlo] "s"(tlo), [thi] "s"(thi)
    : "vcc", "scc", "memory", "v232", "v233", "v234", "v235", "v236", "v237", "v238", "v239", "v240", "v241", "v242", "v243");
  return u32x2{d0, d1};
}
#define PCHK2(A2, A3, B2, B3, LBL) \
    "v_mov_b32 %[t0], " A2 "\n\tv_mov_b32 %[t1], " A3 "\n\t" \
    "v_cmp_eq_u32 vcc, %[tlo], %[t0]\n\tv_cmp_eq_u32 %[sc], %[thi], %[t1]\n\t" \
    "v_mov_b32 %[t0], " B2 "\n\tv_mov_b32 %[t1], " B3 "\n\t" \
    "s_and_b64 vcc, vcc, %[sc]\n\t" \
    "v_cmp_eq_u32 %[sc], %[tlo], %[t0]\n\tv_cmp_eq_u32 %[sd], %[thi], %[t1]\n\t" \
    "s_and_b64 vcc, vcc, %[sc]\n\ts_and_b64 vcc, vcc, %[sd]\n\t" \
    "s_cmp_eq_u64 vcc, exec\n\ts_cbranch_scc1 " LBL "\n\t"
#define PLD2(A, B) "buffer_load_dwordx4 " A ", %[off0], %[rs], 0 offen sc1\n\tbuffer_load_dwordx4 " B ", %[off1], %[rs], 0 offen sc1\n\t"
#define PRD2(A0, A1, B0, B1) "v_mov_b32 %[d0], " A0 "\n\tv_mov_b32 %[d1], " A1 "\n\tv_mov_b32 %[d2], " B0 "\n\tv_mov_b32 %[d3], " B1 "\n\t"
__device__ __forceinline__ u32x4 gpoll2p(rsrc_t r, u32 off0, u32 off1, u64 tag) {
  u32 d0, d1, d2, d3, t0, t1; u64 sc, sd; int cnt = SPIN_MAX;
  const u32 tlo = __builtin_amdgcn_readfirstlane((u32)tag), thi = __builtin_amdgcn_readfirstlane((u32)(tag >> 32));
  asm volatile(
    PLD2("v[232:235]", "v[236:239]") "s_sleep " STR(PSLEEP) "\n\t"
    PLD2("v[240:243]", "v[244:247]") "s_sleep " STR(PSLEEP) "\n\t"
    PLD2("v[248:251]", "v[252:255]")
    "1:\n\t"
    "s_waitcnt vmcnt(4)\n\t" PCHK2("v234", "v235", "v238", "v239", "2f")
    PLD2("v[232:235]", "v[236:239]")
    "s_waitcnt vmcnt(4)\n\t" PCHK2("v242", "v243", "v246", "v247", "3f")
    PLD2("v[240:243]", "v[244:247]")
    "s_waitcnt vmcnt(4)\n\t" PCHK2("v250", "v251", "v254", "v255", "4f")
    PLD2("v[248:251]", "v[252:255]")
    "s_sub_u32 %[cnt], %[cnt], 1\n\ts_cmp_lg_u32 %[cnt], 0\n\ts_cbranch_scc1 1b\n\t"
    "s_waitcnt vmcnt(0)\n\t"
    "2:\n\t" PRD2("v232", "v233", "v236", "v237") "s_branch 5f\n\t"
    "3:\n\t" PRD2("v240", "v241", "v244", "v245") "s_branch 5f\n\t"
    "4:\n\t" PRD2("v248", "v249", "v252", "v253")
    "5:\n\t"
    : [d0] "=&v"(d0), [d1] "=&v"(d1), [d2] "=&v"(d2), [d3] "=&v"(d3), [t0] "=&v"(t0), [t1] "=&v"(t1), [sc] "=&s"(sc), [sd] "=&s"(sd), [cnt] "+s"(cnt)
    : [off0] "v"(off0), [off1] "v"(off1), [rs] "s"(r), [tlo] "s"(tlo), [thi] "s"(thi)
    : "vcc", "scc", "memory", "v232", "v233", "v234", "v235", "v236", "v237", "v238", "v239", "v240", "v241", "v242", "v243",
      "v244", "v245", "v246", "v247", "v248", "v249", "v250", "v251", "v252", "v253", "v254", "v255");
  return u32x4{d0, d1, d2, d3};
}
__device__ __forceinline__ void wait_flagsp(rsrc_t r, u32 off, u64 tag) {
  u32 t0, t1; u64 sc; int cnt = SPIN_MAX;
  const u32 tlo = __builtin_amdgcn_readfirstlane((u32)tag), thi = __builtin_amdgcn_readfirstlane((u32)(tag >> 32));
  asm volatile(
    "buffer_load_dwordx2 v[232:233], %[off], %[rs], 0 offen sc1\n\ts_sleep " STR(PSLEEP) "\n\t"
    "buffer_load_dwordx2 v[234:235], %[off], %[rs], 0 offen sc1\n\ts_sleep " STR(PSLEEP) "\n\t"
    "buffer_load_dwordx2 v[236:237], %[off], %[rs], 0 offen sc1\n\t"
    "1:\n\t"
    "s_waitcnt vmcnt(2)\n\t" PCHK1("v232", "v233", "2f")
    "buffer_load_dwordx2 v[232:233], %[off], %[rs], 0 offen sc1\n\t"
    "s_waitcnt vmcnt(2)\n\t" PCHK1("v234", "v235", "2f")
    "buffer_load_dwordx2 v[234:235], %[off], %[rs], 0 offen sc1\n\t"
    "s_waitcnt vmcnt(2)\n\t" PCHK1("v236", "v237", "2f")
    "buffer_load_dwordx2 v[236:237], %[off], %[rs], 0 offen sc1\n\t"
    "s_sub_u32 %[cnt], %[cnt], 1\n\ts_cmp_lg_u32 %[cnt], 0\n\ts_cbranch_scc1 1b\n\t"
    "2:\n\t"
    : [t0] "=&v"(t0), [t1] "=&v"(t1), [sc] "=&s"(sc), [cnt] "+s"(cnt)
    : [off] "v"(off), [rs] "s"(r), [tlo] "s"(tlo), [thi] "s"(thi)
    : "vcc", "scc", "memory", "v232", "v233", "v234", "v235", "v236", "v237");
}

#define STAMPS
#define STAMP(i) do { if (((i) == 0 || (i) == PROBE_A || (i) == PROBE_B || (i) == PROBE_C) && (threadIdx.x & 63) == 0) { stamps[(i)] = __builtin_amdgcn_s_memrealtime(); } } while (0)
#define STAMP_FLUSH() do { \
  const u64 tg_ = epoch + 1ull; \
  u64 sel_ = isB ? stamps[PROBE_B & 15] : (slot < 16 ? stamps[PROBE_C & 15] : stamps[PROBE_A & 15]); if (PROBE_A == 99) sel_ = __builtin_amdgcn_s_memrealtime(); if (sel_ == 0) sel_ = stamps[0]; \
  if (threadIdx.x == 0) gst(wsr, WS_PROBE + blockIdx.x * 16, (u32)stamps[0], (u32)sel_, tg_); \
  if (blockIdx.x == 0 && threadIdx.x < 64) { \
    u32 off_[4] = {WS_PROBE + lane * 16u, WS_PROBE + (64u + lane) * 16u, WS_PROBE + (128u + lane) * 16u, WS_PROBE + (192u + lane) * 16u}; u32x4 g_[4]; \
    gpoll<4>(wsr, off_, g_, true, tg_); \
    const u32 ref_ = __builtin_amdgcn_readfirstlane(g_[0][0]); \
    int mn_ = 0x7fffffff, mx_ = -0x7fffffff; \
    for (int i_ = 0; i_ < 4; ++i_) { mn_ = min(mn_, (int)(g_[i_][0] - ref_)); mx_ = max(mx_, (int)(g_[i_][1] - ref_)); } \
    for (int o_ = 32; o_ >= 1; o_ >>= 1) { mn_ = min(mn_, __shfl_xor(mn_, o_)); mx_ = max(mx_, __shfl_xor(mx_, o_)); } \
    int d_ = (mx_ - mn_) * PROBE_MUL; if (d_ > 20000) d_ = 20000; \
    const u64 s0_ = __builtin_amdgcn_s_memrealtime(); \
    while ((long long)(__builtin_amdgcn_s_memrealtime() - s0_) < (long long)d_) __builtin_amdgcn_s_sleep(1); \
  } } while (0)

__device__ __forceinline__ bf16x8 tr_pair(const unsigned char* a0, const unsigned char* a1) {
  const s16x4 t0 = __builtin_amdgcn_ds_read_tr16_b64_v4i16((LAS s16x4*)a0);
  const s16x4 t1 = __builtin_amdgcn_ds_read_tr16_b64_v4i16((LAS s16x4*)a1);
  return __builtin_bit_cast(bf16x8, __builtin_shufflevector(t0, t1, 0, 1, 2, 3, 4, 5, 6, 7));
}

__device__ __forceinline__ void proj_load(const unsigned char* lds, int wbase, int wave, int lane, bf16x8 (&wf)[5][2]) {
  const int g4 = lane >> 4, i16 = lane & 15, qq = i16 >> 2, pp = i16 & 3;
  const int r0 = 4 * g4 + qq;
  const int sw = (r0 >> 1) & 3;
#pragma unroll
  for (int i = 0; i < 5; ++i) {
    const int nb = wave + 8 * i < 36 ? wave + 8 * i : 35;
    const unsigned char* base = lds + wbase + r0 * 1152 + ((nb ^ sw) * 32) + pp * 8;
#pragma unroll
    for (int ks = 0; ks < 2; ++ks) wf[i][ks] = tr_pair(base + (32 * ks) * 1152, base + (32 * ks + 16) * 1152);
  }
}
__device__ __forceinline__ void proj_run(const unsigned char* lds, const bf16x8 (&wf)[5][2], rsrc_t wsr, u32 out_off, u64 tag, int wave, int lane, int ovb, bool st) {
  const int g4 = lane >> 4, i16 = lane & 15;
  bf16x8 af[2];
#pragma unroll
  for (int ks = 0; ks < 2; ++ks) {
    u32x2 lo = {0u, 0u}, hi = {0u, 0u};
    if (i16 < 2) {
      lo = *(const u32x2*)(lds + ovb + i16 * 128 + (32 * ks + 4 * g4) * 2);
      hi = *(const u32x2*)(lds + ovb + i16 * 128 + (32 * ks + 16 + 4 * g4) * 2);
    }
    af[ks] = __builtin_bit_cast(bf16x8, u32x4{lo[0], lo[1], hi[0], hi[1]});
  }
  f32x4 d[5];
#pragma unroll
  for (int i = 0; i < 5; ++i) {
    d[i] = f32x4{0.f, 0.f, 0.f, 0.f};
#pragma unroll
    for (int ks = 0; ks < 2; ++ks) d[i] = __builtin_amdgcn_mfma_f32_16x16x32_bf16(af[ks], wf[i][ks], d[i], 0, 0, 0);
  }
  if (lane < 16 && st) {
#pragma unroll
    for (int i = 0; i < 5; ++i) {
      const int nb = wave + 8 * i;
      if (nb < 36) gst(wsr, out_off + (16 * nb + lane) * 16, __float_as_uint(d[i][0]), __float_as_uint(d[i][1]), tag);
    }
  }
}
__device__ __forceinline__ void final_reduce(unsigned char* lds, rsrc_t wsr, u32 part_off, u64 tag, int f, int t, u16* out, int base, bool real = true) {
  const int wave = t >> 6, lane = t & 63;
  const int pidx = t >> 3, sub = t & 7;
  u32 off[2] = {part_off + (u32)(pidx * 576 + 16 * f + sub) * 16, part_off + (u32)(pidx * 576 + 16 * f + 8 + sub) * 16};
  u32x4 g[2] = {u32x4{0u, 0u, 0u, 0u}, u32x4{0u, 0u, 0u, 0u}};
  if (!real) {} else if (PMASK & 1) { const u32x4 r_ = gpoll2p(wsr, off[0], off[1], tag); g[0][0] = r_[0]; g[0][1] = r_[1]; g[1][0] = r_[2]; g[1][1] = r_[3]; } else gpoll<2>(wsr, off, g, true, tag);
  float v[4] = {__uint_as_float(g[0][0]), __uint_as_float(g[0][1]), __uint_as_float(g[1][0]), __uint_as_float(g[1][1])};
#pragma unroll
  for (int k = 0; k < 4; ++k) { float x = v[k]; x += dppf<0x128>(x); x = xsum16(x); x = xsum32(x); v[k] = x; }
  float* red = (float*)(lds + L_SCR);
  if (lane < 8) *(f32x4*)(red + wave * 32 + lane * 4) = f32x4{v[0], v[1], v[2], v[3]};
  BAR();
  if (t < 32) {
    float s = 0.f;
#pragma unroll
    for (int w = 0; w < 8; ++w) s += red[w * 32 + t];
    const int sb = t >> 2, k = t & 3, bb = k & 1, col = sb + 8 * (k >> 1);
    __bf16 o = (__bf16)s;
    if (real) out[bb * 1152 + base + 16 * f + col] = __builtin_bit_cast(u16, o);
  }
  if (real) DRAIN();
}

template <int NL, int RS>
__device__ __forceinline__ void q_load(const u16* qW, const u16* qx, int qcol0, int t, u32x4 (&qw)[NL], u16 (&qx0)[NL], u16 (&qx1)[NL]) {
  const int cch = t & 3, row0 = t >> 2;
#pragma unroll
  for (int i = 0; i < NL; ++i) {
    const int row = row0 + RS * i, rc = row < 576 ? row : 575;
    qx0[i] = qx[rc]; qx1[i] = qx[576 + rc];
  }
#pragma unroll
  for (int i = 0; i < NL; ++i) {
    const int row = row0 + RS * i, rc = row < 576 ? row : 575;
    qw[i] = ldnt(qW + (size_t)rc * 4096 + qcol0 + cch * 8);
  }
}
template <int NL, int RS>
__device__ __forceinline__ void q_fma(int t, const u32x4 (&qw)[NL], const u16 (&qx0)[NL], const u16 (&qx1)[NL], float (&qa0)[8], float (&qa1)[8]) {
#pragma unroll
  for (int k = 0; k < 8; ++k) { qa0[k] = 0.f; qa1[k] = 0.f; }
#pragma unroll
  for (int i = 0; i < NL; ++i) {
    const bool live = (t >> 2) + RS * i < 576;
    const float x0 = live ? bf2f(qx0[i]) : 0.f, x1 = live ? bf2f(qx1[i]) : 0.f;
#pragma unroll
    for (int k = 0; k < 4; ++k) {
      const float lo = bflo(qw[i][k]), hi = bfhi(qw[i][k]);
      qa0[2 * k] = fmaf(x0, lo, qa0[2 * k]); qa0[2 * k + 1] = fmaf(x0, hi, qa0[2 * k + 1]);
      qa1[2 * k] = fmaf(x1, lo, qa1[2 * k]); qa1[2 * k + 1] = fmaf(x1, hi, qa1[2 * k + 1]);
    }
  }
}
__device__ __forceinline__ void q_reduce(unsigned char* lds, int wave, int lane, float (&qa0)[8], float (&qa1)[8]) {
#pragma unroll
  for (int k = 0; k < 8; ++k) {
    float x = qa0[k]; x += dppf<0x124>(x); x += dppf<0x128>(x); x = xsum16(x); x = xsum32(x); qa0[k] = x;
    float y = qa1[k]; y += dppf<0x124>(y); y += dppf<0x128>(y); y = xsum16(y); y = xsum32(y); qa1[k] = y;
  }
  float* red_ = (float*)(lds + L_SCR);
  if (lane < 4) {
    float* r0 = red_ + wave * 64 + lane * 8;
    *(f32x4*)r0 = f32x4{qa0[0], qa0[1], qa0[2], qa0[3]}; *(f32x4*)(r0 + 4) = f32x4{qa0[4], qa0[5], qa0[6], qa0[7]};
    *(f32x4*)(r0 + 32) = f32x4{qa1[0], qa1[1], qa1[2], qa1[3]}; *(f32x4*)(r0 + 36) = f32x4{qa1[4], qa1[5], qa1[6], qa1[7]};
  }
}
template <int NW>
__device__ __forceinline__ void q_publish(unsigned char* lds, rsrc_t wsr, int qmat, int qcol0, int t, u64 tag) {
  if (t < 16) {
    const float* red_ = (const float*)(lds + L_SCR);
    f32x4 s_ = *(const f32x4*)(red_ + t * 4);
#pragma unroll
    for (int w = 1; w < NW; ++w) s_ += *(const f32x4*)(red_ + w * 64 + t * 4);
    gst(wsr, WS_QG + ((qmat * 2 + (t >> 3)) * 1024 + (qcol0 >> 2) + (t & 7)) * 16, pack_bf16(s_[0], s_[1]), pack_bf16(s_[2], s_[3]), tag);
  }
}
__device__ __forceinline__ void q2_load(const u16* qW, const u16* qx, int col0, int rbase, int t, int wave, u32x4 (&qw)[5], u16 (&qx0)[5], u16 (&qx1)[5]) {
  const int cch = t & 7, row0 = rbase + (t >> 3), row4 = row0 + (wave < 4 ? 256 : 192);
#pragma unroll
  for (int i = 0; i < 4; ++i) qw[i] = ldnt(qW + (size_t)(row0 + 64 * i) * 4096 + col0 + cch * 8);
  qw[4] = ldnt(qW + (size_t)row4 * 4096 + col0 + cch * 8);
#pragma unroll
  for (int i = 0; i < 4; ++i) { qx0[i] = qx[row0 + 64 * i]; qx1[i] = qx[576 + row0 + 64 * i]; }
  qx0[4] = qx[row4]; qx1[4] = qx[576 + row4];
}
__device__ __forceinline__ float rs32(float a, float b) { auto r = __builtin_amdgcn_permlane32_swap(__float_as_uint(a), __float_as_uint(b), false, false); return __uint_as_float(r[0]) + __uint_as_float(r[1]); }
__device__ __forceinline__ float rs16(float a, float b) { auto r = __builtin_amdgcn_permlane16_swap(__float_as_uint(a), __float_as_uint(b), false, false); return __uint_as_float(r[0]) + __uint_as_float(r[1]); }
__device__ __forceinline__ void q2_fma_reduce(unsigned char* red_b, int wave, int lane, const u32x4 (&qw)[5], const u16 (&qx0)[5], const u16 (&qx1)[5]) {
  float qa0[8], qa1[8];
  const u32 live = wave < 4 ? 0xffffu : 0u;
  {
    const u32 x0 = qx0[4] & live, x1 = qx1[4] & live, y0 = x0 << 16, y1 = x1 << 16;
#pragma unroll
    for (int k = 0; k < 4; ++k) {
      qa0[2 * k] = dot2(qw[4][k], x0, 0.f); qa0[2 * k + 1] = dot2(qw[4][k], y0, 0.f);
      qa1[2 * k] = dot2(qw[4][k], x1, 0.f); qa1[2 * k + 1] = dot2(qw[4][k], y1, 0.f);
    }
  }
#pragma unroll
  for (int i = 0; i < 4; i += 2) {
    const u32 x0 = (u32)qx0[i] | ((u32)qx0[i + 1] << 16), x1 = (u32)qx1[i] | ((u32)qx1[i + 1] << 16);
#pragma unroll
    for (int k = 0; k < 4; ++k) {
      const u32 lo = __builtin_amdgcn_perm(qw[i + 1][k], qw[i][k], 0x05040100u), hi = __builtin_amdgcn_perm(qw[i + 1][k], qw[i][k], 0x07060302u);
      qa0[2 * k] = dot2(lo, x0, qa0[2 * k]); qa0[2 * k + 1] = dot2(hi, x0, qa0[2 * k + 1]);
      qa1[2 * k] = dot2(lo, x1, qa1[2 * k]); qa1[2 * k + 1] = dot2(hi, x1, qa1[2 * k + 1]);
    }
  }
  float v[8], u[4];
#pragma unroll
  for (int k = 0; k < 8; ++k) v[k] = rs32(qa0[k], qa1[k]);
#pragma unroll
  for (int k = 0; k < 4; ++k) { u[k] = rs16(v[k], v[k + 4]); u[k] += dppf<0x128>(u[k]); }
  if (!(lane & 8)) *(f32x4*)((float*)red_b + wave * 128 + (lane >> 5) * 64 + (lane & 7) * 8 + ((lane >> 4) & 1) * 4) = f32x4{u[0], u[1], u[2], u[3]};
}
__device__ __forceinline__ void q2_publish(const unsigned char* red_b, rsrc_t wsr, int qmat, int kh2, int col0, int t, u64 tag) {
  if (t < 64) {
    const float* red_ = (const float*)red_b;
    float2 s_ = *(const float2*)(red_ + t * 2);
#pragma unroll
    for (int w = 1; w < 8; ++w) { const float2 v = *(const float2*)(red_ + w * 128 + t * 2); s_.x += v.x; s_.y += v.y; }
    gst(wsr, WS_QG2 + (u32)(((qmat * 2 + kh2) * 2 + (t >> 5)) * 2048 + (col0 >> 1) + (t & 31)) * 16, __float_as_uint(s_.x), __float_as_uint(s_.y), tag);
  }
}
__device__ __forceinline__ void q2_fetch(unsigned char* dst, rsrc_t wsr, u32 off0, u64 tag, int lane) {
  u32 off[2] = {off0, off0 + 65536};
  u32x4 g[2];
  gpoll<2>(wsr, off, g, true, tag);
  *(u32*)(dst + lane * 4) = pack_bf16(__uint_as_float(g[0][0]) + __uint_as_float(g[1][0]), __uint_as_float(g[0][1]) + __uint_as_float(g[1][1]));
}

#define LA3 1
#define LA2 1
#define ADEPTH 1
#define PREMODE 1
#define NPRE3 0
#define EARLY3 8
#define EARLY2 8
#define D1 0
#define D2 0
#define DELAY(n) do { _Pragma("unroll") for (int d_ = 0; d_ < (n); ++d_) __builtin_amdgcn_s_sleep(8); } while (0)
constexpr int U2 = (256 - 4 * U3) / 2;

#define A_LOAD(KK, VV, it) do { _Pragma("unroll") for (int i_ = 0; i_ < 4; ++i_) { KK[i_] = ldnt(Kb + (size_t)(it) * 16384 + i_ * 512); } \
                                _Pragma("unroll") for (int i_ = 0; i_ < 4; ++i_) { VV[i_] = ldnt(Vb + (size_t)(it) * 16384 + i_ * 512); } } while (0)
#define A_COMPUTE(KK, VV) do { \
      float s0_[4], s1_[4]; \
      _Pragma("unroll") for (int i_ = 0; i_ < 4; ++i_) { \
        float d0_ = 0.f, d1_ = 0.f; \
        _Pragma("unroll") for (int k_ = 0; k_ < 4; ++k_) { d0_ = dot2(KK[i_][k_], q0[k_], d0_); d1_ = dot2(KK[i_][k_], q1[k_], d1_); } \
        d0_ = row16_sum(d0_); d1_ = row16_sum(d1_); \
        s0_[i_] = bfr(bfr(d0_) * SCALE); s1_[i_] = bfr(bfr(d1_) * SCALE); } \
      const float mx0_ = fmaxf(fmaxf(s0_[0], s0_[1]), fmaxf(s0_[2], s0_[3])), mx1_ = fmaxf(fmaxf(s1_[0], s1_[1]), fmaxf(s1_[2], s1_[3])); \
      if (__any(mx0_ > m0 || mx1_ > m1)) { \
        const float n0_ = fmaxf(m0, mx0_), n1_ = fmaxf(m1, mx1_); \
        const float al0_ = ex2((m0 - n0_) * L2E), al1_ = ex2((m1 - n1_) * L2E); \
        l0 *= al0_; l1 *= al1_; m0 = n0_; m1 = n1_; \
        _Pragma("unroll") for (int k_ = 0; k_ < 8; ++k_) { acc0[k_] *= al0_; acc1[k_] *= al1_; } } \
      float p0_[4], p1_[4]; \
      _Pragma("unroll") for (int i_ = 0; i_ < 4; ++i_) { p0_[i_] = ex2((s0_[i_] - m0) * L2E); p1_[i_] = ex2((s1_[i_] - m1) * L2E); } \
      l0 += (p0_[0] + p0_[1]) + (p0_[2] + p0_[3]); l1 += (p1_[0] + p1_[1]) + (p1_[2] + p1_[3]); \
      const u32 pa0_ = pack_bf16(p0_[0], p0_[1]), pb0_ = pack_bf16(p0_[2], p0_[3]), pa1_ = pack_bf16(p1_[0], p1_[1]), pb1_ = pack_bf16(p1_[2], p1_[3]); \
      _Pragma("unroll") for (int k_ = 0; k_ < 4; ++k_) { \
        const u32 lo01_ = __builtin_amdgcn_perm(VV[1][k_], VV[0][k_], 0x05040100u), hi01_ = __builtin_amdgcn_perm(VV[1][k_], VV[0][k_], 0x07060302u); \
        const u32 lo23_ = __builtin_amdgcn_perm(VV[3][k_], VV[2][k_], 0x05040100u), hi23_ = __builtin_amdgcn_perm(VV[3][k_], VV[2][k_], 0x07060302u); \
        acc0[2 * k_] = dot2(lo23_, pb0_, dot2(lo01_, pa0_, acc0[2 * k_])); acc0[2 * k_ + 1] = dot2(hi23_, pb0_, dot2(hi01_, pa0_, acc0[2 * k_ + 1])); \
        acc1[2 * k_] = dot2(lo23_, pb1_, dot2(lo01_, pa1_, acc1[2 * k_])); acc1[2 * k_ + 1] = dot2(hi23_, pb1_, dot2(hi01_, pa1_, acc1[2 * k_ + 1])); } \
    } while (0)

#define A_LOADK(KK, it) do { _Pragma("unroll") for (int i_ = 0; i_ < 4; ++i_) { KK[i_] = ldnt(Kb + (size_t)(it) * 16384 + i_ * 512); } } while (0)
#define A_LOADV(VV, it) do { _Pragma("unroll") for (int i_ = 0; i_ < 4; ++i_) { VV[i_] = ldnt(Vb + (size_t)(it) * 16384 + i_ * 512); } } while (0)
#define A_SCORE(KK) do { \
      float s0_[4], s1_[4]; \
      _Pragma("unroll") for (int i_ = 0; i_ < 4; ++i_) { \
        float d0_ = 0.f, d1_ = 0.f; \
        _Pragma("unroll") for (int k_ = 0; k_ < 4; ++k_) { d0_ = dot2(KK[i_][k_], q0[k_], d0_); d1_ = dot2(KK[i_][k_], q1[k_], d1_); } \
        d0_ = row16_sum(d0_); d1_ = row16_sum(d1_); \
        s0_[i_] = bfr(bfr(d0_) * SCALE); s1_[i_] = bfr(bfr(d1_) * SCALE); } \
      const float mx0_ = fmaxf(fmaxf(s0_[0], s0_[1]), fmaxf(s0_[2], s0_[3])), mx1_ = fmaxf(fmaxf(s1_[0], s1_[1]), fmaxf(s1_[2], s1_[3])); \
      if (__any(mx0_ > m0 || mx1_ > m1)) { \
        const float n0_ = fmaxf(m0, mx0_), n1_ = fmaxf(m1, mx1_); \
        const float al0_ = ex2((m0 - n0_) * L2E), al1_ = ex2((m1 - n1_) * L2E); \
        l0 *= al0_; l1 *= al1_; m0 = n0_; m1 = n1_; \
        _Pragma("unroll") for (int k_ = 0; k_ < 8; ++k_) { acc0[k_] *= al0_; acc1[k_] *= al1_; } } \
      float p0_[4], p1_[4]; \
      _Pragma("unroll") for (int i_ = 0; i_ < 4; ++i_) { p0_[i_] = ex2((s0_[i_] - m0) * L2E); p1_[i_] = ex2((s1_[i_] - m1) * L2E); } \
      l0 += (p0_[0] + p0_[1]) + (p0_[2] + p0_[3]); l1 += (p1_[0] + p1_[1]) + (p1_[2] + p1_[3]); \
      pa0 = pack_bf16(p0_[0], p0_[1]); pb0 = pack_bf16(p0_[2], p0_[3]); pa1 = pack_bf16(p1_[0], p1_[1]); pb1 = pack_bf16(p1_[2], p1_[3]); \
    } while (0)
#define A_PV(VV) do { \
      _Pragma("unroll") for (int k_ = 0; k_ < 4; ++k_) { \
        const u32 lo01_ = __builtin_amdgcn_perm(VV[1][k_], VV[0][k_], 0x05040100u), hi01_ = __builtin_amdgcn_perm(VV[1][k_], VV[0][k_], 0x07060302u); \
        const u32 lo23_ = __builtin_amdgcn_perm(VV[3][k_], VV[2][k_], 0x05040100u), hi23_ = __builtin_amdgcn_perm(VV[3][k_], VV[2][k_], 0x07060302u); \
        acc0[2 * k_] = dot2(lo23_, pb0, dot2(lo01_, pa0, acc0[2 * k_])); acc0[2 * k_ + 1] = dot2(hi23_, pb0, dot2(hi01_, pa0, acc0[2 * k_ + 1])); \
        acc1[2 * k_] = dot2(lo23_, pb1, dot2(lo01_, pa1, acc1[2 * k_])); acc1[2 * k_ + 1] = dot2(hi23_, pb1, dot2(hi01_, pa1, acc1[2 * k_ + 1])); } \
    } while (0)
#define A_HSTEP(u) do { \
    if ((u) < NU || ((u) == NU && extra)) { \
      DRAIN(); A_LOADV(V0, (u)); __builtin_amdgcn_sched_barrier(0); A_SCORE(K0); __builtin_amdgcn_sched_barrier(0); \
      DRAIN(); if ((u) + 1 < NU || ((u) + 1 == NU && extra)) { A_LOADK(K0, (u) + 1); } __builtin_amdgcn_sched_barrier(0); A_PV(V0); __builtin_amdgcn_sched_barrier(0); } \
    } while (0)
#define WAITD() asm volatile("s_waitcnt vmcnt(0)" ::: "memory")
#define A_STEP(KK, VV, KN, VN, u) do { \
    if ((u) + LA < NU) { DRAIN(); A_LOAD(KN, VN, (u) + LA); } else if ((u) + LA == NU) { if (extra) { DRAIN(); A_LOAD(KN, VN, (u) + LA); } } \
    __builtin_amdgcn_sched_barrier(0); \
    if ((u) < NU) { A_COMPUTE(KK, VV); } else if ((u) == NU) { if (extra) A_COMPUTE(KK, VV); } \
    __builtin_amdgcn_sched_barrier(0); } while (0)
template <int NU, int LA>
__device__ __forceinline__ void a_stream(bool pre, bool extra, bool poll, int pollg, const u16* Kb, const u16* Vb, unsigned char* lds, rsrc_t wsr, u32 qoff, u64 tag, int lane,
                                         float& m0, float& m1, float& l0, float& l1, float (&acc0)[8], float (&acc1)[8],
                                         u32x4 (&K0)[4], u32x4 (&V0)[4], u32x4 (&K1)[4], u32x4 (&V1)[4]) {
  u32x4 K2[4], V2[4];
  if (LA == 2) { A_LOAD(K0, V0, 0); __builtin_amdgcn_sched_barrier(0); }
  if (poll) {
    u32 off[1] = {qoff};
    u32x4 g[1];
    if (PMASK & 2) { const u32x2 r_ = gpoll1p(wsr, off[0], tag); g[0][0] = r_[0]; g[0][1] = r_[1]; } else gpoll<1>(wsr, off, g, true, tag);
    *(u32x2*)(lds + L_SCR + 2048 + lane * 8) = u32x2{g[0][0], g[0][1]};
  }
  if (LA == 2) { DRAIN(); A_LOAD(K1, V1, 1); } else if (LA == 0) { A_LOADK(K0, 0); } else {
    if (!pre) { A_LOAD(K0, V0, 0); } }
  BAR();
  const u32x4 q0 = *(const u32x4*)(lds + L_SCR + 2048 + (lane & 15) * 16), q1 = *(const u32x4*)(lds + L_SCR + 2048 + 256 + (lane & 15) * 16);
  if (LA == 0) {
    u32 pa0, pb0, pa1, pb1;
    A_HSTEP(0); A_HSTEP(1); A_HSTEP(2); A_HSTEP(3); A_HSTEP(4); A_HSTEP(5); A_HSTEP(6); A_HSTEP(7);
  } else if (LA == 1) {
    A_STEP(K0, V0, K1, V1, 0); A_STEP(K1, V1, K0, V0, 1); A_STEP(K0, V0, K1, V1, 2); A_STEP(K1, V1, K0, V0, 3);
    A_STEP(K0, V0, K1, V1, 4); A_STEP(K1, V1, K0, V0, 5); A_STEP(K0, V0, K1, V1, 6); A_STEP(K1, V1, K0, V0, 7);
  } else {
    A_STEP(K0, V0, K2, V2, 0); A_STEP(K1, V1, K0, V0, 1); A_STEP(K2, V2, K1, V1, 2); A_STEP(K0, V0, K2, V2, 3);
    A_STEP(K1, V1, K0, V0, 4); A_STEP(K2, V2, K1, V1, 5); A_STEP(K0, V0, K2, V2, 6); A_STEP(K1, V1, K0, V0, 7);
  }
}

__device__ __forceinline__ u32 touch_pages(const Params& p, int lane, const u16* Kb, const u16* Vb, u32 lds_dummy) {
  const unsigned char* a = (const unsigned char*)p.Ax;
  const int g = lane >> 2, k = lane & 3;
  const u32 big = k == 3 ? 0u : (u32)k << 21;
  if (g == 1) a = (const unsigned char*)p.Awq + big;
  if (g == 2) a = (const unsigned char*)p.Awo + big;
  if (g == 3) a = (const unsigned char*)p.Bwuv + (k == 2 ? 4194300u : big);
  if (g == 4) a = (const unsigned char*)p.Bwo + big;
  if (g == 5) a = p.ws + (k == 2 ? WS_END - 4 : big);
  if (g == 6) a = (const unsigned char*)p.out;
  if (g == 7) a = (const unsigned char*)(k & 1 ? Vb : Kb) + (k & 2 ? (u32)U2 * 2048u : 0u);
  u32 v = 0;
  if (lane < 32) {
    u32 keep;
    asm volatile("s_mov_b32 %0, m0\n\ts_mov_b32 m0, %2\n\ts_nop 0\n\tglobal_load_lds_dword %1, off\n\ts_mov_b32 m0, %0"
                 : "=&s"(keep) : "v"(a), "s"(lds_dummy) : "memory");
  }
  return v;
}

__global__ void __launch_bounds__(NT, 2) fused_kernel(Params p) {
  __shared__ __attribute__((aligned(16))) unsigned char lds[LDS_BYTES];
  u64 pc0; asm volatile("s_getpc_b64 %0" : "=s"(pc0)); pc0 &= ~255ull;
  const int bid = blockIdx.x, t = threadIdx.x, lane = t & 63;
  const int wave = __builtin_amdgcn_readfirstlane(t >> 6);
  unsigned char* ws = p.ws;
  const rsrc_t wsr = __builtin_amdgcn_make_buffer_rsrc(ws, 0, WS_END, 0x00020000);
  const u32 ldsb = (u32)(uintptr_t)(LAS unsigned char*)lds;
  u64 stamps[16];
#pragma unroll
  for (int i = 0; i < 16; ++i) stamps[i] = 0;
  STAMP(0);
  u32 vz; asm volatile("v_mov_b32 %0, 0" : "=v"(vz));
  const u64 epoch = flag_get(ws + vz, WS_EPOCH);
  const int xcd = bid & 7, slot = bid >> 3;
  const bool isB = slot < 8;

  if (!isB) {
    const bool isR2 = slot < 16;
    const int r2 = (slot - 8) * 8 + xcd, r3 = (slot - 16) * 8 + xcd;
    const int pair = isR2 ? (r2 >> 1) : (r3 >> 2), sub = isR2 ? 4 + (r2 & 1) : (r3 & 3);
    const int ai = pair * 6 + sub;
    const int b = pair >> 4, hkv = pair & 15;
    const int ustart = (isR2 ? 4 * U3 + (sub - 4) * U2 : sub * U3) + (7 - wave);
    const u16* Kb = p.Ak + (size_t)pair * 4096 * 128 + (size_t)ustart * 2048 + (lane >> 4) * 128 + (lane & 15) * 8;
    const u16* Vb = p.Av + (size_t)pair * 4096 * 128 + (size_t)ustart * 2048 + (lane >> 4) * 128 + (lane & 15) * 8;
    const u32 qoff = WS_QG + (u32)((0 * 2 + b) * 1024 + (2 * hkv) * 32 + lane) * 16;
    float m0 = -INFINITY, m1 = -INFINITY, l0 = 0.f, l1 = 0.f;
    float acc0[8], acc1[8];
#pragma unroll
    for (int k = 0; k < 8; ++k) { acc0[k] = 0.f; acc1[k] = 0.f; }
    u64 tag;
    u32 tch = 0;
    u32x4 K0[4], V0[4], K1[4], V1[4];
    if (!isR2) {
      const int qc = (((slot - 16) >> 1) << 4) | (xcd << 1) | ((slot - 16) & 1);
      const int qcol0 = qc * 32;
      u32x4 qwB[5], qwA[5]; u16 bx0[5], bx1[5], ax0[5], ax1[5];
      q_load<5, 128>(p.Bwq, p.Bx, qcol0, t, qwB, bx0, bx1);
      if (wave == 7) tch = touch_pages(p, lane, Kb, Vb, ldsb + L_ST + 1024);
      __builtin_amdgcn_sched_barrier(0);
      float qa0[8], qa1[8];
      q_fma<5, 128>(t, qwB, bx0, bx1, qa0, qa1);
      __builtin_amdgcn_sched_barrier(0);
      q_load<5, 128>(p.Awq, p.Ax, qcol0, t, qwA, ax0, ax1);
      if (EARLY3 == 0 || (EARLY3 < 8 && wave >= EARLY3)) A_LOAD(K0, V0, 0);
      __builtin_amdgcn_sched_barrier(0);
      q_reduce(lds, wave, lane, qa0, qa1);
      tag = epoch + 1ull;
      BAR();
      q_publish<8>(lds, wsr, 1, qcol0, t, tag);
      STAMP(1);
      q_fma<5, 128>(t, qwA, ax0, ax1, qa0, qa1);
      q_reduce(lds - 4096, wave, lane, qa0, qa1);
      BAR();
      q_publish<8>(lds - 4096, wsr, 0, qcol0, t, tag);
      STAMP(2);
      constexpr int NLO = U3 / 8, WSPLIT = 8 - (U3 - NLO * 8);
      a_stream<NLO, LA3>(EARLY3 == 0 || (EARLY3 < 8 && wave >= EARLY3), wave >= WSPLIT, Q2 ? wave < 2 : wave == 0, wave, Kb, Vb, lds, wsr, qoff, tag, lane, m0, m1, l0, l1, acc0, acc1, K0, V0, K1, V1);
    } else {
      const int hh = r2 >> 1, half = r2 & 1;
      u32x4 wk[8];
      DELAY(D2);
#pragma unroll
      for (int i = 0; i < 8; ++i) wk[i] = ldnt(p.Bwuk + ((size_t)(256 * half + (t >> 4) + 32 * i) * 32 + hh) * 128 + (t & 15) * 8);
      if (wave == 7) tch = touch_pages(p, lane, Kb, Vb, ldsb + L_ST + 1024);
      {
        const unsigned char* src = (const unsigned char*)(p.Awo + (size_t)r2 * 64 * 576);
        for (int i = 0; i < EARLYWO; ++i) {
          const int L = i * 512 + t, r = L / 72, pc = L - r * 72;
          const int c = (((pc >> 1) ^ ((r >> 1) & 3)) << 1) | (pc & 1);
          glds16(src + r * 1152 + c * 16, ldsb + (u32)(i * 512 + wave * 64) * 16);
        }
      }
      if (EARLY2 == 0 || (EARLY2 < 8 && wave >= EARLY2)) A_LOAD(K0, V0, 0);
      __builtin_amdgcn_sched_barrier(0);
      tag = epoch + 1ull;
      if (wave == 0) {
        u32 off[1] = {WS_QG + (u32)((1 * 2 + (lane >> 5)) * 1024 + hh * 32 + (lane & 31)) * 16};
        u32x4 g[1];
        if (PMASK & 4) { const u32x2 r_ = gpoll1p(wsr, off[0], tag); g[0][0] = r_[0]; g[0][1] = r_[1]; } else gpoll<1>(wsr, off, g, true, tag);
        *(u32x2*)(lds + L_ST + lane * 8) = u32x2{g[0][0], g[0][1]};
      }
      BAR();
      STAMP(1);
      const u32x4 qa = *(const u32x4*)(lds + L_ST + (t & 15) * 16), qb = *(const u32x4*)(lds + L_ST + 256 + (t & 15) * 16);
      float* qs = (float*)(lds + L_SCR);
#pragma unroll
      for (int i = 0; i < 8; ++i) {
        float d0 = 0.f, d1 = 0.f;
#pragma unroll
        for (int k = 0; k < 4; ++k) { d0 = dot2(wk[i][k], qa[k], d0); d1 = dot2(wk[i][k], qb[k], d1); }
        d0 = row16_sum(d0); d1 = row16_sum(d1);
        if ((t & 15) == 0) { qs[(t >> 4) + 32 * i] = d0; qs[256 + (t >> 4) + 32 * i] = d1; }
      }
      BAR();
      if (wave == 0) {
        const int bb = lane >> 5, c8 = lane & 31;
        u32x4 hi, lo;
#pragma unroll
        for (int k = 0; k < 4; ++k) {
          const float v0 = qs[bb * 256 + c8 * 8 + 2 * k], v1 = qs[bb * 256 + c8 * 8 + 2 * k + 1];
          const float h0 = bfr(v0), h1 = bfr(v1);
          hi[k] = pack_bf16(h0, h1); lo[k] = pack_bf16(v0 - h0, v1 - h1);
        }
        const u32 o = ((bb * 32 + hh) * 512 + 256 * half + c8 * 8) * 2;
        st128(wsr, WS_QH + o, hi); st128(wsr, WS_QL + o, lo);
        DRAIN();
        if (lane == 0) flag_set(ws, WS_B2FLAG + r2 * 8, tag);
      }
      BAR();
      STAMP(2);
      {
        const unsigned char* src = (const unsigned char*)(p.Awo + (size_t)r2 * 64 * 576);
        for (int i = EARLYWO; i < 9; ++i) {
          const int L = i * 512 + t, r = L / 72, pc = L - r * 72;
          const int c = (((pc >> 1) ^ ((r >> 1) & 3)) << 1) | (pc & 1);
          glds16(src + r * 1152 + c * 16, ldsb + (u32)(i * 512 + wave * 64) * 16);
        }
      }
      constexpr int NLO = U2 / 8, WSPLIT = 8 - (U2 - NLO * 8);
      a_stream<NLO, LA2>(EARLY2 == 0 || (EARLY2 < 8 && wave >= EARLY2), wave >= WSPLIT, Q2 ? wave < 2 : wave == 1, wave, Kb, Vb, lds, wsr, qoff, tag, lane, m0, m1, l0, l1, acc0, acc1, K0, V0, K1, V1);
    }
    asm volatile("" :: "v"(tch));
    STAMP(3);
    float* mrg = (float*)(lds + L_MRG);
    float* mml = (float*)(lds + L_ST);
    {
      const float M0 = xmax32(xmax16(m0)), M1 = xmax32(xmax16(m1));
      const float a0 = ex2((m0 - M0) * L2E), a1 = ex2((m1 - M1) * L2E);
      l0 = xsum32(xsum16(l0 * a0)); l1 = xsum32(xsum16(l1 * a1));
#pragma unroll
      for (int k = 0; k < 8; ++k) { acc0[k] = xsum32(xsum16(acc0[k] * a0)); acc1[k] = xsum32(xsum16(acc1[k] * a1)); }
      if (lane < 16) {
        float* r0 = mrg + (wave * 2 + 0) * 128 + lane * 8; float* r1 = mrg + (wave * 2 + 1) * 128 + lane * 8;
        *(f32x4*)r0 = f32x4{acc0[0], acc0[1], acc0[2], acc0[3]}; *(f32x4*)(r0 + 4) = f32x4{acc0[4], acc0[5], acc0[6], acc0[7]};
        *(f32x4*)r1 = f32x4{acc1[0], acc1[1], acc1[2], acc1[3]}; *(f32x4*)(r1 + 4) = f32x4{acc1[4], acc1[5], acc1[6], acc1[7]};
        if (lane == 0) *(f32x4*)(mml + wave * 4) = f32x4{M0, l0, M1, l1};
      }
    }
    BAR();
    if (t < 128) {
      const int g = t >> 6, k = t & 63;
      float M = -INFINITY;
#pragma unroll
      for (int r = 0; r < 8; ++r) M = fmaxf(M, mml[r * 4 + g * 2]);
      float L = 0.f, o0 = 0.f, o1 = 0.f;
#pragma unroll
      for (int r = 0; r < 8; ++r) {
        const float w = ex2((mml[r * 4 + g * 2] - M) * L2E); L = fmaf(w, mml[r * 4 + g * 2 + 1], L);
        const float2 v = *(const float2*)(mrg + (r * 2 + g) * 128 + 2 * k);
        o0 = fmaf(w, v.x, o0); o1 = fmaf(w, v.y, o1);
      }
      gst(wsr, WS_APG + ((ai * 2 + g) * 65 + k) * 16, __float_as_uint(o0), __float_as_uint(o1), tag);
      if (k == 0) gst(wsr, WS_APG + ((ai * 2 + g) * 65 + 64) * 16, __float_as_uint(M), __float_as_uint(L), tag);
    }
    STAMP(4);
    if (isR2) {
      const int hq = r2 >> 1, dh = r2 & 1, thkv = hq >> 1, g = hq & 1;
      const int bb = lane >> 5, k = lane & 31;
      const int a0i = (bb * 16 + thkv) * 6;
      const int ovb = L_MRG + 8192 + 6144 + wave * 256;
      bf16x8 wf[5][2];
      proj_load(lds, 0, wave, lane, wf);
#pragma nounroll
      for (int pass = DRY ? 0 : 1; pass < 2; ++pass) {
        int real; asm volatile("s_mov_b32 %0, %1" : "=s"(real) : "s"(pass));
        if (real) {
          if (wave < 6) {
            const u32x4 r_ = gpoll2p(wsr, WS_APG + (((a0i + wave) * 2 + g) * 65 + 32 * dh + k) * 16, WS_APG + (((a0i + wave) * 2 + g) * 65 + 64) * 16, tag);
            *(u32x4*)(lds + L_MRG + 8192 + (wave * 64 + lane) * 16) = r_;
          }
          BAR();
          STAMP(5);
        }
        if (real || wave >= 6) {
          u32x4 gg[6];
#pragma unroll
          for (int s = 0; s < 6; ++s) gg[s] = *(const u32x4*)(lds + L_MRG + 8192 + (s * 64 + lane) * 16);
          float M = __uint_as_float(gg[0][2]);
#pragma unroll
          for (int s = 1; s < 6; ++s) M = fmaxf(M, __uint_as_float(gg[s][2]));
          float L = 0.f, o0 = 0.f, o1 = 0.f;
#pragma unroll
          for (int s = 0; s < 6; ++s) {
            const float w = ex2((__uint_as_float(gg[s][2]) - M) * L2E);
            L = fmaf(w, __uint_as_float(gg[s][3]), L); o0 = fmaf(w, __uint_as_float(gg[s][0]), o0); o1 = fmaf(w, __uint_as_float(gg[s][1]), o1);
          }
          *(u32*)(lds + ovb + bb * 128 + k * 4) = pack_bf16(o0 / L, o1 / L);
          LGKM0();
          proj_run(lds, wf, wsr, WS_OUTA + r2 * 9216, tag, wave, lane, ovb, real != 0);
        }
      }
      STAMP(6);
      DRAIN();
    } else if (r3 < 36) {
      final_reduce(lds, wsr, WS_OUTA, tag, r3, t, p.out, 0);
      if (r3 == 0) {
        if (wave == 0) { wait_flags(ws, WS_B3FLAG + lane * 8, true, tag); if (lane == 0) flag_set(ws, WS_EPOCH, tag); }
      }
      STAMP(7);
    }
    STAMP_FLUSH();
    return;
  }

  const int j = slot * 8 + xcd;
  const int hh = j >> 1, half = j & 1;
  const int b3b = j >> 5, b3ks = j & 31;
  const int kb = wave & 3, kh = wave >> 2;
  u32x4 wo[9];
  {
    DELAY(D1);
    const unsigned char* src = (const unsigned char*)(p.Bc + ((size_t)b3b * 4096 + 128 * b3ks + 16 * wave) * 512) + lane * 16;
#pragma unroll
    for (int r = 0; r < 16; ++r) glds16(src + r * 1024, ldsb + L_C + (u32)(16 * wave + r) * CSTR);
    if (wave == 7) code_prefetch(pc0, lane, ldsb + L_ST + 1024);
    if (wave == 6) { const u32 tch = touch_pages(p, lane, p.Ak, p.Av, ldsb + L_ST + 1024); asm volatile("" :: "v"(tch)); }
#define WO_LOAD(lo, hi) do { _Pragma("unroll") for (int i = (lo); i < (hi); ++i) wo[i] = ldnt(p.Bwo + (size_t)j * 64 * 576 + (size_t)(i * 512 + t) * 8); __builtin_amdgcn_sched_barrier(0); } while (0)
    __builtin_amdgcn_sched_barrier(0);
    WO_LOAD(0, 9);
    asm volatile("s_waitcnt vmcnt(9)" ::: "memory");
  }
  asm volatile("" :: "v"(epoch));
  BAR();
  STAMP(5);
  const u64 tag = epoch + 1ull;
  const int head = lane & 31, hi = lane >> 5;
  const int g4 = lane >> 4, i16 = lane & 15, qq = i16 >> 2, pp = i16 & 3;
  u32x4 cf[16];
  bf16x8 pv[2][8];
  {
    const int pi = 16 * (head >> 4) + 4 * (head & 3) + 2 * ((head >> 3) & 1) + ((head >> 2) & 1);
    const unsigned char* cr = lds + L_C + (32 * kb + pi) * CSTR + (256 * kh + 8 * hi) * 2;
#pragma unroll
    for (int s = 0; s < 16; ++s) cf[s] = *(const u32x4*)(cr + s * 32);
    const unsigned char* tb = lds + L_C + (4 * qq + 2 * hi) * CSTR + (64 * wave + 16 * (g4 & 1) + 4 * pp) * 2;
#pragma unroll
    for (int ks = 0; ks < 8; ++ks) {
      pv[0][ks] = tr_pair(tb + (16 * ks) * CSTR, tb + (16 * ks + 1) * CSTR);
      pv[1][ks] = tr_pair(tb + (16 * ks) * CSTR + 64, tb + (16 * ks + 1) * CSTR + 64);
    }
  }
  STAMP(4);
  BAR();
#define WUV_DMA() WUV_PART(0, 8)
#define WUV_PART(lo, hi) do { \
    const unsigned char* src = (const unsigned char*)(p.Bwuv + (size_t)hh * 128 + 64 * half); \
    _Pragma("unroll") for (int i = (lo); i < (hi); ++i) { \
      const int L = wave * 512 + i * 64 + lane, r = L >> 3, pc = L & 7; \
      const int c = (((pc >> 1) ^ ((r >> 1) & 3)) << 1) | (pc & 1); \
      glds16(src + (size_t)r * 8192 + c * 16, __builtin_amdgcn_readfirstlane(ldsb + L_WUV + (u32)(wave * 512 + i * 64) * 16)); } } while (0)
  if (WUVPOS == 0) WUV_DMA();
  if (WUVPOS == 4) WUV_PART(0, 4);
  u32 ck[4];
#pragma unroll
  for (int k = 0; k < 4; ++k) ck[k] = cskey(4 * (lane & 7) + k);
  __builtin_amdgcn_sched_barrier(0);
#pragma nounroll
  for (int pass = DRYB ? 0 : 1; pass < 2; ++pass) {
  int real; asm volatile("s_mov_b32 %0, %1" : "=s"(real) : "s"(pass));
  int lane_o = lane; asm volatile("" : "+v"(lane_o));
  const int lane = lane_o, t = wave * 64 + lane, head = lane & 31, hi = lane >> 5;
  if (real) {
  if (PMASK & 16) wait_flagsp(wsr, WS_B2FLAG + (wave * 8 + (lane & 7)) * 8, tag); else wait_flags(ws, WS_B2FLAG + (wave * 8 + (lane & 7)) * 8, true, tag);
  STAMP(6);
  {
    u32x4 vh[4], vl[4];
#pragma unroll
    for (int i = 0; i < 4; ++i) {
      vh[i] = ld128(wsr, WS_QH + b3b * 32768 + ((wave * 4 + i) * 64 + lane) * 16);
      vl[i] = ld128(wsr, WS_QL + b3b * 32768 + ((wave * 4 + i) * 64 + lane) * 16);
    }
#pragma unroll
    for (int i = 0; i < 4; ++i) {
      *(u32x4*)(lds + L_QH + (wave * 4 + i) * CSTR + lane * 16) = vh[i];
      *(u32x4*)(lds + L_QL + (wave * 4 + i) * CSTR + lane * 16) = vl[i];
    }
    if (WUVPOS == 1) WUV_DMA();
  }
  }
  BAR();
  STAMP(7);
  if (WUVPOS == 4) WUV_PART(4, 8);
  if (WUVPOS == 5) WUV_PART(0, 4);
  {
    float* smax = (float*)(lds + L_ST);
    float* slsum = smax + 128;
    f32x16 sacc;
#pragma unroll
    for (int r = 0; r < 16; ++r) sacc[r] = 0.f;
    {
      const unsigned char* bhp = lds + L_QH + head * CSTR + (256 * kh + 8 * hi) * 2;
      const unsigned char* blp = lds + L_QL + head * CSTR + (256 * kh + 8 * hi) * 2;
#pragma unroll
      for (int s = 0; s < 16; ++s) {
        const bf16x8 bh = __builtin_bit_cast(bf16x8, *(const u32x4*)(bhp + s * 32));
        const bf16x8 bl = __builtin_bit_cast(bf16x8, *(const u32x4*)(blp + s * 32));
        sacc = __builtin_amdgcn_mfma_f32_32x32x16_bf16(__builtin_bit_cast(bf16x8, cf[s]), bh, sacc, 0, 0, 0);
        sacc = __builtin_amdgcn_mfma_f32_32x32x16_bf16(__builtin_bit_cast(bf16x8, cf[s]), bl, sacc, 0, 0, 0);
      }
    }
  STAMP(8);
    if (WUVPOS == 2) WUV_DMA();
    if (WUVPOS == 5) WUV_PART(4, 8);
    if (WOPOS == 1) { __builtin_amdgcn_sched_barrier(0); WO_LOAD(0, 5); }
    if (kh == 1) {
#pragma unroll
      for (int r4 = 0; r4 < 4; ++r4)
        *(f32x4*)(lds + L_XB + ((kb * 4 + r4) * 64 + lane) * 16) = f32x4{sacc[4 * r4], sacc[4 * r4 + 1], sacc[4 * r4 + 2], sacc[4 * r4 + 3]};
    }
    BAR();
    float sv[16];
    if (kh == 0) {
      float mx = -INFINITY;
#pragma unroll
      for (int r4 = 0; r4 < 4; ++r4) {
        const f32x4 o = *(const f32x4*)(lds + L_XB + ((kb * 4 + r4) * 64 + lane) * 16);
#pragma unroll
        for (int e = 0; e < 4; ++e) { sv[4 * r4 + e] = bfr(bfr(sacc[4 * r4 + e] + o[e]) * SCALE); mx = fmaxf(mx, sv[4 * r4 + e]); }
      }
      mx = xmax32(mx);
      if (lane < 32) smax[kb * 32 + head] = mx;
    }
    BAR();
    if (kh == 0) {
      const float m = fmaxf(fmaxf(smax[head], smax[32 + head]), fmaxf(smax[64 + head], smax[96 + head]));
      float ls = 0.f;
#pragma unroll
      for (int r4 = 0; r4 < 4; ++r4) {
        float pe[4];
#pragma unroll
        for (int e = 0; e < 4; ++e) { pe[e] = ex2((sv[4 * r4 + e] - m) * L2E); ls += pe[e]; }
        *(u32x2*)(lds + L_P + head * 272 + (32 * kb + 8 * r4 + 4 * hi) * 2) = u32x2{pack_bf16(pe[0], pe[1]), pack_bf16(pe[2], pe[3])};
      }
      ls = xsum32(ls);
      if (lane < 32) slsum[kb * 32 + head] = ls;
    }
    BAR();
  STAMP(9);
    if (WUVPOS == 3) WUV_DMA();
    if (WOPOS == 1) { __builtin_amdgcn_sched_barrier(0); WO_LOAD(5, 9); }
    if (WOPOS == 2) { __builtin_amdgcn_sched_barrier(0); WO_LOAD(0, 5); }
    f32x16 oacc0, oacc1;
#pragma unroll
    for (int r = 0; r < 16; ++r) { oacc0[r] = 0.f; oacc1[r] = 0.f; }
    {
      const unsigned char* pa = lds + L_P + head * 272 + hi * 16;
#pragma unroll
      for (int ks = 0; ks < 8; ++ks) {
        const bf16x8 a = __builtin_bit_cast(bf16x8, *(const u32x4*)(pa + ks * 32));
        oacc0 = __builtin_amdgcn_mfma_f32_32x32x16_bf16(pv[0][ks], a, oacc0, 0, 0, 0);
        oacc1 = __builtin_amdgcn_mfma_f32_32x32x16_bf16(pv[1][ks], a, oacc1, 0, 0, 0);
      }
    }
  STAMP(10);
    if (WOPOS == 2) { __builtin_amdgcn_sched_barrier(0); WO_LOAD(5, 9); }
    {
      unsigned char* ot = lds + L_OT + wave * 4608;
#pragma unroll
      for (int r4 = 0; r4 < 4; ++r4) {
        *(u32x2*)(ot + head * 144 + (8 * r4 + 4 * hi) * 2) = u32x2{pack_bf16(oacc0[4 * r4], oacc0[4 * r4 + 1]), pack_bf16(oacc0[4 * r4 + 2], oacc0[4 * r4 + 3])};
        *(u32x2*)(ot + head * 144 + (32 + 8 * r4 + 4 * hi) * 2) = u32x2{pack_bf16(oacc1[4 * r4], oacc1[4 * r4 + 1]), pack_bf16(oacc1[4 * r4 + 2], oacc1[4 * r4 + 3])};
      }
      LGKM0();
#pragma unroll
      for (int i = 0; i < 4; ++i) {
        const int hrow = (lane >> 3) + 8 * i, ch = lane & 7;
        const u32x4 v = *(const u32x4*)(ot + hrow * 144 + ch * 16);
        if (real) st128(wsr, WS_BPART + (((b3b * 32 + b3ks) * 32 + hrow) * 512 + 64 * wave + 8 * ch) * 2, v);
        else asm volatile("" :: "v"(v));
      }
    }
    if (t < 32) {
      const float L = (slsum[t] + slsum[32 + t]) + (slsum[64 + t] + slsum[96 + t]);
      const float m = fmaxf(fmaxf(smax[t], smax[32 + t]), fmaxf(smax[64 + t], smax[96 + t]));
      if (real) st64(wsr, WS_BML + ((b3b * 32 + b3ks) * 32 + t) * 8, u32x2{__float_as_uint(m), __float_as_uint(L)});
    }
    if (real) DRAIN();
    BAR();
    if (t == 0 && real) flag_set(ws, WS_B3FLAG + j * 8, tag);
  STAMP(11);
  }
  }
  {
#pragma unroll
    for (int i = 0; i < 9; ++i) {
      const int L = i * 512 + t, r = L / 72, c = L - r * 72;
      const int pc = (((c >> 1) ^ ((r >> 1) & 3)) << 1) | (c & 1);
      *(u32x4*)(lds + L_WO + (r * 72 + pc) * 16) = wo[i];
    }
  }
  bf16x8 uf[4][2], wf[5][2];
  {
    const int r0 = 4 * g4 + qq, sw = (r0 >> 1) & 3;
#pragma unroll
    for (int nb = 0; nb < 4; ++nb) {
      const unsigned char* base = lds + L_WUV + (64 * wave + r0) * 128 + ((nb ^ sw) * 32) + pp * 8;
#pragma unroll
      for (int ks = 0; ks < 2; ++ks) uf[nb][ks] = tr_pair(base + (32 * ks) * 128, base + (32 * ks + 16) * 128);
    }
  }
  BAR();
  proj_load(lds, L_WO, wave, lane, wf);
#pragma nounroll
  for (int pass = DRYC ? 0 : 1; pass < 2; ++pass) {
  int real; asm volatile("s_mov_b32 %0, %1" : "=s"(real) : "s"(pass));
  int lane_o = lane; asm volatile("" : "+v"(lane_o));
  const int lane = lane_o, t = wave * 64 + lane, g4 = lane >> 4, i16 = lane & 15, qq = i16 >> 2, pp = i16 & 3;
  {
    if (wave == 0 && real) { if (PMASK & 32) wait_flagsp(wsr, WS_B3FLAG + lane * 8, tag); else wait_flags(ws, WS_B3FLAG + lane * 8, true, tag); }
    BAR();
  STAMP(12);
    const int rg = lane >> 3, ch = lane & 7;
    u32x4 g[8]; u32x2 ml = {0u, 0u};
#pragma unroll
    for (int i = 0; i < 8; ++i) g[i] = u32x4{0u, 0u, 0u, 0u};
    if (real) {
#pragma unroll
    for (int i = 0; i < 8; ++i) g[i] = ld128(wsr, WS_BPART + (((rg + 8 * i) * 32 + hh) * 512 + 64 * wave + 8 * ch) * 2);
    ml = ld64(wsr, WS_BML + (lane * 32 + hh) * 8);
    }
    float wgt;
    {
      const float m = __uint_as_float(ml[0]), l = __uint_as_float(ml[1]);
      float M = m;
      M = fmaxf(M, dppf<0x128>(M)); M = fmaxf(M, dppf<0x124>(M)); M = fmaxf(M, dppf<0x122>(M)); M = fmaxf(M, dppf<0x121>(M)); M = xmax16(M);
      const float w = ex2((m - M) * L2E);
      const float L = xsum16(row16_sum(w * l));
      wgt = w / L;
    }
    float s0[8], s1[8];
#pragma unroll
    for (int k = 0; k < 8; ++k) { s0[k] = 0.f; s1[k] = 0.f; }
#pragma unroll
    for (int i = 0; i < 4; ++i) {
      const float w0 = __shfl(wgt, rg + 8 * i), w1 = __shfl(wgt, 32 + rg + 8 * i);
#pragma unroll
      for (int k = 0; k < 4; ++k) {
        s0[2 * k] = fmaf(w0, bflo(g[i][k]), s0[2 * k]); s0[2 * k + 1] = fmaf(w0, bfhi(g[i][k]), s0[2 * k + 1]);
        s1[2 * k] = fmaf(w1, bflo(g[i + 4][k]), s1[2 * k]); s1[2 * k + 1] = fmaf(w1, bfhi(g[i + 4][k]), s1[2 * k + 1]);
      }
    }
#pragma unroll
    for (int k = 0; k < 8; ++k) {
      float x = s0[k]; x += dppf<0x128>(x); x = xsum16(x); x = xsum32(x); s0[k] = x;
      float y = s1[k]; y += dppf<0x128>(y); y = xsum16(y); y = xsum32(y); s1[k] = y;
    }
  STAMP(13);
    unsigned char* ca = lds + L_CA + wave * 512;
    if (lane < 8) {
      u32x4 h0, l0, h1, l1;
#pragma unroll
      for (int k = 0; k < 4; ++k) {
        const float a = bfr(s0[2 * k]), bq = bfr(s0[2 * k + 1]), c = bfr(s1[2 * k]), d = bfr(s1[2 * k + 1]);
        h0[k] = pack_bf16(a, bq); l0[k] = pack_bf16(s0[2 * k] - a, s0[2 * k + 1] - bq);
        h1[k] = pack_bf16(c, d); l1[k] = pack_bf16(s1[2 * k] - c, s1[2 * k + 1] - d);
      }
      *(u32x4*)(ca + 0 * 128 + lane * 16) = h0; *(u32x4*)(ca + 1 * 128 + lane * 16) = l0;
      *(u32x4*)(ca + 2 * 128 + lane * 16) = h1; *(u32x4*)(ca + 3 * 128 + lane * 16) = l1;
    }
    LGKM0();
    bf16x8 af[2];
#pragma unroll
    for (int ks = 0; ks < 2; ++ks) {
      u32x2 lo = {0u, 0u}, hq = {0u, 0u};
      if (i16 < 4) {
        lo = *(const u32x2*)(ca + i16 * 128 + (32 * ks + 4 * g4) * 2);
        hq = *(const u32x2*)(ca + i16 * 128 + (32 * ks + 16 + 4 * g4) * 2);
      }
      af[ks] = __builtin_bit_cast(bf16x8, u32x4{lo[0], lo[1], hq[0], hq[1]});
    }
    float* red2 = (float*)(lds + L_RED2);
    {
#pragma unroll
      for (int nb = 0; nb < 4; ++nb) {
        f32x4 d = {0.f, 0.f, 0.f, 0.f};
#pragma unroll
        for (int ks = 0; ks < 2; ++ks) d = __builtin_amdgcn_mfma_f32_16x16x32_bf16(af[ks], uf[nb][ks], d, 0, 0, 0);
        if (lane < 16) { red2[(wave * 2 + 0) * 64 + 16 * nb + lane] = d[0] + d[1]; red2[(wave * 2 + 1) * 64 + 16 * nb + lane] = d[2] + d[3]; }
      }
    }
    BAR();
    {
      float2 s = *(const float2*)(red2 + lane * 2);
#pragma unroll
      for (int w = 1; w < 8; ++w) { const float2 v = *(const float2*)(red2 + w * 128 + lane * 2); s.x += v.x; s.y += v.y; }
      *(u32*)(lds + L_CA + wave * 512 + lane * 4) = pack_bf16(s.x, s.y);
      LGKM0();
    }
  }
  STAMP(14);
  proj_run(lds, wf, wsr, WS_OUTB + j * 9216, tag, wave, lane, L_CA + wave * 512, real != 0);
  if (j < 36) final_reduce(lds, wsr, WS_OUTB, tag, j, t, p.out, 576, real != 0);
  if (!real) BAR();
  }
  STAMP(15);
  STAMP_FLUSH();
}

extern "C" void kernel_launch(void* const* d_in, const int* in_sizes, int n_in, void* d_out, int out_size, void* d_ws, size_t ws_size,
                              hipStream_t stream) {
  Params p;
  p.Ax = (const u16*)d_in[0]; p.Ak = (const u16*)d_in[1]; p.Av = (const u16*)d_in[2]; p.Awq = (const u16*)d_in[3]; p.Awo = (const u16*)d_in[4];
  p.Bx = (const u16*)d_in[5]; p.Bc = (const u16*)d_in[6]; p.Bwq = (const u16*)d_in[7]; p.Bwuk = (const u16*)d_in[8]; p.Bwuv = (const u16*)d_in[9];
  p.Bwo = (const u16*)d_in[10];
  p.out = (u16*)d_out; p.ws = (unsigned char*)d_ws;
  fused_kernel<<<dim3(NWG), dim3(NT), 0, stream>>>(p);
}
```

```cpp
#include <hip/hip_runtime.h>
#include <stdint.h>
#define PMASK 8
#define NOMASK
#define DRYB 0
#define DRYC 0
#define DRY 0
#define EARLYWO 9
#define WOEARLY
#define U3 44

typedef unsigned short u16;
typedef unsigned int u32;
typedef unsigned long long u64;
typedef u32 u32x4 __attribute__((ext_vector_type(4)));
typedef u32 u32x2 __attribute__((ext_vector_type(2)));
typedef float f32x4 __attribute__((ext_vector_type(4)));
typedef float f32x16 __attribute__((ext_vector_type(16)));
typedef __bf16 bf16x2 __attribute__((ext_vector_type(2)));
typedef __bf16 bf16x8 __attribute__((ext_vector_type(8)));
typedef short s16x4 __attribute__((ext_vector_type(4)));
typedef short s16x8 __attribute__((ext_vector_type(8)));
#define GAS __attribute__((address_space(1)))
#define LAS __attribute__((address_space(3)))

#define GSLEEP 1
#define FSLEEP 1
constexpr int NT = 512;
constexpr int NWG = 256;
constexpr int ASPLIT = 6;
constexpr float SCALE = 0.08838834764831845f;
constexpr float L2E = 1.4426950408889634f;
constexpr int SPIN_MAX = 300000;

constexpr u32 WS_EPOCH = 0;
constexpr u32 WS_B2FLAG = 256;
constexpr u32 WS_B3FLAG = WS_B2FLAG + 512;
constexpr u32 WS_QG = 4096;
constexpr u32 WS_QH = WS_QG + 65536;
constexpr u32 WS_QL = WS_QH + 65536;
constexpr u32 WS_BML = WS_QL + 65536;
constexpr u32 WS_OUTB = WS_BML + 16384;
constexpr u32 WS_OUTA = WS_OUTB + 589824;
constexpr u32 WS_APG = WS_OUTA + 589824;
constexpr u32 WS_BPART = WS_APG + 401408;
constexpr u32 WS_END = WS_BPART + 2097152;

constexpr int CSTR = 1040;
constexpr int L_C = 0;
constexpr int L_WUV = 0;
constexpr int L_QH = 65536;
constexpr int L_QL = 98816;
constexpr int L_OT = 65536;
constexpr int L_WO = 65536;
constexpr int L_CA = 139264;
constexpr int L_RED2 = 143360;
constexpr int L_XB = 133120;
constexpr int L_P = 149504;
constexpr int L_ST = 158208;
constexpr int L_SCR = 159744;
constexpr int L_MRG = 81920;
constexpr int LDS_BYTES = 163840;

struct Params {
  const u16* Ax; const u16* Ak; const u16* Av; const u16* Awq; const u16* Awo;
  const u16* Bx; const u16* Bc; const u16* Bwq; const u16* Bwuk; const u16* Bwuv; const u16* Bwo;
  u16* out; unsigned char* ws;
};

__device__ __forceinline__ float bf2f(u16 v) { return __uint_as_float(((u32)v) << 16); }
__device__ __forceinline__ float bflo(u32 v) { return __uint_as_float(v << 16); }
__device__ __forceinline__ float bfhi(u32 v) { return __uint_as_float(v & 0xffff0000u); }
__device__ __forceinline__ float bfr(float f) { return (float)(__bf16)f; }
__device__ __forceinline__ u32 pack_bf16(float lo, float hi) {
  bf16x2 v = {(__bf16)lo, (__bf16)hi};
  return __builtin_bit_cast(u32, v);
}
__device__ __forceinline__ float dot2(u32 a, u32 b, float c) {
  return __builtin_amdgcn_fdot2_f32_bf16(__builtin_bit_cast(bf16x2, a), __builtin_bit_cast(bf16x2, b), c, false);
}
template <int CTRL> __device__ __forceinline__ float dppf(float x) {
  return __builtin_bit_cast(float, __builtin_amdgcn_mov_dpp(__builtin_bit_cast(int, x), CTRL, 0xf, 0xf, true));
}
__device__ __forceinline__ float row16_sum(float x) {
  x += dppf<0x128>(x); x += dppf<0x124>(x); x += dppf<0x122>(x); x += dppf<0x121>(x);
  return x;
}
__device__ __forceinline__ float xsum16(float x) { auto r = __builtin_amdgcn_permlane16_swap(__float_as_uint(x), __float_as_uint(x), false, false); return __uint_as_float(r[0]) + __uint_as_float(r[1]); }
__device__ __forceinline__ float xsum32(float x) { auto r = __builtin_amdgcn_permlane32_swap(__float_as_uint(x), __float_as_uint(x), false, false); return __uint_as_float(r[0]) + __uint_as_float(r[1]); }
__device__ __forceinline__ float xmax16(float x) { auto r = __builtin_amdgcn_permlane16_swap(__float_as_uint(x), __float_as_uint(x), false, false); return fmaxf(__uint_as_float(r[0]), __uint_as_float(r[1])); }
__device__ __forceinline__ float xmax32(float x) { auto r = __builtin_amdgcn_permlane32_swap(__float_as_uint(x), __float_as_uint(x), false, false); return fmaxf(__uint_as_float(r[0]), __uint_as_float(r[1])); }
__device__ __forceinline__ float ex2(float x) { return __builtin_amdgcn_exp2f(x); }

typedef __amdgpu_buffer_rsrc_t rsrc_t;
__device__ __forceinline__ u32x4 ld128(rsrc_t r, u32 off) { return __builtin_amdgcn_raw_buffer_load_b128(r, off, 0, 16); }
__device__ __forceinline__ u32x2 ld64(rsrc_t r, u32 off) { return __builtin_amdgcn_raw_buffer_load_b64(r, off, 0, 16); }
__device__ __forceinline__ void st128(rsrc_t r, u32 off, u32x4 v) { __builtin_amdgcn_raw_buffer_store_b128(v, r, off, 0, 16); }
__device__ __forceinline__ void st64(rsrc_t r, u32 off, u32x2 v) { __builtin_amdgcn_raw_buffer_store_b64(v, r, off, 0, 16); }
__device__ __forceinline__ u32x4 ldnt(const u16* p) { return __builtin_nontemporal_load((const u32x4*)p); }

__device__ __forceinline__ void flag_set(unsigned char* ws, u32 off, u64 tag) {
  __hip_atomic_store((GAS u64*)(ws + off), tag, __ATOMIC_RELAXED, __HIP_MEMORY_SCOPE_AGENT);
}
__device__ __forceinline__ u64 flag_get(unsigned char* ws, u32 off) {
  return __hip_atomic_load((GAS u64*)(ws + off), __ATOMIC_RELAXED, __HIP_MEMORY_SCOPE_AGENT);
}
#define DRAIN() asm volatile("s_waitcnt vmcnt(0)" ::: "memory")
#define BAR() asm volatile("s_waitcnt lgkmcnt(0)\n\ts_barrier" ::: "memory")
#define LGKM0() asm volatile("s_waitcnt lgkmcnt(0)" ::: "memory")

__device__ __forceinline__ void glds16(const void* gsrc, u32 lds_dst) {
  u32 keep;
  asm volatile("s_mov_b32 %0, m0\n\ts_mov_b32 m0, %2\n\ts_nop 0\n\tglobal_load_lds_dwordx4 %1, off nt\n\ts_mov_b32 m0, %0"
               : "=&s"(keep) : "v"(gsrc), "s"(lds_dst) : "memory");
}

constexpr int CODE_BYTES = 49152;
__device__ __forceinline__ void code_prefetch(u64 pc0, int lane, u32 lds_dummy) {
#pragma unroll
  for (int i = 0; i < (CODE_BYTES + 8191) / 8192; ++i) {
    int off = i * 8192 + lane * 128; off = off < CODE_BYTES - 4 ? off : CODE_BYTES - 4;
    const unsigned char* g = (const unsigned char*)pc0 + off;
    u32 keep;
    asm volatile("s_mov_b32 %0, m0\n\ts_mov_b32 m0, %2\n\ts_nop 0\n\tglobal_load_lds_dword %1, off\n\ts_mov_b32 m0, %0"
                 : "=&s"(keep) : "v"(g), "s"(lds_dummy) : "memory");
  }
}
__device__ __forceinline__ void wait_flags(unsigned char* ws, u32 off, bool active, u64 tag) {
  for (int spin = 0; spin < SPIN_MAX; ++spin) {
    u64 v = tag;
    if (active) v = flag_get(ws, off);
    if (__all(v == tag)) break;
    __builtin_amdgcn_s_sleep(FSLEEP);
  }
  __builtin_amdgcn_fence(__ATOMIC_ACQUIRE, "wavefront");
}
__device__ __forceinline__ void gst(rsrc_t r, u32 off, u32 d0, u32 d1, u64 tag) { st128(r, off, u32x4{d0, d1, (u32)tag, (u32)(tag >> 32)}); }
__device__ __forceinline__ bool gok(u32x4 g, u64 tag) { return g[2] == (u32)tag && g[3] == (u32)(tag >> 32); }
template <int N> __device__ __forceinline__ void gpoll(rsrc_t r, const u32 (&off)[N], u32x4 (&g)[N], bool need, u64 tag) {
  for (int spin = 0; spin < SPIN_MAX; ++spin) {
    bool ok = true;
#pragma unroll
    for (int i = 0; i < N; ++i) { g[i] = ld128(r, off[i]); ok = ok && gok(g[i], tag); }
    if (__all(ok || !need)) break;
    __builtin_amdgcn_s_sleep(N > 1 ? GSLEEP : 1);
  }
}

#define PSLEEP 8
#define STR2(x) #x
#define STR(x) STR2(x)
#define PCHK1(A2, A3, LBL) \
    "v_mov_b32 %[t0], " A2 "\n\tv_mov_b32 %[t1], " A3 "\n\t" \
    "v_cmp_eq_u32 vcc, %[tlo], %[t0]\n\tv_cmp_eq_u32 %[sc], %[thi], %[t1]\n\t" \
    "s_and_b64 vcc, vcc, %[sc]\n\ts_cmp_eq_u64 vcc, exec\n\ts_cbranch_scc1 " LBL "\n\t"
__device__ __forceinline__ u32x2 gpoll1p(rsrc_t r, u32 off, u64 tag) {
  u32 d0, d1, t0, t1; u64 sc; int cnt = SPIN_MAX;
  const u32 tlo = __builtin_amdgcn_readfirstlane((u32)tag), thi = __builtin_amdgcn_readfirstlane((u32)(tag >> 32));
  asm volatile(
    "buffer_load_dwordx4 v[232:235], %[off], %[rs], 0 offen sc1\n\ts_sleep " STR(PSLEEP) "\n\t"
    "buffer_load_dwordx4 v[236:239], %[off], %[rs], 0 offen sc1\n\ts_sleep " STR(PSLEEP) "\n\t"
    "buffer_load_dwordx4 v[240:243], %[off], %[rs], 0 offen sc1\n\t"
    "1:\n\t"
    "s_waitcnt vmcnt(2)\n\t" PCHK1("v234", "v235", "2f")
    "buffer_load_dwordx4 v[232:235], %[off], %[rs], 0 offen sc1\n\t"
    "s_waitcnt vmcnt(2)\n\t" PCHK1("v238", "v239", "3f")
    "buffer_load_dwordx4 v[236:239], %[off], %[rs], 0 offen sc1\n\t"
    "s_waitcnt vmcnt(2)\n\t" PCHK1("v242", "v243", "4f")
    "buffer_load_dwordx4 v[240:243], %[off], %[rs], 0 offen sc1\n\t"
    "s_sub_u32 %[cnt], %[cnt], 1\n\ts_cmp_lg_u32 %[cnt], 0\n\ts_cbranch_scc1 1b\n\t"
    "s_waitcnt vmcnt(0)\n\t"
    "2:\n\tv_mov_b32 %[d0], v232\n\tv_mov_b32 %[d1], v233\n\ts_branch 5f\n\t"
    "3:\n\tv_mov_b32 %[d0], v236\n\tv_mov_b32 %[d1], v237\n\ts_branch 5f\n\t"
    "4:\n\tv_mov_b32 %[d0], v240\n\tv_mov_b32 %[d1], v241\n\t"
    "5:\n\t"
    : [d0] "=&v"(d0), [d1] "=&v"(d1), [t0] "=&v"(t0), [t1] "=&v"(t1), [sc] "=&s"(sc), [cnt] "+s"(cnt)
    : [off] "v"(off), [rs] "s"(r), [tlo] "s"(tlo), [thi] "s"(thi)
    : "vcc", "scc", "memory", "v232", "v233", "v234", "v235", "v236", "v237", "v238", "v239", "v240", "v241", "v242", "v243");
  return u32x2{d0, d1};
}
#define PCHK2(A2, A3, B2, B3, LBL) \
    "v_mov_b32 %[t0], " A2 "\n\tv_mov_b32 %[t1], " A3 "\n\t" \
    "v_cmp_eq_u32 vcc, %[tlo], %[t0]\n\tv_cmp_eq_u32 %[sc], %[thi], %[t1]\n\t" \
    "v_mov_b32 %[t0], " B2 "\n\tv_mov_b32 %[t1], " B3 "\n\t" \
    "s_and_b64 vcc, vcc, %[sc]\n\t" \
    "v_cmp_eq_u32 %[sc], %[tlo], %[t0]\n\tv_cmp_eq_u32 %[sd], %[thi], %[t1]\n\t" \
    "s_and_b64 vcc, vcc, %[sc]\n\ts_and_b64 vcc, vcc, %[sd]\n\t" \
    "s_cmp_eq_u64 vcc, exec\n\ts_cbranch_scc1 " LBL "\n\t"
#define PLD2(A, B) "buffer_load_dwordx4 " A ", %[off0], %[rs], 0 offen sc1\n\tbuffer_load_dwordx4 " B ", %[off1], %[rs], 0 offen sc1\n\t"
#define PRD2(A0, A1, B0, B1) "v_mov_b32 %[d0], " A0 "\n\tv_mov_b32 %[d1], " A1 "\n\tv_mov_b32 %[d2], " B0 "\n\tv_mov_b32 %[d3], " B1 "\n\t"
__device__ __forceinline__ u32x4 gpoll2p(rsrc_t r, u32 off0, u32 off1, u64 tag) {
  u32 d0, d1, d2, d3, t0, t1; u64 sc, sd; int cnt = SPIN_MAX;
  const u32 tlo = __builtin_amdgcn_readfirstlane((u32)tag), thi = __builtin_amdgcn_readfirstlane((u32)(tag >> 32));
  asm volatile(
    PLD2("v[232:235]", "v[236:239]") "s_sleep " STR(PSLEEP) "\n\t"
    PLD2("v[240:243]", "v[244:247]") "s_sleep " STR(PSLEEP) "\n\t"
    PLD2("v[248:251]", "v[252:255]")
    "1:\n\t"
    "s_waitcnt vmcnt(4)\n\t" PCHK2("v234", "v235", "v238", "v239", "2f")
    PLD2("v[232:235]", "v[236:239]")
    "s_waitcnt vmcnt(4)\n\t" PCHK2("v242", "v243", "v246", "v247", "3f")
    PLD2("v[240:243]", "v[244:247]")
    "s_waitcnt vmcnt(4)\n\t" PCHK2("v250", "v251", "v254", "v255", "4f")
    PLD2("v[248:251]", "v[252:255]")
    "s_sub_u32 %[cnt], %[cnt], 1\n\ts_cmp_lg_u32 %[cnt], 0\n\ts_cbranch_scc1 1b\n\t"
    "s_waitcnt vmcnt(0)\n\t"
    "2:\n\t" PRD2("v232", "v233", "v236", "v237") "s_branch 5f\n\t"
    "3:\n\t" PRD2("v240", "v241", "v244", "v245") "s_branch 5f\n\t"
    "4:\n\t" PRD2("v248", "v249", "v252", "v253")
    "5:\n\t"
    : [d0] "=&v"(d0), [d1] "=&v"(d1), [d2] "=&v"(d2), [d3] "=&v"(d3), [t0] "=&v"(t0), [t1] "=&v"(t1), [sc] "=&s"(sc), [sd] "=&s"(sd), [cnt] "+s"(cnt)
    : [off0] "v"(off0), [off1] "v"(off1), [rs] "s"(r), [tlo] "s"(tlo), [thi] "s"(thi)
    : "vcc", "scc", "memory", "v232", "v233", "v234", "v235", "v236", "v237", "v238", "v239", "v240", "v241", "v242", "v243",
      "v244", "v245", "v246", "v247", "v248", "v249", "v250", "v251", "v252", "v253", "v254", "v255");
  return u32x4{d0, d1, d2, d3};
}
__device__ __forceinline__ void wait_flagsp(rsrc_t r, u32 off, u64 tag) {
  u32 t0, t1; u64 sc; int cnt = SPIN_MAX;
  const u32 tlo = __builtin_amdgcn_readfirstlane((u32)tag), thi = __builtin_amdgcn_readfirstlane((u32)(tag >> 32));
  asm volatile(
    "buffer_load_dwordx2 v[232:233], %[off], %[rs], 0 offen sc1\n\ts_sleep " STR(PSLEEP) "\n\t"
    "buffer_load_dwordx2 v[234:235], %[off], %[rs], 0 offen sc1\n\ts_sleep " STR(PSLEEP) "\n\t"
    "buffer_load_dwordx2 v[236:237], %[off], %[rs], 0 offen sc1\n\t"
    "1:\n\t"
    "s_waitcnt vmcnt(2)\n\t" PCHK1("v232", "v233", "2f")
    "buffer_load_dwordx2 v[232:233], %[off], %[rs], 0 offen sc1\n\t"
    "s_waitcnt vmcnt(2)\n\t" PCHK1("v234", "v235", "2f")
    "buffer_load_dwordx2 v[234:235], %[off], %[rs], 0 offen sc1\n\t"
    "s_waitcnt vmcnt(2)\n\t" PCHK1("v236", "v237", "2f")
    "buffer_load_dwordx2 v[236:237], %[off], %[rs], 0 offen sc1\n\t"
    "s_sub_u32 %[cnt], %[cnt], 1\n\ts_cmp_lg_u32 %[cnt], 0\n\ts_cbranch_scc1 1b\n\t"
    "2:\n\t"
    : [t0] "=&v"(t0), [t1] "=&v"(t1), [sc] "=&s"(sc), [cnt] "+s"(cnt)
    : [off] "v"(off), [rs] "s"(r), [tlo] "s"(tlo), [thi] "s"(thi)
    : "vcc", "scc", "memory", "v232", "v233", "v234", "v235", "v236", "v237");
}

#define STAMP(i) do {} while (0)
#define STAMP_FLUSH() do {} while (0)

__device__ __forceinline__ bf16x8 tr_pair(const unsigned char* a0, const unsigned char* a1) {
  const s16x4 t0 = __builtin_amdgcn_ds_read_tr16_b64_v4i16((LAS s16x4*)a0);
  const s16x4 t1 = __builtin_amdgcn_ds_read_tr16_b64_v4i16((LAS s16x4*)a1);
  return __builtin_bit_cast(bf16x8, __builtin_shufflevector(t0, t1, 0, 1, 2, 3, 4, 5, 6, 7));
}

__device__ __forceinline__ void proj_load(const unsigned char* lds, int wbase, int wave, int lane, bf16x8 (&wf)[5][2]) {
  const int g4 = lane >> 4, i16 = lane & 15, qq = i16 >> 2, pp = i16 & 3;
  const int r0 = 4 * g4 + qq;
  const int sw = (r0 >> 1) & 3;
#pragma unroll
  for (int i = 0; i < 5; ++i) {
    const int nb = wave + 8 * i < 36 ? wave + 8 * i : 35;
    const unsigned char* base = lds + wbase + r0 * 1152 + ((nb ^ sw) * 32) + pp * 8;
#pragma unroll
    for (int ks = 0; ks < 2; ++ks) wf[i][ks] = tr_pair(base + (32 * ks) * 1152, base + (32 * ks + 16) * 1152);
  }
}
__device__ __forceinline__ void proj_run(const unsigned char* lds, const bf16x8 (&wf)[5][2], rsrc_t wsr, u32 out_off, u64 tag, int wave, int lane, int ovb, bool st) {
  const int g4 = lane >> 4, i16 = lane & 15;
  bf16x8 af[2];
#pragma unroll
  for (int ks = 0; ks < 2; ++ks) {
    u32x2 lo = {0u, 0u}, hi = {0u, 0u};
    if (i16 < 2) {
      lo = *(const u32x2*)(lds + ovb + i16 * 128 + (32 * ks + 4 * g4) * 2);
      hi = *(const u32x2*)(lds + ovb + i16 * 128 + (32 * ks + 16 + 4 * g4) * 2);
    }
    af[ks] = __builtin_bit_cast(bf16x8, u32x4{lo[0], lo[1], hi[0], hi[1]});
  }
  f32x4 d[5];
#pragma unroll
  for (int i = 0; i < 5; ++i) {
    d[i] = f32x4{0.f, 0.f, 0.f, 0.f};
#pragma unroll
    for (int ks = 0; ks < 2; ++ks) d[i] = __builtin_amdgcn_mfma_f32_16x16x32_bf16(af[ks], wf[i][ks], d[i], 0, 0, 0);
  }
  if (lane < 16 && st) {
#pragma unroll
    for (int i = 0; i < 5; ++i) {
      const int nb = wave + 8 * i;
      if (nb < 36) gst(wsr, out_off + (16 * nb + lane) * 16, __float_as_uint(d[i][0]), __float_as_uint(d[i][1]), tag);
    }
  }
}
__device__ __forceinline__ void final_reduce(unsigned char* lds, rsrc_t wsr, u32 part_off, u64 tag, int f, int t, u16* out, int base, bool real = true) {
  const int wave = t >> 6, lane = t & 63;
  const int pidx = t >> 3, sub = t & 7;
  u32 off[2] = {part_off + (u32)(pidx * 576 + 16 * f + sub) * 16, part_off + (u32)(pidx * 576 + 16 * f + 8 + sub) * 16};
  u32x4 g[2] = {u32x4{0u, 0u, 0u, 0u}, u32x4{0u, 0u, 0u, 0u}};
  if (!real) {} else if (PMASK & 1) { const u32x4 r_ = gpoll2p(wsr, off[0], off[1], tag); g[0][0] = r_[0]; g[0][1] = r_[1]; g[1][0] = r_[2]; g[1][1] = r_[3]; } else gpoll<2>(wsr, off, g, true, tag);
  float v[4] = {__uint_as_float(g[0][0]), __uint_as_float(g[0][1]), __uint_as_float(g[1][0]), __uint_as_float(g[1][1])};
#pragma unroll
  for (int k = 0; k < 4; ++k) { float x = v[k]; x += dppf<0x128>(x); x = xsum16(x); x = xsum32(x); v[k] = x; }
  float* red = (float*)(lds + L_SCR);
  if (lane < 8) *(f32x4*)(red + wave * 32 + lane * 4) = f32x4{v[0], v[1], v[2], v[3]};
  BAR();
  if (t < 32) {
    float s = 0.f;
#pragma unroll
    for (int w = 0; w < 8; ++w) s += red[w * 32 + t];
    const int sb = t >> 2, k = t & 3, bb = k & 1, col = sb + 8 * (k >> 1);
    __bf16 o = (__bf16)s;
    if (real) out[bb * 1152 + base + 16 * f + col] = __builtin_bit_cast(u16, o);
  }
  if (real) DRAIN();
}

template <int NL, int RS>
__device__ __forceinline__ void q_load(const u16* qW, const u16* qx, int qcol0, int t, u32x4 (&qw)[NL], u16 (&qx0)[NL], u16 (&qx1)[NL]) {
  const int cch = t & 3, row0 = t >> 2;
#pragma unroll
  for (int i = 0; i < NL; ++i) {
    const int row = row0 + RS * i, rc = row < 576 ? row : 575;
    qx0[i] = qx[rc]; qx1[i] = qx[576 + rc];
  }
#pragma unroll
  for (int i = 0; i < NL; ++i) {
    const int row = row0 + RS * i, rc = row < 576 ? row : 575;
    qw[i] = ldnt(qW + (size_t)rc * 4096 + qcol0 + cch * 8);
  }
}
template <int NL, int RS>
__device__ __forceinline__ void q_fma(int t, const u32x4 (&qw)[NL], const u16 (&qx0)[NL], const u16 (&qx1)[NL], float (&qa0)[8], float (&qa1)[8]) {
#pragma unroll
  for (int k = 0; k < 8; ++k) { qa0[k] = 0.f; qa1[k] = 0.f; }
#pragma unroll
  for (int i = 0; i < NL; ++i) {
    const bool live = (t >> 2) + RS * i < 576;
    const float x0 = live ? bf2f(qx0[i]) : 0.f, x1 = live ? bf2f(qx1[i]) : 0.f;
#pragma unroll
    for (int k = 0; k < 4; ++k) {
      const float lo = bflo(qw[i][k]), hi = bfhi(qw[i][k]);
      qa0[2 * k] = fmaf(x0, lo, qa0[2 * k]); qa0[2 * k + 1] = fmaf(x0, hi, qa0[2 * k + 1]);
      qa1[2 * k] = fmaf(x1, lo, qa1[2 * k]); qa1[2 * k + 1] = fmaf(x1, hi, qa1[2 * k + 1]);
    }
  }
}
__device__ __forceinline__ void q_reduce(unsigned char* lds, int wave, int lane, float (&qa0)[8], float (&qa1)[8]) {
#pragma unroll
  for (int k = 0; k < 8; ++k) {
    float x = qa0[k]; x += dppf<0x124>(x); x += dppf<0x128>(x); x = xsum16(x); x = xsum32(x); qa0[k] = x;
    float y = qa1[k]; y += dppf<0x124>(y); y += dppf<0x128>(y); y = xsum16(y); y = xsum32(y); qa1[k] = y;
  }
  float* red_ = (float*)(lds + L_SCR);
  if (lane < 4) {
    float* r0 = red_ + wave * 64 + lane * 8;
    *(f32x4*)r0 = f32x4{qa0[0], qa0[1], qa0[2], qa0[3]}; *(f32x4*)(r0 + 4) = f32x4{qa0[4], qa0[5], qa0[6], qa0[7]};
    *(f32x4*)(r0 + 32) = f32x4{qa1[0], qa1[1], qa1[2], qa1[3]}; *(f32x4*)(r0 + 36) = f32x4{qa1[4], qa1[5], qa1[6], qa1[7]};
  }
}
template <int NW>
__device__ __forceinline__ void q_publish(unsigned char* lds, rsrc_t wsr, int qmat, int qcol0, int t, u64 tag) {
  if (t < 16) {
    const float* red_ = (const float*)(lds + L_SCR);
    f32x4 s_ = *(const f32x4*)(red_ + t * 4);
#pragma unroll
    for (int w = 1; w < NW; ++w) s_ += *(const f32x4*)(red_ + w * 64 + t * 4);
    gst(wsr, WS_QG + ((qmat * 2 + (t >> 3)) * 1024 + (qcol0 >> 2) + (t & 7)) * 16, pack_bf16(s_[0], s_[1]), pack_bf16(s_[2], s_[3]), tag);
  }
}

#define LA3 1
#define LA2 1
#define ADEPTH 1
#define PREMODE 1
#define NPRE3 0
#define EARLY3 8
#define EARLY2 8
#define D1 0
#define D2 0
#define DELAY(n) do { _Pragma("unroll") for (int d_ = 0; d_ < (n); ++d_) __builtin_amdgcn_s_sleep(8); } while (0)
constexpr int U2 = (256 - 4 * U3) / 2;

#define A_LOAD(KK, VV, it) do { _Pragma("unroll") for (int i_ = 0; i_ < 4; ++i_) { KK[i_] = ldnt(Kb + (size_t)(it) * 16384 + i_ * 512); } \
                                _Pragma("unroll") for (int i_ = 0; i_ < 4; ++i_) { VV[i_] = ldnt(Vb + (size_t)(it) * 16384 + i_ * 512); } } while (0)
#define A_COMPUTE(KK, VV) do { \
      float s0_[4], s1_[4]; \
      _Pragma("unroll") for (int i_ = 0; i_ < 4; ++i_) { \
        float d0_ = 0.f, d1_ = 0.f; \
        _Pragma("unroll") for (int k_ = 0; k_ < 4; ++k_) { d0_ = dot2(KK[i_][k_], q0[k_], d0_); d1_ = dot2(KK[i_][k_], q1[k_], d1_); } \
        d0_ = row16_sum(d0_); d1_ = row16_sum(d1_); \
        s0_[i_] = bfr(bfr(d0_) * SCALE); s1_[i_] = bfr(bfr(d1_) * SCALE); } \
      const float mx0_ = fmaxf(fmaxf(s0_[0], s0_[1]), fmaxf(s0_[2], s0_[3])), mx1_ = fmaxf(fmaxf(s1_[0], s1_[1]), fmaxf(s1_[2], s1_[3])); \
      if (__any(mx0_ > m0 || mx1_ > m1)) { \
        const float n0_ = fmaxf(m0, mx0_), n1_ = fmaxf(m1, mx1_); \
        const float al0_ = ex2((m0 - n0_) * L2E), al1_ = ex2((m1 - n1_) * L2E); \
        l0 *= al0_; l1 *= al1_; m0 = n0_; m1 = n1_; \
        _Pragma("unroll") for (int k_ = 0; k_ < 8; ++k_) { acc0[k_] *= al0_; acc1[k_] *= al1_; } } \
      float p0_[4], p1_[4]; \
      _Pragma("unroll") for (int i_ = 0; i_ < 4; ++i_) { p0_[i_] = ex2((s0_[i_] - m0) * L2E); p1_[i_] = ex2((s1_[i_] - m1) * L2E); } \
      l0 += (p0_[0] + p0_[1]) + (p0_[2] + p0_[3]); l1 += (p1_[0] + p1_[1]) + (p1_[2] + p1_[3]); \
      const u32 pa0_ = pack_bf16(p0_[0], p0_[1]), pb0_ = pack_bf16(p0_[2], p0_[3]), pa1_ = pack_bf16(p1_[0], p1_[1]), pb1_ = pack_bf16(p1_[2], p1_[3]); \
      _Pragma("unroll") for (int k_ = 0; k_ < 4; ++k_) { \
        const u32 lo01_ = __builtin_amdgcn_perm(VV[1][k_], VV[0][k_], 0x05040100u), hi01_ = __builtin_amdgcn_perm(VV[1][k_], VV[0][k_], 0x07060302u); \
        const u32 lo23_ = __builtin_amdgcn_perm(VV[3][k_], VV[2][k_], 0x05040100u), hi23_ = __builtin_amdgcn_perm(VV[3][k_], VV[2][k_], 0x07060302u); \
        acc0[2 * k_] = dot2(lo23_, pb0_, dot2(lo01_, pa0_, acc0[2 * k_])); acc0[2 * k_ + 1] = dot2(hi23_, pb0_, dot2(hi01_, pa0_, acc0[2 * k_ + 1])); \
        acc1[2 * k_] = dot2(lo23_, pb1_, dot2(lo01_, pa1_, acc1[2 * k_])); acc1[2 * k_ + 1] = dot2(hi23_, pb1_, dot2(hi01_, pa1_, acc1[2 * k_ + 1])); } \
    } while (0)

#define A_LOADK(KK, it) do { _Pragma("unroll") for (int i_ = 0; i_ < 4; ++i_) { KK[i_] = ldnt(Kb + (size_t)(it) * 16384 + i_ * 512); } } while (0)
#define A_LOADV(VV, it) do { _Pragma("unroll") for (int i_ = 0; i_ < 4; ++i_) { VV[i_] = ldnt(Vb + (size_t)(it) * 16384 + i_ * 512); } } while (0)
#define A_SCORE(KK) do { \
      float s0_[4], s1_[4]; \
      _Pragma("unroll") for (int i_ = 0; i_ < 4; ++i_) { \
        float d0_ = 0.f, d1_ = 0.f; \
        _Pragma("unroll") for (int k_ = 0; k_ < 4; ++k_) { d0_ = dot2(KK[i_][k_], q0[k_], d0_); d1_ = dot2(KK[i_][k_], q1[k_], d1_); } \
        d0_ = row16_sum(d0_); d1_ = row16_sum(d1_); \
        s0_[i_] = bfr(bfr(d0_) * SCALE); s1_[i_] = bfr(bfr(d1_) * SCALE); } \
      const float mx0_ = fmaxf(fmaxf(s0_[0], s0_[1]), fmaxf(s0_[2], s0_[3])), mx1_ = fmaxf(fmaxf(s1_[0], s1_[1]), fmaxf(s1_[2], s1_[3])); \
      if (__any(mx0_ > m0 || mx1_ > m1)) { \
        const float n0_ = fmaxf(m0, mx0_), n1_ = fmaxf(m1, mx1_); \
        const float al0_ = ex2((m0 - n0_) * L2E), al1_ = ex2((m1 - n1_) * L2E); \
        l0 *= al0_; l1 *= al1_; m0 = n0_; m1 = n1_; \
        _Pragma("unroll") for (int k_ = 0; k_ < 8; ++k_) { acc0[k_] *= al0_; acc1[k_] *= al1_; } } \
      float p0_[4], p1_[4]; \
      _Pragma("unroll") for (int i_ = 0; i_ < 4; ++i_) { p0_[i_] = ex2((s0_[i_] - m0) * L2E); p1_[i_] = ex2((s1_[i_] - m1) * L2E); } \
      l0 += (p0_[0] + p0_[1]) + (p0_[2] + p0_[3]); l1 += (p1_[0] + p1_[1]) + (p1_[2] + p1_[3]); \
      pa0 = pack_bf16(p0_[0], p0_[1]); pb0 = pack_bf16(p0_[2], p0_[3]); pa1 = pack_bf16(p1_[0], p1_[1]); pb1 = pack_bf16(p1_[2], p1_[3]); \
    } while (0)
#define A_PV(VV) do { \
      _Pragma("unroll") for (int k_ = 0; k_ < 4; ++k_) { \
        const u32 lo01_ = __builtin_amdgcn_perm(VV[1][k_], VV[0][k_], 0x05040100u), hi01_ = __builtin_amdgcn_perm(VV[1][k_], VV[0][k_], 0x07060302u); \
        const u32 lo23_ = __builtin_amdgcn_perm(VV[3][k_], VV[2][k_], 0x05040100u), hi23_ = __builtin_amdgcn_perm(VV[3][k_], VV[2][k_], 0x07060302u); \
        acc0[2 * k_] = dot2(lo23_, pb0, dot2(lo01_, pa0, acc0[2 * k_])); acc0[2 * k_ + 1] = dot2(hi23_, pb0, dot2(hi01_, pa0, acc0[2 * k_ + 1])); \
        acc1[2 * k_] = dot2(lo23_, pb1, dot2(lo01_, pa1, acc1[2 * k_])); acc1[2 * k_ + 1] = dot2(hi23_, pb1, dot2(hi01_, pa1, acc1[2 * k_ + 1])); } \
    } while (0)
#define A_HSTEP(u) do { \
    if ((u) < NU || ((u) == NU && extra)) { \
      DRAIN(); A_LOADV(V0, (u)); __builtin_amdgcn_sched_barrier(0); A_SCORE(K0); __builtin_amdgcn_sched_barrier(0); \
      DRAIN(); if ((u) + 1 < NU || ((u) + 1 == NU && extra)) { A_LOADK(K0, (u) + 1); } __builtin_amdgcn_sched_barrier(0); A_PV(V0); __builtin_amdgcn_sched_barrier(0); } \
    } while (0)
#define WAITD() asm volatile("s_waitcnt vmcnt(0)" ::: "memory")
#define A_STEP(KK, VV, KN, VN, u) do { \
    if ((u) + LA < NU) { DRAIN(); A_LOAD(KN, VN, (u) + LA); } else if ((u) + LA == NU) { if (extra) { DRAIN(); A_LOAD(KN, VN, (u) + LA); } } \
    __builtin_amdgcn_sched_barrier(0); \
    if ((u) < NU) { A_COMPUTE(KK, VV); } else if ((u) == NU) { if (extra) A_COMPUTE(KK, VV); } \
    __builtin_amdgcn_sched_barrier(0); } while (0)
template <int NU, int LA>
__device__ __forceinline__ void a_stream(bool pre, bool extra, bool poll, const u16* Kb, const u16* Vb, unsigned char* lds, rsrc_t wsr, u32 qoff, u64 tag, int lane,
                                         float& m0, float& m1, float& l0, float& l1, float (&acc0)[8], float (&acc1)[8],
                                         u32x4 (&K0)[4], u32x4 (&V0)[4], u32x4 (&K1)[4], u32x4 (&V1)[4]) {
  u32x4 K2[4], V2[4];
  if (LA == 2) { A_LOAD(K0, V0, 0); __builtin_amdgcn_sched_barrier(0); }
  if (poll) {
    u32 off[1] = {qoff};
    u32x4 g[1];
    if (PMASK & 2) { const u32x2 r_ = gpoll1p(wsr, off[0], tag); g[0][0] = r_[0]; g[0][1] = r_[1]; } else gpoll<1>(wsr, off, g, true, tag);
    *(u32x2*)(lds + L_SCR + 2048 + lane * 8) = u32x2{g[0][0], g[0][1]};
  }
  if (LA == 2) { DRAIN(); A_LOAD(K1, V1, 1); } else if (LA == 0) { A_LOADK(K0, 0); } else {
    if (!pre) { A_LOAD(K0, V0, 0); } }
  BAR();
  const u32x4 q0 = *(const u32x4*)(lds + L_SCR + 2048 + (lane & 15) * 16), q1 = *(const u32x4*)(lds + L_SCR + 2048 + 256 + (lane & 15) * 16);
  if (LA == 0) {
    u32 pa0, pb0, pa1, pb1;
    A_HSTEP(0); A_HSTEP(1); A_HSTEP(2); A_HSTEP(3); A_HSTEP(4); A_HSTEP(5); A_HSTEP(6); A_HSTEP(7);
  } else if (LA == 1) {
    A_STEP(K0, V0, K1, V1, 0); A_STEP(K1, V1, K0, V0, 1); A_STEP(K0, V0, K1, V1, 2); A_STEP(K1, V1, K0, V0, 3);
    A_STEP(K0, V0, K1, V1, 4); A_STEP(K1, V1, K0, V0, 5); A_STEP(K0, V0, K1, V1, 6); A_STEP(K1, V1, K0, V0, 7);
  } else {
    A_STEP(K0, V0, K2, V2, 0); A_STEP(K1, V1, K0, V0, 1); A_STEP(K2, V2, K1, V1, 2); A_STEP(K0, V0, K2, V2, 3);
    A_STEP(K1, V1, K0, V0, 4); A_STEP(K2, V2, K1, V1, 5); A_STEP(K0, V0, K2, V2, 6); A_STEP(K1, V1, K0, V0, 7);
  }
}

__device__ __forceinline__ u32 touch_pages(const Params& p, int lane, const u16* Kb, const u16* Vb, u32 lds_dummy) {
  const unsigned char* a = (const unsigned char*)p.Ax;
  const int g = lane >> 2, k = lane & 3;
  const u32 big = k == 3 ? 0u : (u32)k << 21;
  if (g == 1) a = (const unsigned char*)p.Awq + big;
  if (g == 2) a = (const unsigned char*)p.Awo + big;
  if (g == 3) a = (const unsigned char*)p.Bwuv + (k == 2 ? 4194300u : big);
  if (g == 4) a = (const unsigned char*)p.Bwo + big;
  if (g == 5) a = p.ws + (k == 2 ? WS_END - 4 : big);
  if (g == 6) a = (const unsigned char*)p.out;
  if (g == 7) a = (const unsigned char*)(k & 1 ? Vb : Kb) + (k & 2 ? (u32)U2 * 2048u : 0u);
  u32 v = 0;
  if (lane < 32) {
    u32 keep;
    asm volatile("s_mov_b32 %0, m0\n\ts_mov_b32 m0, %2\n\ts_nop 0\n\tglobal_load_lds_dword %1, off\n\ts_mov_b32 m0, %0"
                 : "=&s"(keep) : "v"(a), "s"(lds_dummy) : "memory");
  }
  return v;
}

__global__ void __launch_bounds__(NT, 2) fused_kernel(Params p) {
  __shared__ __attribute__((aligned(16))) unsigned char lds[LDS_BYTES];
  u64 pc0; asm volatile("s_getpc_b64 %0" : "=s"(pc0)); pc0 &= ~255ull;
  const int bid = blockIdx.x, t = threadIdx.x, lane = t & 63;
  const int wave = __builtin_amdgcn_readfirstlane(t >> 6);
  unsigned char* ws = p.ws;
  const rsrc_t wsr = __builtin_amdgcn_make_buffer_rsrc(ws, 0, WS_END, 0x00020000);
  const u32 ldsb = (u32)(uintptr_t)(LAS unsigned char*)lds;
  STAMP(0);
  u32 vz; asm volatile("v_mov_b32 %0, 0" : "=v"(vz));
  const u64 epoch = flag_get(ws + vz, WS_EPOCH);
  const int xcd = bid & 7, slot = bid >> 3;
  const bool isB = slot < 8;

  if (!isB) {
    const bool isR2 = slot < 16;
    const int r2 = (slot - 8) * 8 + xcd, r3 = (slot - 16) * 8 + xcd;
    const int pair = isR2 ? (r2 >> 1) : (r3 >> 2), sub = isR2 ? 4 + (r2 & 1) : (r3 & 3);
    const int ai = pair * 6 + sub;
    const int b = pair >> 4, hkv = pair & 15;
    const int ustart = (isR2 ? 4 * U3 + (sub - 4) * U2 : sub * U3) + (7 - wave);
    const u16* Kb = p.Ak + (size_t)pair * 4096 * 128 + (size_t)ustart * 2048 + (lane >> 4) * 128 + (lane & 15) * 8;
    const u16* Vb = p.Av + (size_t)pair * 4096 * 128 + (size_t)ustart * 2048 + (lane >> 4) * 128 + (lane & 15) * 8;
    const u32 qoff = WS_QG + (u32)((0 * 2 + b) * 1024 + (2 * hkv) * 32 + lane) * 16;
    float m0 = -INFINITY, m1 = -INFINITY, l0 = 0.f, l1 = 0.f;
    float acc0[8], acc1[8];
#pragma unroll
    for (int k = 0; k < 8; ++k) { acc0[k] = 0.f; acc1[k] = 0.f; }
    u64 tag;
    u32 tch = 0;
    u32x4 K0[4], V0[4], K1[4], V1[4];
    if (!isR2) {
      const int qc = (((slot - 16) >> 1) << 4) | (xcd << 1) | ((slot - 16) & 1);
      const int qcol0 = qc * 32;
      u32x4 qwB[5], qwA[5]; u16 bx0[5], bx1[5], ax0[5], ax1[5];
      q_load<5, 128>(p.Bwq, p.Bx, qcol0, t, qwB, bx0, bx1);
      if (wave == 7) tch = touch_pages(p, lane, Kb, Vb, ldsb + L_ST + 1024);
      __builtin_amdgcn_sched_barrier(0);
      float qa0[8], qa1[8];
      q_fma<5, 128>(t, qwB, bx0, bx1, qa0, qa1);
      __builtin_amdgcn_sched_barrier(0);
      q_load<5, 128>(p.Awq, p.Ax, qcol0, t, qwA, ax0, ax1);
      if (EARLY3 == 0 || (EARLY3 < 8 && wave >= EARLY3)) A_LOAD(K0, V0, 0);
      __builtin_amdgcn_sched_barrier(0);
      q_reduce(lds, wave, lane, qa0, qa1);
      tag = epoch + 1ull;
      BAR();
      q_publish<8>(lds, wsr, 1, qcol0, t, tag);
      STAMP(1);
      q_fma<5, 128>(t, qwA, ax0, ax1, qa0, qa1);
      q_reduce(lds - 4096, wave, lane, qa0, qa1);
      BAR();
      q_publish<8>(lds - 4096, wsr, 0, qcol0, t, tag);
      STAMP(2);
      constexpr int NLO = U3 / 8, WSPLIT = 8 - (U3 - NLO * 8);
      a_stream<NLO, LA3>(EARLY3 == 0 || (EARLY3 < 8 && wave >= EARLY3), wave >= WSPLIT, wave == 0, Kb, Vb, lds, wsr, qoff, tag, lane, m0, m1, l0, l1, acc0, acc1, K0, V0, K1, V1);
    } else {
      const int hh = r2 >> 1, half = r2 & 1;
      u32x4 wk[8];
      DELAY(D2);
#pragma unroll
      for (int i = 0; i < 8; ++i) wk[i] = ldnt(p.Bwuk + ((size_t)(256 * half + (t >> 4) + 32 * i) * 32 + hh) * 128 + (t & 15) * 8);
      if (wave == 7) tch = touch_pages(p, lane, Kb, Vb, ldsb + L_ST + 1024);
      {
        const unsigned char* src = (const unsigned char*)(p.Awo + (size_t)r2 * 64 * 576);
        for (int i = 0; i < EARLYWO; ++i) {
          const int L = i * 512 + t, r = L / 72, pc = L - r * 72;
          const int c = (((pc >> 1) ^ ((r >> 1) & 3)) << 1) | (pc & 1);
          glds16(src + r * 1152 + c * 16, ldsb + (u32)(i * 512 + wave * 64) * 16);
        }
      }
      if (EARLY2 == 0 || (EARLY2 < 8 && wave >= EARLY2)) A_LOAD(K0, V0, 0);
      __builtin_amdgcn_sched_barrier(0);
      tag = epoch + 1ull;
      if (wave == 0) {
        u32 off[1] = {WS_QG + (u32)((1 * 2 + (lane >> 5)) * 1024 + hh * 32 + (lane & 31)) * 16};
        u32x4 g[1];
        if (PMASK & 4) { const u32x2 r_ = gpoll1p(wsr, off[0], tag); g[0][0] = r_[0]; g[0][1] = r_[1]; } else gpoll<1>(wsr, off, g, true, tag);
        *(u32x2*)(lds + L_ST + lane * 8) = u32x2{g[0][0], g[0][1]};
      }
      BAR();
      STAMP(1);
      const u32x4 qa = *(const u32x4*)(lds + L_ST + (t & 15) * 16), qb = *(const u32x4*)(lds + L_ST + 256 + (t & 15) * 16);
      float* qs = (float*)(lds + L_SCR);
#pragma unroll
      for (int i = 0; i < 8; ++i) {
        float d0 = 0.f, d1 = 0.f;
#pragma unroll
        for (int k = 0; k < 4; ++k) { d0 = dot2(wk[i][k], qa[k], d0); d1 = dot2(wk[i][k], qb[k], d1); }
        d0 = row16_sum(d0); d1 = row16_sum(d1);
        if ((t & 15) == 0) { qs[(t >> 4) + 32 * i] = d0; qs[256 + (t >> 4) + 32 * i] = d1; }
      }
      BAR();
      if (wave == 0) {
        const int bb = lane >> 5, c8 = lane & 31;
        u32x4 hi, lo;
#pragma unroll
        for (int k = 0; k < 4; ++k) {
          const float v0 = qs[bb * 256 + c8 * 8 + 2 * k], v1 = qs[bb * 256 + c8 * 8 + 2 * k + 1];
          const float h0 = bfr(v0), h1 = bfr(v1);
          hi[k] = pack_bf16(h0, h1); lo[k] = pack_bf16(v0 - h0, v1 - h1);
        }
        const u32 o = ((bb * 32 + hh) * 512 + 256 * half + c8 * 8) * 2;
        st128(wsr, WS_QH + o, hi); st128(wsr, WS_QL + o, lo);
        DRAIN();
        if (lane == 0) flag_set(ws, WS_B2FLAG + r2 * 8, tag);
      }
      BAR();
      STAMP(2);
      {
        const unsigned char* src = (const unsigned char*)(p.Awo + (size_t)r2 * 64 * 576);
        for (int i = EARLYWO; i < 9; ++i) {
          const int L = i * 512 + t, r = L / 72, pc = L - r * 72;
          const int c = (((pc >> 1) ^ ((r >> 1) & 3)) << 1) | (pc & 1);
          glds16(src + r * 1152 + c * 16, ldsb + (u32)(i * 512 + wave * 64) * 16);
        }
      }
      constexpr int NLO = U2 / 8, WSPLIT = 8 - (U2 - NLO * 8);
      a_stream<NLO, LA2>(EARLY2 == 0 || (EARLY2 < 8 && wave >= EARLY2), wave >= WSPLIT, wave == 1, Kb, Vb, lds, wsr, qoff, tag, lane, m0, m1, l0, l1, acc0, acc1, K0, V0, K1, V1);
    }
    asm volatile("" :: "v"(tch));
    STAMP(3);
    float* mrg = (float*)(lds + L_MRG);
    float* mml = (float*)(lds + L_ST);
    {
      const float M0 = xmax32(xmax16(m0)), M1 = xmax32(xmax16(m1));
      const float a0 = ex2((m0 - M0) * L2E), a1 = ex2((m1 - M1) * L2E);
      l0 = xsum32(xsum16(l0 * a0)); l1 = xsum32(xsum16(l1 * a1));
#pragma unroll
      for (int k = 0; k < 8; ++k) { acc0[k] = xsum32(xsum16(acc0[k] * a0)); acc1[k] = xsum32(xsum16(acc1[k] * a1)); }
      if (lane < 16) {
        float* r0 = mrg + (wave * 2 + 0) * 128 + lane * 8; float* r1 = mrg + (wave * 2 + 1) * 128 + lane * 8;
        *(f32x4*)r0 = f32x4{acc0[0], acc0[1], acc0[2], acc0[3]}; *(f32x4*)(r0 + 4) = f32x4{acc0[4], acc0[5], acc0[6], acc0[7]};
        *(f32x4*)r1 = f32x4{acc1[0], acc1[1], acc1[2], acc1[3]}; *(f32x4*)(r1 + 4) = f32x4{acc1[4], acc1[5], acc1[6], acc1[7]};
        if (lane == 0) *(f32x4*)(mml + wave * 4) = f32x4{M0, l0, M1, l1};
      }
    }
    BAR();
    if (t < 128) {
      const int g = t >> 6, k = t & 63;
      float M = -INFINITY;
#pragma unroll
      for (int r = 0; r < 8; ++r) M = fmaxf(M, mml[r * 4 + g * 2]);
      float L = 0.f, o0 = 0.f, o1 = 0.f;
#pragma unroll
      for (int r = 0; r < 8; ++r) {
        const float w = ex2((mml[r * 4 + g * 2] - M) * L2E); L = fmaf(w, mml[r * 4 + g * 2 + 1], L);
        const float2 v = *(const float2*)(mrg + (r * 2 + g) * 128 + 2 * k);
        o0 = fmaf(w, v.x, o0); o1 = fmaf(w, v.y, o1);
      }
      gst(wsr, WS_APG + ((ai * 2 + g) * 65 + k) * 16, __float_as_uint(o0), __float_as_uint(o1), tag);
      if (k == 0) gst(wsr, WS_APG + ((ai * 2 + g) * 65 + 64) * 16, __float_as_uint(M), __float_as_uint(L), tag);
    }
    STAMP(4);
    if (isR2) {
      const int hq = r2 >> 1, dh = r2 & 1, thkv = hq >> 1, g = hq & 1;
      const int bb = lane >> 5, k = lane & 31;
      const int a0i = (bb * 16 + thkv) * 6;
      const int ovb = L_MRG + 8192 + 6144 + wave * 256;
      bf16x8 wf[5][2];
      proj_load(lds, 0, wave, lane, wf);
#pragma nounroll
      for (int pass = DRY ? 0 : 1; pass < 2; ++pass) {
        int real; asm volatile("s_mov_b32 %0, %1" : "=s"(real) : "s"(pass));
        if (real) {
          if (wave < 6) {
            const u32x4 r_ = gpoll2p(wsr, WS_APG + (((a0i + wave) * 2 + g) * 65 + 32 * dh + k) * 16, WS_APG + (((a0i + wave) * 2 + g) * 65 + 64) * 16, tag);
            *(u32x4*)(lds + L_MRG + 8192 + (wave * 64 + lane) * 16) = r_;
          }
          BAR();
          STAMP(5);
        }
        if (real || wave >= 6) {
          u32x4 gg[6];
#pragma unroll
          for (int s = 0; s < 6; ++s) gg[s] = *(const u32x4*)(lds + L_MRG + 8192 + (s * 64 + lane) * 16);
          float M = __uint_as_float(gg[0][2]);
#pragma unroll
          for (int s = 1; s < 6; ++s) M = fmaxf(M, __uint_as_float(gg[s][2]));
          float L = 0.f, o0 = 0.f, o1 = 0.f;
#pragma unroll
          for (int s = 0; s < 6; ++s) {
            const float w = ex2((__uint_as_float(gg[s][2]) - M) * L2E);
            L = fmaf(w, __uint_as_float(gg[s][3]), L); o0 = fmaf(w, __uint_as_float(gg[s][0]), o0); o1 = fmaf(w, __uint_as_float(gg[s][1]), o1);
          }
          *(u32*)(lds + ovb + bb * 128 + k * 4) = pack_bf16(o0 / L, o1 / L);
          LGKM0();
          proj_run(lds, wf, wsr, WS_OUTA + r2 * 9216, tag, wave, lane, ovb, real != 0);
        }
      }
      STAMP(6);
      DRAIN();
    } else if (r3 < 36) {
      final_reduce(lds, wsr, WS_OUTA, tag, r3, t, p.out, 0);
      if (r3 == 0) {
        if (wave == 0) { wait_flags(ws, WS_B3FLAG + lane * 8, true, tag); if (lane == 0) flag_set(ws, WS_EPOCH, tag); }
      }
      STAMP(7);
    }
    STAMP_FLUSH();
    return;
  }

  const int j = slot * 8 + xcd;
  const int hh = j >> 1, half = j & 1;
  const int b3b = j >> 5, b3ks = j & 31;
  const int kb = wave & 3, kh = wave >> 2;
  u32x4 wo[9];
  {
    DELAY(D1);
    const unsigned char* src = (const unsigned char*)(p.Bc + ((size_t)b3b * 4096 + 128 * b3ks + 16 * wave) * 512) + lane * 16;
#pragma unroll
    for (int r = 0; r < 16; ++r) glds16(src + r * 1024, ldsb + L_C + (u32)(16 * wave + r) * CSTR);
    if (wave == 7) code_prefetch(pc0, lane, ldsb + L_ST + 1024);
    if (wave == 6) { const u32 tch = touch_pages(p, lane, p.Ak, p.Av, ldsb + L_ST + 1024); asm volatile("" :: "v"(tch)); }
    __builtin_amdgcn_sched_barrier(0);
#pragma unroll
    for (int i = 0; i < 9; ++i) wo[i] = ldnt(p.Bwo + (size_t)j * 64 * 576 + (size_t)(i * 512 + t) * 8);
    __builtin_amdgcn_sched_barrier(0);
    asm volatile("s_waitcnt vmcnt(9)" ::: "memory");
  }
  asm volatile("" :: "v"(epoch));
  BAR();
  STAMP(5);
  const u64 tag = epoch + 1ull;
  const int head = lane & 31, hi = lane >> 5;
  const int g4 = lane >> 4, i16 = lane & 15, qq = i16 >> 2, pp = i16 & 3;
  u32x4 cf[16];
  bf16x8 pv[2][8];
  {
    const int pi = 16 * (head >> 4) + 4 * (head & 3) + 2 * ((head >> 3) & 1) + ((head >> 2) & 1);
    const unsigned char* cr = lds + L_C + (32 * kb + pi) * CSTR + (256 * kh + 8 * hi) * 2;
#pragma unroll
    for (int s = 0; s < 16; ++s) cf[s] = *(const u32x4*)(cr + s * 32);
    const unsigned char* tb = lds + L_C + (4 * qq + 2 * hi) * CSTR + (64 * wave + 16 * (g4 & 1) + 4 * pp) * 2;
#pragma unroll
    for (int ks = 0; ks < 8; ++ks) {
      pv[0][ks] = tr_pair(tb + (16 * ks) * CSTR, tb + (16 * ks + 1) * CSTR);
      pv[1][ks] = tr_pair(tb + (16 * ks) * CSTR + 64, tb + (16 * ks + 1) * CSTR + 64);
    }
  }
  STAMP(4);
  BAR();
  {
    const unsigned char* src = (const unsigned char*)(p.Bwuv + (size_t)hh * 128 + 64 * half);
#pragma unroll
    for (int i = 0; i < 8; ++i) {
      const int L = i * 512 + t, r = L >> 3, pc = L & 7;
      const int c = (((pc >> 1) ^ ((r >> 1) & 3)) << 1) | (pc & 1);
      glds16(src + (size_t)r * 8192 + c * 16, ldsb + L_WUV + (u32)(i * 512 + wave * 64) * 16);
    }
  }
  __builtin_amdgcn_sched_barrier(0);
#pragma nounroll
  for (int pass = DRYB ? 0 : 1; pass < 2; ++pass) {
  int real; asm volatile("s_mov_b32 %0, %1" : "=s"(real) : "s"(pass));
  int lane_o = lane; asm volatile("" : "+v"(lane_o));
  const int lane = lane_o, t = wave * 64 + lane, head = lane & 31, hi = lane >> 5;
  if (real) {
  if (PMASK & 16) wait_flagsp(wsr, WS_B2FLAG + (wave * 8 + (lane & 7)) * 8, tag); else wait_flags(ws, WS_B2FLAG + (wave * 8 + (lane & 7)) * 8, true, tag);
  STAMP(6);
  {
    u32x4 vh[4], vl[4];
#pragma unroll
    for (int i = 0; i < 4; ++i) {
      vh[i] = ld128(wsr, WS_QH + b3b * 32768 + ((wave * 4 + i) * 64 + lane) * 16);
      vl[i] = ld128(wsr, WS_QL + b3b * 32768 + ((wave * 4 + i) * 64 + lane) * 16);
    }
#pragma unroll
    for (int i = 0; i < 4; ++i) {
      *(u32x4*)(lds + L_QH + (wave * 4 + i) * CSTR + lane * 16) = vh[i];
      *(u32x4*)(lds + L_QL + (wave * 4 + i) * CSTR + lane * 16) = vl[i];
    }
  }
  }
  BAR();
  STAMP(7);
  {
    float* smax = (float*)(lds + L_ST);
    float* slsum = smax + 128;
    f32x16 sacc;
#pragma unroll
    for (int r = 0; r < 16; ++r) sacc[r] = 0.f;
    {
      const unsigned char* bhp = lds + L_QH + head * CSTR + (256 * kh + 8 * hi) * 2;
      const unsigned char* blp = lds + L_QL + head * CSTR + (256 * kh + 8 * hi) * 2;
#pragma unroll
      for (int s = 0; s < 16; ++s) {
        const bf16x8 bh = __builtin_bit_cast(bf16x8, *(const u32x4*)(bhp + s * 32));
        const bf16x8 bl = __builtin_bit_cast(bf16x8, *(const u32x4*)(blp + s * 32));
        sacc = __builtin_amdgcn_mfma_f32_32x32x16_bf16(__builtin_bit_cast(bf16x8, cf[s]), bh, sacc, 0, 0, 0);
        sacc = __builtin_amdgcn_mfma_f32_32x32x16_bf16(__builtin_bit_cast(bf16x8, cf[s]), bl, sacc, 0, 0, 0);
      }
    }
  STAMP(8);
    if (kh == 1) {
#pragma unroll
      for (int r4 = 0; r4 < 4; ++r4)
        *(f32x4*)(lds + L_XB + ((kb * 4 + r4) * 64 + lane) * 16) = f32x4{sacc[4 * r4], sacc[4 * r4 + 1], sacc[4 * r4 + 2], sacc[4 * r4 + 3]};
    }
    BAR();
    float sv[16];
    if (kh == 0) {
      float mx = -INFINITY;
#pragma unroll
      for (int r4 = 0; r4 < 4; ++r4) {
        const f32x4 o = *(const f32x4*)(lds + L_XB + ((kb * 4 + r4) * 64 + lane) * 16);
#pragma unroll
        for (int e = 0; e < 4; ++e) { sv[4 * r4 + e] = bfr(bfr(sacc[4 * r4 + e] + o[e]) * SCALE); mx = fmaxf(mx, sv[4 * r4 + e]); }
      }
      mx = xmax32(mx);
      if (lane < 32) smax[kb * 32 + head] = mx;
    }
    BAR();
    if (kh == 0) {
      const float m = fmaxf(fmaxf(smax[head], smax[32 + head]), fmaxf(smax[64 + head], smax[96 + head]));
      float ls = 0.f;
#pragma unroll
      for (int r4 = 0; r4 < 4; ++r4) {
        float pe[4];
#pragma unroll
        for (int e = 0; e < 4; ++e) { pe[e] = ex2((sv[4 * r4 + e] - m) * L2E); ls += pe[e]; }
        *(u32x2*)(lds + L_P + head * 272 + (32 * kb + 8 * r4 + 4 * hi) * 2) = u32x2{pack_bf16(pe[0], pe[1]), pack_bf16(pe[2], pe[3])};
      }
      ls = xsum32(ls);
      if (lane < 32) slsum[kb * 32 + head] = ls;
    }
    BAR();
  STAMP(9);
    f32x16 oacc0, oacc1;
#pragma unroll
    for (int r = 0; r < 16; ++r) { oacc0[r] = 0.f; oacc1[r] = 0.f; }
    {
      const unsigned char* pa = lds + L_P + head * 272 + hi * 16;
#pragma unroll
      for (int ks = 0; ks < 8; ++ks) {
        const bf16x8 a = __builtin_bit_cast(bf16x8, *(const u32x4*)(pa + ks * 32));
        oacc0 = __builtin_amdgcn_mfma_f32_32x32x16_bf16(pv[0][ks], a, oacc0, 0, 0, 0);
        oacc1 = __builtin_amdgcn_mfma_f32_32x32x16_bf16(pv[1][ks], a, oacc1, 0, 0, 0);
      }
    }
  STAMP(10);
    {
      unsigned char* ot = lds + L_OT + wave * 4608;
#pragma unroll
      for (int r4 = 0; r4 < 4; ++r4) {
        *(u32x2*)(ot + head * 144 + (8 * r4 + 4 * hi) * 2) = u32x2{pack_bf16(oacc0[4 * r4], oacc0[4 * r4 + 1]), pack_bf16(oacc0[4 * r4 + 2], oacc0[4 * r4 + 3])};
        *(u32x2*)(ot + head * 144 + (32 + 8 * r4 + 4 * hi) * 2) = u32x2{pack_bf16(oacc1[4 * r4], oacc1[4 * r4 + 1]), pack_bf16(oacc1[4 * r4 + 2], oacc1[4 * r4 + 3])};
      }
      LGKM0();
#pragma unroll
      for (int i = 0; i < 4; ++i) {
        const int hrow = (lane >> 3) + 8 * i, ch = lane & 7;
        const u32x4 v = *(const u32x4*)(ot + hrow * 144 + ch * 16);
        if (real) st128(wsr, WS_BPART + (((b3b * 32 + b3ks) * 32 + hrow) * 512 + 64 * wave + 8 * ch) * 2, v);
        else asm volatile("" :: "v"(v));
      }
    }
    if (t < 32) {
      const float L = (slsum[t] + slsum[32 + t]) + (slsum[64 + t] + slsum[96 + t]);
      const float m = fmaxf(fmaxf(smax[t], smax[32 + t]), fmaxf(smax[64 + t], smax[96 + t]));
      if (real) st64(wsr, WS_BML + ((b3b * 32 + b3ks) * 32 + t) * 8, u32x2{__float_as_uint(m), __float_as_uint(L)});
    }
    if (real) DRAIN();
    BAR();
    if (t == 0 && real) flag_set(ws, WS_B3FLAG + j * 8, tag);
  STAMP(11);
  }
  }
  {
#pragma unroll
    for (int i = 0; i < 9; ++i) {
      const int L = i * 512 + t, r = L / 72, c = L - r * 72;
      const int pc = (((c >> 1) ^ ((r >> 1) & 3)) << 1) | (c & 1);
      *(u32x4*)(lds + L_WO + (r * 72 + pc) * 16) = wo[i];
    }
  }
  bf16x8 uf[4][2], wf[5][2];
  {
    const int r0 = 4 * g4 + qq, sw = (r0 >> 1) & 3;
#pragma unroll
    for (int nb = 0; nb < 4; ++nb) {
      const unsigned char* base = lds + L_WUV + (64 * wave + r0) * 128 + ((nb ^ sw) * 32) + pp * 8;
#pragma unroll
      for (int ks = 0; ks < 2; ++ks) uf[nb][ks] = tr_pair(base + (32 * ks) * 128, base + (32 * ks + 16) * 128);
    }
  }
  BAR();
  proj_load(lds, L_WO, wave, lane, wf);
#pragma nounroll
  for (int pass = DRYC ? 0 : 1; pass < 2; ++pass) {
  int real; asm volatile("s_mov_b32 %0, %1" : "=s"(real) : "s"(pass));
  int lane_o = lane; asm volatile("" : "+v"(lane_o));
  const int lane = lane_o, t = wave * 64 + lane, g4 = lane >> 4, i16 = lane & 15, qq = i16 >> 2, pp = i16 & 3;
  {
    if (wave == 0 && real) { if (PMASK & 32) wait_flagsp(wsr, WS_B3FLAG + lane * 8, tag); else wait_flags(ws, WS_B3FLAG + lane * 8, true, tag); }
    BAR();
  STAMP(12);
    const int rg = lane >> 3, ch = lane & 7;
    u32x4 g[8]; u32x2 ml = {0u, 0u};
#pragma unroll
    for (int i = 0; i < 8; ++i) g[i] = u32x4{0u, 0u, 0u, 0u};
    if (real) {
#pragma unroll
    for (int i = 0; i < 8; ++i) g[i] = ld128(wsr, WS_BPART + (((rg + 8 * i) * 32 + hh) * 512 + 64 * wave + 8 * ch) * 2);
    ml = ld64(wsr, WS_BML + (lane * 32 + hh) * 8);
    }
    float wgt;
    {
      const float m = __uint_as_float(ml[0]), l = __uint_as_float(ml[1]);
      float M = m;
      M = fmaxf(M, dppf<0x128>(M)); M = fmaxf(M, dppf<0x124>(M)); M = fmaxf(M, dppf<0x122>(M)); M = fmaxf(M, dppf<0x121>(M)); M = xmax16(M);
      const float w = ex2((m - M) * L2E);
      const float L = xsum16(row16_sum(w * l));
      wgt = w / L;
    }
    float s0[8], s1[8];
#pragma unroll
    for (int k = 0; k < 8; ++k) { s0[k] = 0.f; s1[k] = 0.f; }
#pragma unroll
    for (int i = 0; i < 4; ++i) {
      const float w0 = __shfl(wgt, rg + 8 * i), w1 = __shfl(wgt, 32 + rg + 8 * i);
#pragma unroll
      for (int k = 0; k < 4; ++k) {
        s0[2 * k] = fmaf(w0, bflo(g[i][k]), s0[2 * k]); s0[2 * k + 1] = fmaf(w0, bfhi(g[i][k]), s0[2 * k + 1]);
        s1[2 * k] = fmaf(w1, bflo(g[i + 4][k]), s1[2 * k]); s1[2 * k + 1] = fmaf(w1, bfhi(g[i + 4][k]), s1[2 * k + 1]);
      }
    }
#pragma unroll
    for (int k = 0; k < 8; ++k) {
      float x = s0[k]; x += dppf<0x128>(x); x = xsum16(x); x = xsum32(x); s0[k] = x;
      float y = s1[k]; y += dppf<0x128>(y); y = xsum16(y); y = xsum32(y); s1[k] = y;
    }
  STAMP(13);
    unsigned char* ca = lds + L_CA + wave * 512;
    if (lane < 8) {
      u32x4 h0, l0, h1, l1;
#pragma unroll
      for (int k = 0; k < 4; ++k) {
        const float a = bfr(s0[2 * k]), bq = bfr(s0[2 * k + 1]), c = bfr(s1[2 * k]), d = bfr(s1[2 * k + 1]);
        h0[k] = pack_bf16(a, bq); l0[k] = pack_bf16(s0[2 * k] - a, s0[2 * k + 1] - bq);
        h1[k] = pack_bf16(c, d); l1[k] = pack_bf16(s1[2 * k] - c, s1[2 * k + 1] - d);
      }
      *(u32x4*)(ca + 0 * 128 + lane * 16) = h0; *(u32x4*)(ca + 1 * 128 + lane * 16) = l0;
      *(u32x4*)(ca + 2 * 128 + lane * 16) = h1; *(u32x4*)(ca + 3 * 128 + lane * 16) = l1;
    }
    LGKM0();
    bf16x8 af[2];
#pragma unroll
    for (int ks = 0; ks < 2; ++ks) {
      u32x2 lo = {0u, 0u}, hq = {0u, 0u};
      if (i16 < 4) {
        lo = *(const u32x2*)(ca + i16 * 128 + (32 * ks + 4 * g4) * 2);
        hq = *(const u32x2*)(ca + i16 * 128 + (32 * ks + 16 + 4 * g4) * 2);
      }
      af[ks] = __builtin_bit_cast(bf16x8, u32x4{lo[0], lo[1], hq[0], hq[1]});
    }
    float* red2 = (float*)(lds + L_RED2);
    {
#pragma unroll
      for (int nb = 0; nb < 4; ++nb) {
        f32x4 d = {0.f, 0.f, 0.f, 0.f};
#pragma unroll
        for (int ks = 0; ks < 2; ++ks) d = __builtin_amdgcn_mfma_f32_16x16x32_bf16(af[ks], uf[nb][ks], d, 0, 0, 0);
        if (lane < 16) { red2[(wave * 2 + 0) * 64 + 16 * nb + lane] = d[0] + d[1]; red2[(wave * 2 + 1) * 64 + 16 * nb + lane] = d[2] + d[3]; }
      }
    }
    BAR();
    {
      float2 s = *(const float2*)(red2 + lane * 2);
#pragma unroll
      for (int w = 1; w < 8; ++w) { const float2 v = *(const float2*)(red2 + w * 128 + lane * 2); s.x += v.x; s.y += v.y; }
      *(u32*)(lds + L_CA + wave * 512 + lane * 4) = pack_bf16(s.x, s.y);
      LGKM0();
    }
  }
  STAMP(14);
  proj_run(lds, wf, wsr, WS_OUTB + j * 9216, tag, wave, lane, L_CA + wave * 512, real != 0);
  if (j < 36) final_reduce(lds, wsr, WS_OUTB, tag, j, t, p.out, 576, real != 0);
  if (!real) BAR();
  }
  STAMP(15);
  STAMP_FLUSH();
}

extern "C" void kernel_launch(void* const* d_in, const int* in_sizes, int n_in, void* d_out, int out_size, void* d_ws, size_t ws_size,
                              hipStream_t stream) {
  Params p;
  p.Ax = (const u16*)d_in[0]; p.Ak = (const u16*)d_in[1]; p.Av = (const u16*)d_in[2]; p.Awq = (const u16*)d_in[3]; p.Awo = (const u16*)d_in[4];
  p.Bx = (const u16*)d_in[5]; p.Bc = (const u16*)d_in[6]; p.Bwq = (const u16*)d_in[7]; p.Bwuk = (const u16*)d_in[8]; p.Bwuv = (const u16*)d_in[9];
  p.Bwo = (const u16*)d_in[10];
  p.out = (u16*)d_out; p.ws = (unsigned char*)d_ws;
  fused_kernel<<<dim3(NWG), dim3(NT), 0, stream>>>(p);
}
```
